```python
import torch

def _softmax(x, axis=-1):
    return torch.softmax(x, dim=axis)
def _gelu(x):
    return torch.nn.functional.gelu(x, approximate="tanh")
def _silu(x):
    return torch.nn.functional.silu(x)
def _sigmoid(x):
    return torch.sigmoid(x)
def _layernorm(x, axis=-1, eps=1e-5):
    m = x.mean(dim=axis, keepdim=True)
    v = x.var(dim=axis, keepdim=True, unbiased=False)
    return (x - m) / torch.sqrt(v + eps)
def _scatter_add(tgt, idx, src, axis=0):
    return tgt.clone().index_add_(axis, idx, src)
def _segment_sum(src, seg, n):
    out = torch.zeros((n,) + src.shape[1:], dtype=src.dtype)
    return out.index_add_(0, seg, src)
def _pad(x, pw):
    flat = tuple(v for pair in reversed(pw) for v in pair)
    return torch.nn.functional.pad(x, flat)

kernel_name = 'standalone__mha_block_cached__near_ridge'

def setup_inputs():

    _w = (lambda a: (lambda t: t.to(torch.bfloat16) if t.is_floating_point() else t)(torch.as_tensor(a)))
    B, Sq, D = 8, 160, 1024
    H, Sk, Dh = 32, 512, 128
    return {
        "x": _w((lambda *s: torch.randn(*s, dtype=torch.bfloat16) * 0.8)(B, Sq, D)),
        "k_cache": _w((lambda *s: torch.randn(*s, dtype=torch.bfloat16) * 0.8)(B, H, Sk, Dh)),
        "v_cache": _w((lambda *s: torch.randn(*s, dtype=torch.bfloat16) * 0.05)(B, H, Sk, Dh)),
        "wq": _w((lambda *s: torch.randn(*s, dtype=torch.bfloat16) * 0.8)(D, H * Dh) * (D ** -0.5)),
        "wo": _w((lambda *s: torch.randn(*s, dtype=torch.bfloat16) * 0.05)(H * Dh, D) * ((H * Dh) ** -0.5)),
    }

def reference(x, k_cache, v_cache, wq, wo):
    B, Sq, D = x.shape
    H, Dh = 32, 128
    q = (lambda x, *d: x.permute(*d))((x @ wq).reshape(B, Sq, H, Dh), 0, 2, 1, 3)
    scores = torch.einsum("bhqd,bhkd->bhqk", q, k_cache) * (Dh ** -0.5)
    p = _softmax(scores, axis=-1)
    o = torch.einsum("bhqk,bhkd->bhqd", p, v_cache)
    return (lambda x, *d: x.permute(*d))(o, 0, 2, 1, 3).reshape(B, Sq, H * Dh) @ wo
```

```cpp
#include <hip/hip_runtime.h>
#include <hip/hip_bf16.h>
typedef __attribute__((ext_vector_type(8))) short s16x8;
typedef __attribute__((ext_vector_type(8))) __bf16 bf16x8;
typedef __attribute__((ext_vector_type(4))) float f32x4;
typedef unsigned short u16;
static __device__ __forceinline__ float bf2f(u16 v){ return __uint_as_float(((unsigned)v)<<16);}
static __device__ __forceinline__ u16 f2bf(float f){ __bf16 b=(__bf16)f; return __builtin_bit_cast(u16,b);}

__global__ __launch_bounds__(256) void gemm_simple(const u16* A,const u16* B,u16* C,int M,int N,int K){
  __shared__ u16 As[64][40]; __shared__ u16 Bs[64][40];
  int tid=threadIdx.x, w=tid>>6, l=tid&63;
  int bm=blockIdx.y*64, bn=blockIdx.x*64;
  f32x4 acc[4]={};
  for(int k0=0;k0<K;k0+=32){
    for(int i=tid;i<64*32;i+=256){int r=i>>5,c=i&31; As[r][c]=A[(long)(bm+r)*K+k0+c];}
    for(int i=tid;i<64*32;i+=256){int k=i>>6,n=i&63; Bs[n][k]=B[(long)(k0+k)*N+bn+n];}
    __syncthreads();
    bf16x8 a=*(const bf16x8*)&As[w*16+(l&15)][(l>>4)*8];
    for(int j=0;j<4;j++){ bf16x8 b=*(const bf16x8*)&Bs[j*16+(l&15)][(l>>4)*8];
      acc[j]=__builtin_amdgcn_mfma_f32_16x16x32_bf16(a,b,acc[j],0,0,0);}
    __syncthreads();
  }
  for(int j=0;j<4;j++)for(int r=0;r<4;r++) C[(long)(bm+w*16+(l>>4)*4+r)*N+bn+j*16+(l&15)]=f2bf(acc[j][r]);
}
__global__ __launch_bounds__(256) void attn_simple(const u16* Q,const u16* Kc,const u16* Vc,u16* O){
  __shared__ float qs[8][128]; __shared__ float s[8][512]; __shared__ float red[8];
  int bh=blockIdx.y, b=bh>>5, h=bh&31, q0=blockIdx.x*8, tid=threadIdx.x;
  for(int i=tid;i<8*128;i+=256){int r=i>>7,d=i&127; qs[r][d]=bf2f(Q[(long)(b*160+q0+r)*4096+h*128+d]);}
  __syncthreads();
  const u16* K=Kc+(long)bh*512*128; const u16* V=Vc+(long)bh*512*128;
  for(int k=tid;k<512;k+=256){ float a[8]={};
    for(int d=0;d<128;d+=8){ s16x8 kv=*(const s16x8*)&K[k*128+d];
      for(int e=0;e<8;e++){float kf=bf2f((u16)kv[e]); for(int r=0;r<8;r++)a[r]+=qs[r][d+e]*kf;}}
    for(int r=0;r<8;r++) s[r][k]=a[r]*0.08838834764831845f; }
  __syncthreads();
  { int w=tid>>6,l=tid&63; for(int r=w*2;r<w*2+2;r++){ float m=-1e30f; for(int k=l;k<512;k+=64)m=fmaxf(m,s[r][k]);
      for(int o=32;o;o>>=1)m=fmaxf(m,__shfl_xor(m,o)); float su=0; for(int k=l;k<512;k+=64){float e=__expf(s[r][k]-m); s[r][k]=e; su+=e;}
      for(int o=32;o;o>>=1)su+=__shfl_xor(su,o); if(l==0)red[r]=1.f/su; } }
  __syncthreads();
  { int d=tid&127, rh=tid>>7; float a[4]={};
    for(int k=0;k<512;k++){ float v=bf2f(V[k*128+d]); for(int r=0;r<4;r++)a[r]+=s[rh*4+r][k]*v;}
    for(int r=0;r<4;r++) O[(long)(b*160+q0+rh*4+r)*4096+h*128+d]=f2bf(a[r]*red[rh*4+r]); }
}
extern "C" void kernel_launch(void* const* d_in, const int* in_sizes, int n_in, void* d_out, int out_size, void* d_ws, size_t ws_size, hipStream_t stream){
  const u16* x=(const u16*)d_in[0]; const u16* k=(const u16*)d_in[1]; const u16* v=(const u16*)d_in[2]; const u16* wq=(const u16*)d_in[3]; const u16* wo=(const u16*)d_in[4];
  u16* q=(u16*)d_ws; u16* o=q+1280*4096;
  gemm_simple<<<dim3(64,20),256,0,stream>>>(x,wq,q,1280,4096,1024);
  attn_simple<<<dim3(20,256),256,0,stream>>>(q,k,v,o);
  gemm_simple<<<dim3(16,20),256,0,stream>>>(o,wo,(u16*)d_out,1280,1024,4096);
}
```

```cpp
#include <hip/hip_runtime.h>
#include <hip/hip_bf16.h>
typedef __attribute__((ext_vector_type(8))) short s16x8;
typedef __attribute__((ext_vector_type(8))) __bf16 bf16x8;
typedef __attribute__((ext_vector_type(4))) float f32x4;
typedef __attribute__((ext_vector_type(16))) float f32x16;
typedef __attribute__((ext_vector_type(4))) unsigned u32x4;
typedef unsigned short u16;
typedef unsigned long long u64;
static __device__ __forceinline__ float bf2f(u16 v){ return __uint_as_float(((unsigned)v)<<16);}
static __device__ __forceinline__ u16 f2bf(float f){ __bf16 b=(__bf16)f; return __builtin_bit_cast(u16,b);}
__global__ __launch_bounds__(256) void gemm_simple(const u16* A,const u16* B,u16* C,int M,int N,int K){
  __shared__ u16 As[64][40]; __shared__ u16 Bs[64][40];
  int tid=threadIdx.x, w=tid>>6, l=tid&63;
  int bm=blockIdx.y*64, bn=blockIdx.x*64;
  f32x4 acc[4]={};
  for(int k0=0;k0<K;k0+=32){
    { int r=tid>>2, c=(tid&3)*8; *(s16x8*)&As[r][c]=*(const s16x8*)&A[(long)(bm+r)*K+k0+c]; }
    { int k=tid>>3, n=(tid&7)*8; s16x8 v=*(const s16x8*)&B[(long)(k0+k)*N+bn+n]; for(int e=0;e<8;e++) Bs[n+e][k]=v[e]; }
    __syncthreads();
    bf16x8 a=*(const bf16x8*)&As[w*16+(l&15)][(l>>4)*8];
    for(int j=0;j<4;j++){ bf16x8 b=*(const bf16x8*)&Bs[j*16+(l&15)][(l>>4)*8];
      acc[j]=__builtin_amdgcn_mfma_f32_16x16x32_bf16(a,b,acc[j],0,0,0);}
    __syncthreads();
  }
  for(int j=0;j<4;j++)for(int r=0;r<4;r++) C[(long)(bm+w*16+(l>>4)*4+r)*N+bn+j*16+(l&15)]=f2bf(acc[j][r]);
}
__global__ __launch_bounds__(256) void attn_simple(const u16* Q,const u16* Kc,const u16* Vc,u16* O){
  __shared__ float qs[8][128]; __shared__ float s[8][512]; __shared__ float red[8];
  int bh=blockIdx.y, b=bh>>5, h=bh&31, q0=blockIdx.x*8, tid=threadIdx.x;
  for(int i=tid;i<8*128;i+=256){int r=i>>7,d=i&127; qs[r][d]=bf2f(Q[(long)(b*160+q0+r)*4096+h*128+d]);}
  __syncthreads();
  const u16* K=Kc+(long)bh*512*128; const u16* V=Vc+(long)bh*512*128;
  for(int k=tid;k<512;k+=256){ float a[8]={};
    for(int d=0;d<128;d+=8){ s16x8 kv=*(const s16x8*)&K[k*128+d];
      for(int e=0;e<8;e++){float kf=bf2f((u16)kv[e]); for(int r=0;r<8;r++)a[r]+=qs[r][d+e]*kf;}}
    for(int r=0;r<8;r++) s[r][k]=a[r]*0.08838834764831845f; }
  __syncthreads();
  { int w=tid>>6,l=tid&63; for(int r=w*2;r<w*2+2;r++){ float m=-1e30f; for(int k=l;k<512;k+=64)m=fmaxf(m,s[r][k]);
      for(int o=32;o;o>>=1)m=fmaxf(m,__shfl_xor(m,o)); float su=0; for(int k=l;k<512;k+=64){float e=__expf(s[r][k]-m); s[r][k]=e; su+=e;}
      for(int o=32;o;o>>=1)su+=__shfl_xor(su,o); if(l==0)red[r]=1.f/su; } }
  __syncthreads();
  { int d=tid&127, rh=tid>>7; float a[4]={};
    for(int k=0;k<512;k++){ float v=bf2f(V[k*128+d]); for(int r=0;r<4;r++)a[r]+=s[rh*4+r][k]*v;}
    for(int r=0;r<4;r++) O[(long)(b*160+q0+rh*4+r)*4096+h*128+d]=f2bf(a[r]*red[rh*4+r]); }
}
static __device__ __forceinline__ u64 rt(){ u64 t; asm volatile("s_memrealtime %0\n\ts_waitcnt lgkmcnt(0)":"=s"(t)::"memory"); return t;}
static __device__ __forceinline__ u64 ct(){ u64 t; asm volatile("s_memtime %0\n\ts_waitcnt lgkmcnt(0)":"=s"(t)::"memory"); return t;}
__global__ void amp(const u64* st,int nwg,int mode,int AMP){
  __shared__ u64 mn[256],mx[256],mx0[256],ax[256],sm[256];
  int t=threadIdx.x; u64 a=~0ull,b=0,c=0,d=0,e=0;
  for(int i=t;i<nwg;i+=256){ u64 t0=st[i*4],t1=st[i*4+1],au=st[i*4+2]; a=min(a,t0); b=max(b,t1); c=max(c,t0); d=max(d,au); e+=au;}
  mn[t]=a;mx[t]=b;mx0[t]=c;ax[t]=d;sm[t]=e; __syncthreads();
  if(t==0){ for(int i=1;i<256;i++){a=min(a,mn[i]);b=max(b,mx[i]);c=max(c,mx0[i]);d=max(d,ax[i]);e+=sm[i];}
    u64 dur= mode==0? b-a : mode==1? c-a : mode==2? st[7*4+2] : mode==3? d : e/nwg;
    if(dur>20000) dur=20000;
    u64 s=rt(); u64 tgt=dur*AMP; while(rt()-s<tgt) __builtin_amdgcn_s_sleep(4);
  }
}
static void run_rest(void* const* d_in, void* d_out, void* d_ws, hipStream_t stream){
  const u16* x=(const u16*)d_in[0]; const u16* k=(const u16*)d_in[1]; const u16* v=(const u16*)d_in[2]; const u16* wq=(const u16*)d_in[3]; const u16* wo=(const u16*)d_in[4];
  u16* q=(u16*)d_ws+(1<<20); u16* o=q+1280*4096;
  gemm_simple<<<dim3(64,20),256,0,stream>>>(x,wq,q,1280,4096,1024);
  attn_simple<<<dim3(20,256),256,0,stream>>>(q,k,v,o);
  gemm_simple<<<dim3(16,20),256,0,stream>>>(o,wo,(u16*)d_out,1280,1024,4096);
}

__global__ __launch_bounds__(256) void rd(const u32x4* k,const u32x4* v,u64* st){
  u64 t0=rt();
  const u32x4* kp=k+(long)blockIdx.x*8192+threadIdx.x; const u32x4* vp=v+(long)blockIdx.x*8192+threadIdx.x;
  u32x4 a={0,0,0,0}; u32x4 r[32],r2[32];
  #pragma unroll
  for(int i=0;i<32;i++) r[i]=kp[i*256];
  #pragma unroll
  for(int i=0;i<32;i++) r2[i]=vp[i*256];
  #pragma unroll
  for(int i=0;i<32;i++) a^=r[i];
  #pragma unroll
  for(int i=0;i<32;i++) a^=r2[i];
  unsigned s=a[0]^a[1]^a[2]^a[3];
  asm volatile(""::"v"(s));
  __syncthreads();
  u64 t1=rt();
  if(threadIdx.x==0){st[blockIdx.x*4]=t0; st[blockIdx.x*4+1]=t1; st[blockIdx.x*4+2]=t1-t0;}
}

extern "C" void kernel_launch(void* const* d_in, const int* in_sizes, int n_in, void* d_out, int out_size, void* d_ws, size_t ws_size, hipStream_t stream){
  u64* st=(u64*)d_ws;
rd<<<256,256,0,stream>>>((const u32x4*)d_in[1],(const u32x4*)d_in[2],st); amp<<<1,256,0,stream>>>(st,256,0,0);
  run_rest(d_in,d_out,d_ws,stream);
}
```

```cpp
#include <hip/hip_runtime.h>
#include <hip/hip_bf16.h>
typedef __attribute__((ext_vector_type(8))) short s16x8;
typedef __attribute__((ext_vector_type(8))) __bf16 bf16x8;
typedef __attribute__((ext_vector_type(4))) float f32x4;
typedef __attribute__((ext_vector_type(16))) float f32x16;
typedef __attribute__((ext_vector_type(4))) unsigned u32x4;
typedef unsigned short u16;
typedef unsigned long long u64;
static __device__ __forceinline__ float bf2f(u16 v){ return __uint_as_float(((unsigned)v)<<16);}
static __device__ __forceinline__ u16 f2bf(float f){ __bf16 b=(__bf16)f; return __builtin_bit_cast(u16,b);}
__global__ __launch_bounds__(256) void gemm_simple(const u16* A,const u16* B,u16* C,int M,int N,int K){
  __shared__ u16 As[64][40]; __shared__ u16 Bs[64][40];
  int tid=threadIdx.x, w=tid>>6, l=tid&63;
  int bm=blockIdx.y*64, bn=blockIdx.x*64;
  f32x4 acc[4]={};
  for(int k0=0;k0<K;k0+=32){
    { int r=tid>>2, c=(tid&3)*8; *(s16x8*)&As[r][c]=*(const s16x8*)&A[(long)(bm+r)*K+k0+c]; }
    { int k=tid>>3, n=(tid&7)*8; s16x8 v=*(const s16x8*)&B[(long)(k0+k)*N+bn+n]; for(int e=0;e<8;e++) Bs[n+e][k]=v[e]; }
    __syncthreads();
    bf16x8 a=*(const bf16x8*)&As[w*16+(l&15)][(l>>4)*8];
    for(int j=0;j<4;j++){ bf16x8 b=*(const bf16x8*)&Bs[j*16+(l&15)][(l>>4)*8];
      acc[j]=__builtin_amdgcn_mfma_f32_16x16x32_bf16(a,b,acc[j],0,0,0);}
    __syncthreads();
  }
  for(int j=0;j<4;j++)for(int r=0;r<4;r++) C[(long)(bm+w*16+(l>>4)*4+r)*N+bn+j*16+(l&15)]=f2bf(acc[j][r]);
}
__global__ __launch_bounds__(256) void attn_simple(const u16* Q,const u16* Kc,const u16* Vc,u16* O){
  __shared__ float qs[8][128]; __shared__ float s[8][512]; __shared__ float red[8];
  int bh=blockIdx.y, b=bh>>5, h=bh&31, q0=blockIdx.x*8, tid=threadIdx.x;
  for(int i=tid;i<8*128;i+=256){int r=i>>7,d=i&127; qs[r][d]=bf2f(Q[(long)(b*160+q0+r)*4096+h*128+d]);}
  __syncthreads();
  const u16* K=Kc+(long)bh*512*128; const u16* V=Vc+(long)bh*512*128;
  for(int k=tid;k<512;k+=256){ float a[8]={};
    for(int d=0;d<128;d+=8){ s16x8 kv=*(const s16x8*)&K[k*128+d];
      for(int e=0;e<8;e++){float kf=bf2f((u16)kv[e]); for(int r=0;r<8;r++)a[r]+=qs[r][d+e]*kf;}}
    for(int r=0;r<8;r++) s[r][k]=a[r]*0.08838834764831845f; }
  __syncthreads();
  { int w=tid>>6,l=tid&63; for(int r=w*2;r<w*2+2;r++){ float m=-1e30f; for(int k=l;k<512;k+=64)m=fmaxf(m,s[r][k]);
      for(int o=32;o;o>>=1)m=fmaxf(m,__shfl_xor(m,o)); float su=0; for(int k=l;k<512;k+=64){float e=__expf(s[r][k]-m); s[r][k]=e; su+=e;}
      for(int o=32;o;o>>=1)su+=__shfl_xor(su,o); if(l==0)red[r]=1.f/su; } }
  __syncthreads();
  { int d=tid&127, rh=tid>>7; float a[4]={};
    for(int k=0;k<512;k++){ float v=bf2f(V[k*128+d]); for(int r=0;r<4;r++)a[r]+=s[rh*4+r][k]*v;}
    for(int r=0;r<4;r++) O[(long)(b*160+q0+rh*4+r)*4096+h*128+d]=f2bf(a[r]*red[rh*4+r]); }
}
static __device__ __forceinline__ u64 rt(){ u64 t; asm volatile("s_memrealtime %0\n\ts_waitcnt lgkmcnt(0)":"=s"(t)::"memory"); return t;}
static __device__ __forceinline__ u64 ct(){ u64 t; asm volatile("s_memtime %0\n\ts_waitcnt lgkmcnt(0)":"=s"(t)::"memory"); return t;}
__global__ void amp(const u64* st,int nwg,int mode,int AMP){
  __shared__ u64 mn[256],mx[256],mx0[256],ax[256],sm[256];
  int t=threadIdx.x; u64 a=~0ull,b=0,c=0,d=0,e=0;
  for(int i=t;i<nwg;i+=256){ u64 t0=st[i*4],t1=st[i*4+1],au=st[i*4+2]; a=min(a,t0); b=max(b,t1); c=max(c,t0); d=max(d,au); e+=au;}
  mn[t]=a;mx[t]=b;mx0[t]=c;ax[t]=d;sm[t]=e; __syncthreads();
  if(t==0){ for(int i=1;i<256;i++){a=min(a,mn[i]);b=max(b,mx[i]);c=max(c,mx0[i]);d=max(d,ax[i]);e+=sm[i];}
    u64 dur= mode==0? b-a : mode==1? c-a : mode==2? st[7*4+2] : mode==3? d : e/nwg;
    if(dur>20000) dur=20000;
    u64 s=rt(); u64 tgt=dur*AMP; while(rt()-s<tgt) __builtin_amdgcn_s_sleep(4);
  }
}
static void run_rest(void* const* d_in, void* d_out, void* d_ws, hipStream_t stream){
  const u16* x=(const u16*)d_in[0]; const u16* k=(const u16*)d_in[1]; const u16* v=(const u16*)d_in[2]; const u16* wq=(const u16*)d_in[3]; const u16* wo=(const u16*)d_in[4];
  u16* q=(u16*)d_ws+(1<<20); u16* o=q+1280*4096;
  gemm_simple<<<dim3(64,20),256,0,stream>>>(x,wq,q,1280,4096,1024);
  attn_simple<<<dim3(20,256),256,0,stream>>>(q,k,v,o);
  gemm_simple<<<dim3(16,20),256,0,stream>>>(o,wo,(u16*)d_out,1280,1024,4096);
}

__global__ void probe(const unsigned* p,u64* st){
  unsigned o=0;
  unsigned v=__builtin_nontemporal_load(p+o/4); asm volatile("s_waitcnt vmcnt(0)"::"v"(v):"memory");
  u64 t0=rt();
  unsigned acc=v;
  for(int i=1;i<=100;i++){ unsigned a=(o+i*8448)/4+((acc&0x7fff7fff)==0x7fff7fff?1:0); unsigned r; asm volatile("global_load_dword %0, %1, off\n\ts_waitcnt vmcnt(0)":"=v"(r):"v"(p+a):"memory"); acc=r; }
  u64 t1=rt();
  asm volatile(""::"v"(acc));
  if(threadIdx.x==0){st[0]=t0; st[1]=t1; st[2]=t1-t0;}
}

extern "C" void kernel_launch(void* const* d_in, const int* in_sizes, int n_in, void* d_out, int out_size, void* d_ws, size_t ws_size, hipStream_t stream){
  u64* st=(u64*)d_ws;
  probe<<<1,1,0,stream>>>((const unsigned*)d_in[0],st);
  amp<<<1,256,0,stream>>>(st,1,0,10);
  run_rest(d_in,d_out,d_ws,stream);
}
```

```cpp
#include <hip/hip_runtime.h>
__global__ __launch_bounds__(512,2) void mha_fused(const void* x,const void* k,const void* v,const void* wq,const void* wo,void* out,void* ws){
  __shared__ __attribute__((aligned(16))) char smem[163840];
  asm volatile(
".Lk_start_%=:\n"
"s_load_dwordx8 s[4:11], %0, 0x0\n"
"s_load_dwordx4 s[12:15], %0, 0x20\n"
"s_load_dwordx2 s[16:17], %0, 0x30\n"
"s_mov_b32 s19, %1\n"
"s_xor_b32 s59, s19, 0\n"
"v_lshrrev_b32 v1, 6, %2\n"
"v_and_b32 v0, 63, %2\n"
"s_and_b32 s61, s59, 7\n"
"s_lshr_b32 s60, s59, 3\n"
"v_readfirstlane_b32 s54, v1\n"
"s_mov_b32 s18, 0\n"
"s_lshr_b32 s57, s61, 1\n"
"s_and_b32 s62, s61, 1\n"
"s_lshl_b32 s62, s62, 2\n"
"s_and_b32 s63, s60, 3\n"
"s_add_u32 s55, s62, s63\n"
"s_lshr_b32 s58, s60, 2\n"
"s_lshl_b32 s56, s57, 3\n"
"s_add_u32 s56, s56, s58\n"
"s_cmp_ge_u32 s54, 4\n"
"s_cbranch_scc1 .Lk_A0_1_%=\n"
"v_lshrrev_b32 v40, 3, v0\n"
"v_and_b32 v41, 7, v0\n"
"v_lshrrev_b32 v42, 4, v0\n"
"v_xor_b32 v41, v41, v42\n"
"v_lshlrev_b32 v41, 4, v41\n"
"v_lshl_add_u32 v60, v40, 11, v41\n"
"v_xor_b32 v61, 64, v60\n"
"v_add_u32 v62, 30720, v60\n"
"v_add_u32 v63, 46080, v61\n"
"v_add_u32 v61, 15360, v61\n"
"s_and_b32 s62, s54, 1\n"
"s_lshl_b32 s62, s62, 6\n"
"v_xor_b32 v64, s62, v60\n"
"s_mul_i32 s61, s55, 327680\n"
"s_waitcnt lgkmcnt(0)\n"
"s_mov_b64 s[20:21], s[4:5]\n"
"s_add_u32 s20, s20, s61\n"
"s_addc_u32 s21, s21, 0\n"
"s_and_b32 s21, s21, 0xffff\n"
"s_mov_b32 s22, 327680\n"
"s_mov_b32 s23, 0x20000\n"
"s_lshl_b32 s61, s54, 16\n"
"s_lshl_b32 s62, s54, 12\n"
"s_add_u32 m0, s62, 0\n"
"s_add_u32 s65, s61, 0\n"
"buffer_load_dwordx4 v60, s[20:23], s65 offen  lds\n"
"buffer_load_dwordx4 v61, s[20:23], s65 offen offset:1024  lds\n"
"buffer_load_dwordx4 v62, s[20:23], s65 offen offset:2048  lds\n"
"buffer_load_dwordx4 v63, s[20:23], s65 offen offset:3072  lds\n"
"s_lshl_b32 s61, s54, 14\n"
"s_lshl_b32 s62, s54, 10\n"
"s_add_u32 m0, s62, 16384\n"
"s_add_u32 s65, s61, 262144\n"
"buffer_load_dwordx4 v64, s[20:23], s65 offen  lds\n"
"s_lshl_b32 s62, s57, 5\n"
"s_add_u32 s62, s62, s60\n"
"s_mul_i32 s62, s62, 10240\n"
"s_lshr_b32 s63, s55, 2\n"
"s_mul_i32 s63, s63, 1310720\n"
"s_add_u32 s62, s62, s63\n"
"s_mul_i32 s63, s54, 2560\n"
"s_add_u32 s62, s62, s63\n"
"s_add_u32 s74, s4, s62\n"
"s_addc_u32 s75, s5, 0\n"
"v_lshlrev_b32 v52, 7, v0\n"
"s_mov_b64 exec, 0xfffff\n"
"global_load_dword v55, v52, s[74:75]\n"
"s_mov_b64 exec, -1\n"
"s_branch .Lk_B_2_%=\n"
".Lk_A0_1_%=:\n"
"s_sub_u32 s78, s54, 4\n"
"s_lshl_b32 s79, s78, 12\n"
"v_lshrrev_b32 v42, 4, v0\n"
"v_and_b32 v41, 15, v0\n"
"v_lshlrev_b32 v43, 1, v42\n"
"v_xor_b32 v41, v41, v43\n"
"v_lshlrev_b32 v41, 4, v41\n"
"v_mul_u32_u24 v43, 8192, v42\n"
"v_add_u32 v6, v43, v41\n"
"v_xor_b32 v8, 128, v6\n"
"v_add_u32 v7, 31744, v6\n"
"v_add_u32 v9, 95232, v8\n"
"v_add_u32 v8, 63488, v8\n"
"s_lshl_b32 s61, s56, 8\n"
"s_waitcnt lgkmcnt(0)\n"
"s_mov_b64 s[24:25], s[10:11]\n"
"s_add_u32 s24, s24, s61\n"
"s_addc_u32 s25, s25, 0\n"
"s_and_b32 s25, s25, 0xffff\n"
"s_sub_u32 s26, 8388608, s61\n"
"s_mov_b32 s27, 0x20000\n"
"s_lshl_b32 s61, s78, 17\n"
"s_add_u32 m0, s79, 61440\n"
"s_add_u32 s65, s61, 0\n"
"buffer_load_dwordx4 v6, s[24:27], s65 offen  lds\n"
"buffer_load_dwordx4 v7, s[24:27], s65 offen offset:1024  lds\n"
"buffer_load_dwordx4 v8, s[24:27], s65 offen offset:2048  lds\n"
"buffer_load_dwordx4 v9, s[24:27], s65 offen offset:3072  lds\n"
"s_getpc_b64 s[74:75]\n"
".Lk_pc_9_%=:\n"
"s_add_u32 s74, s74, .Lk_start_%=-.Lk_pc_9_%=\n"
"s_addc_u32 s75, s75, -1\n"
"s_mov_b32 s62, .Lk_end_%=-.Lk_start_%=-4\n"
"s_lshl_b32 s61, s60, 7\n"
"s_lshl_b32 s63, s78, 14\n"
"s_add_u32 s61, s61, s63\n"
"v_lshl_add_u32 v52, v0, 12, s61\n"
"v_min_u32 v52, s62, v52\n"
"v_and_b32 v52, -4, v52\n"
"s_mov_b64 exec, 0xf\n"
"global_load_dword v55, v52, s[74:75]\n"
"s_mov_b64 exec, -1\n"
"s_and_b32 s66, s54, 1\n"
"s_lshr_b32 s67, s54, 1\n"
"s_and_b32 s67, s67, 1\n"
"s_lshl_b32 s61, s55, 5\n"
"s_add_u32 s61, s61, s56\n"
"s_lshl_b32 s61, s61, 17\n"
"s_mov_b64 s[28:29], s[6:7]\n"
"s_add_u32 s28, s28, s61\n"
"s_addc_u32 s29, s29, 0\n"
"s_and_b32 s29, s29, 0xffff\n"
"s_mov_b32 s30, 131072\n"
"s_mov_b32 s31, 0x20000\n"
"s_mov_b64 s[32:33], s[8:9]\n"
"s_add_u32 s32, s32, s61\n"
"s_addc_u32 s33, s33, 0\n"
"s_and_b32 s33, s33, 0xffff\n"
"s_mov_b32 s34, 131072\n"
"s_mov_b32 s35, 0x20000\n"
"s_lshl_b32 s61, s57, 21\n"
"s_lshl_b32 s62, s58, 8\n"
"s_add_u32 s61, s61, s62\n"
"s_mov_b64 s[36:37], s[12:13]\n"
"s_add_u32 s36, s36, s61\n"
"s_addc_u32 s37, s37, 0\n"
"s_and_b32 s37, s37, 0xffff\n"
"s_sub_u32 s38, 8388608, s61\n"
"s_mov_b32 s39, 0x20000\n"
"s_lshl_b32 s61, s55, 2\n"
"s_add_u32 s61, s61, s57\n"
"s_mul_i32 s61, s61, 327680\n"
"s_mov_b64 s[40:41], s[16:17]\n"
"s_add_u32 s61, s61, 1048576\n"
"s_add_u32 s40, s40, s61\n"
"s_addc_u32 s41, s41, 0\n"
"s_and_b32 s41, s41, 0xffff\n"
"s_mov_b32 s42, 327680\n"
"s_mov_b32 s43, 0x20000\n"
"s_lshl_b32 s61, s55, 3\n"
"s_add_u32 s61, s61, s58\n"
"s_mul_i32 s61, s61, 163840\n"
"s_mov_b64 s[44:45], s[16:17]\n"
"s_add_u32 s61, s61, 11534336\n"
"s_add_u32 s44, s44, s61\n"
"s_addc_u32 s45, s45, 0\n"
"s_and_b32 s45, s45, 0xffff\n"
"s_mov_b32 s46, 163840\n"
"s_mov_b32 s47, 0x20000\n"
"s_mul_i32 s61, s55, 327680\n"
"s_lshl_b32 s62, s58, 8\n"
"s_add_u32 s61, s61, s62\n"
"s_mov_b64 s[48:49], s[14:15]\n"
"s_add_u32 s48, s48, s61\n"
"s_addc_u32 s49, s49, 0\n"
"s_and_b32 s49, s49, 0xffff\n"
"s_sub_u32 s50, 327680, s62\n"
"s_mov_b32 s51, 0x20000\n"
"s_mov_b64 s[52:53], s[16:17]\n"
"v_lshrrev_b32 v42, 4, v0\n"
"v_and_b32 v41, 15, v0\n"
"v_xor_b32 v43, v41, v42\n"
"v_lshlrev_b32 v43, 4, v43\n"
"v_lshl_add_u32 v10, v42, 8, v43\n"
"v_xor_b32 v11, 64, v10\n"
"v_xor_b32 v12, 128, v10\n"
"v_xor_b32 v13, 192, v10\n"
"v_lshlrev_b32 v43, 1, v42\n"
"v_xor_b32 v43, v41, v43\n"
"v_lshlrev_b32 v43, 4, v43\n"
"v_lshl_add_u32 v14, v42, 8, v43\n"
"v_xor_b32 v15, 128, v14\n"
"v_mov_b32 v16, v14\n"
"v_mov_b32 v17, v15\n"
"v_lshlrev_b32 v18, 4, v0\n"
"v_lshrrev_b32 v42, 4, v0\n"
"v_and_b32 v41, 15, v0\n"
"v_lshrrev_b32 v40, 2, v41\n"
"v_and_b32 v43, 3, v0\n"
"v_and_b32 v44, 1, v42\n"
"v_lshl_or_b32 v44, v44, 2, v40\n"
"v_lshl_add_u32 v45, v42, 2, v40\n"
"v_lshlrev_b32 v45, 8, v45\n"
"v_lshl_add_u32 v45, v43, 3, v45\n"
"s_lshl_b32 s61, s66, 2\n"
"s_add_u32 s62, s61, 0\n"
"v_xor_b32 v46, s62, v44\n"
"v_lshl_add_u32 v46, v46, 5, v45\n"
"v_add_u32 v20, 110592, v46\n"
"v_add_u32 v24, 61440, v46\n"
"s_add_u32 s62, s61, 1\n"
"v_xor_b32 v46, s62, v44\n"
"v_lshl_add_u32 v46, v46, 5, v45\n"
"v_add_u32 v21, 110592, v46\n"
"v_add_u32 v25, 61440, v46\n"
"s_add_u32 s62, s61, 2\n"
"v_xor_b32 v46, s62, v44\n"
"v_lshl_add_u32 v46, v46, 5, v45\n"
"v_add_u32 v22, 110592, v46\n"
"v_add_u32 v26, 61440, v46\n"
"s_add_u32 s62, s61, 3\n"
"v_xor_b32 v46, s62, v44\n"
"v_lshl_add_u32 v46, v46, 5, v45\n"
"v_add_u32 v23, 110592, v46\n"
"v_add_u32 v27, 61440, v46\n"
"s_mul_i32 s61, s67, 10240\n"
"v_lshl_add_u32 v19, v0, 4, s61\n"
"v_lshrrev_b32 v43, 1, v42\n"
"v_lshrrev_b32 v44, 1, v41\n"
"v_xor_b32 v43, v43, v44\n"
"v_lshlrev_b32 v43, 4, v43\n"
"v_lshl_add_u32 v43, v41, 7, v43\n"
"v_and_b32 v44, 1, v42\n"
"v_lshl_add_u32 v43, v44, 3, v43\n"
"s_mul_i32 s62, s66, 20480\n"
"s_add_u32 s61, s61, s62\n"
"v_add_u32 v28, s61, v43\n"
"v_xor_b32 v29, 32, v28\n"
"v_xor_b32 v30, 64, v28\n"
"v_xor_b32 v31, 96, v28\n"
"s_mul_i32 s62, s67, 1280\n"
"s_add_u32 s62, s62, 159744\n"
"v_and_b32 v43, 31, v0\n"
"v_lshl_add_u32 v32, v43, 2, s62\n"
"s_add_u32 s61, s79, 110592\n"
"v_lshl_add_u32 v33, v0, 4, s61\n"
"s_lshl_b32 s61, s59, 9\n"
"v_lshl_add_u32 v34, v43, 2, s61\n"
"s_and_b32 s61, s59, 1\n"
"s_lshl_b32 s61, s61, 5\n"
"s_add_u32 s61, s61, s60\n"
"s_lshl_b32 s61, s61, 2\n"
"s_add_u32 s61, s61, s78\n"
"s_lshl_b32 s61, s61, 13\n"
"s_lshl_b32 s62, s57, 21\n"
"s_add_u32 s61, s61, s62\n"
"s_mov_b64 s[76:77], s[12:13]\n"
"s_add_u32 s76, s76, s61\n"
"s_addc_u32 s77, s77, 0\n"
"v_lshlrev_b32 v35, 7, v0\n"
"s_lshl_b32 s64, s58, 1\n"
"s_mul_i32 s61, s55, 327680\n"
"s_mov_b64 s[20:21], s[4:5]\n"
"s_add_u32 s20, s20, s61\n"
"s_addc_u32 s21, s21, 0\n"
"s_and_b32 s21, s21, 0xffff\n"
"s_mov_b32 s22, 327680\n"
"s_mov_b32 s23, 0x20000\n"
"v_lshrrev_b32 v40, 3, v0\n"
"v_and_b32 v41, 7, v0\n"
"v_lshrrev_b32 v42, 4, v0\n"
"v_xor_b32 v41, v41, v42\n"
"v_lshlrev_b32 v41, 4, v41\n"
"v_lshl_add_u32 v2, v40, 11, v41\n"
"v_xor_b32 v3, 64, v2\n"
"v_add_u32 v4, 30720, v2\n"
"v_add_u32 v5, 46080, v3\n"
"v_add_u32 v3, 15360, v3\n"
"s_lshl_b32 s61, s78, 17\n"
"s_add_u32 m0, s79, 77824\n"
"s_add_u32 s65, s61, 524288\n"
"buffer_load_dwordx4 v6, s[24:27], s65 offen  lds\n"
"buffer_load_dwordx4 v7, s[24:27], s65 offen offset:1024  lds\n"
"buffer_load_dwordx4 v8, s[24:27], s65 offen offset:2048  lds\n"
"buffer_load_dwordx4 v9, s[24:27], s65 offen offset:3072  lds\n"
"s_cmp_eq_u32 s78, 1\n"
"s_cbranch_scc1 .Lk_b_12_%=\n"
"s_cmp_eq_u32 s78, 2\n"
"s_cbranch_scc1 .Lk_b_13_%=\n"
"s_cmp_eq_u32 s78, 3\n"
"s_cbranch_scc1 .Lk_b_14_%=\n"
"s_waitcnt vmcnt(5)\n"
"s_barrier\n"
"s_mov_b32 m0, 40960\n"
"s_mov_b32 s65, 256\n"
"buffer_load_dwordx4 v2, s[20:23], s65 offen  lds\n"
"buffer_load_dwordx4 v3, s[20:23], s65 offen offset:1024  lds\n"
"buffer_load_dwordx4 v4, s[20:23], s65 offen offset:2048  lds\n"
"buffer_load_dwordx4 v5, s[20:23], s65 offen offset:3072  lds\n"
"s_mov_b32 m0, 45056\n"
"s_mov_b32 s65, 65792\n"
"buffer_load_dwordx4 v2, s[20:23], s65 offen  lds\n"
"buffer_load_dwordx4 v3, s[20:23], s65 offen offset:1024  lds\n"
"buffer_load_dwordx4 v4, s[20:23], s65 offen offset:2048  lds\n"
"buffer_load_dwordx4 v5, s[20:23], s65 offen offset:3072  lds\n"
"s_mov_b32 m0, 49152\n"
"s_mov_b32 s65, 131328\n"
"buffer_load_dwordx4 v2, s[20:23], s65 offen  lds\n"
"buffer_load_dwordx4 v3, s[20:23], s65 offen offset:1024  lds\n"
"buffer_load_dwordx4 v4, s[20:23], s65 offen offset:2048  lds\n"
"buffer_load_dwordx4 v5, s[20:23], s65 offen offset:3072  lds\n"
"s_waitcnt vmcnt(12)\n"
"s_barrier\n"
"s_mov_b32 m0, 0\n"
"s_mov_b32 s65, 384\n"
"buffer_load_dwordx4 v2, s[20:23], s65 offen  lds\n"
"buffer_load_dwordx4 v3, s[20:23], s65 offen offset:1024  lds\n"
"buffer_load_dwordx4 v4, s[20:23], s65 offen offset:2048  lds\n"
"buffer_load_dwordx4 v5, s[20:23], s65 offen offset:3072  lds\n"
"s_mov_b32 m0, 4096\n"
"s_mov_b32 s65, 65920\n"
"buffer_load_dwordx4 v2, s[20:23], s65 offen  lds\n"
"buffer_load_dwordx4 v3, s[20:23], s65 offen offset:1024  lds\n"
"buffer_load_dwordx4 v4, s[20:23], s65 offen offset:2048  lds\n"
"buffer_load_dwordx4 v5, s[20:23], s65 offen offset:3072  lds\n"
"s_mov_b32 m0, 8192\n"
"s_mov_b32 s65, 131456\n"
"buffer_load_dwordx4 v2, s[20:23], s65 offen  lds\n"
"buffer_load_dwordx4 v3, s[20:23], s65 offen offset:1024  lds\n"
"buffer_load_dwordx4 v4, s[20:23], s65 offen offset:2048  lds\n"
"buffer_load_dwordx4 v5, s[20:23], s65 offen offset:3072  lds\n"
"s_waitcnt vmcnt(12)\n"
"s_barrier\n"
"s_mov_b32 m0, 20480\n"
"s_mov_b32 s65, 512\n"
"buffer_load_dwordx4 v2, s[20:23], s65 offen  lds\n"
"buffer_load_dwordx4 v3, s[20:23], s65 offen offset:1024  lds\n"
"buffer_load_dwordx4 v4, s[20:23], s65 offen offset:2048  lds\n"
"buffer_load_dwordx4 v5, s[20:23], s65 offen offset:3072  lds\n"
"s_mov_b32 m0, 24576\n"
"s_mov_b32 s65, 66048\n"
"buffer_load_dwordx4 v2, s[20:23], s65 offen  lds\n"
"buffer_load_dwordx4 v3, s[20:23], s65 offen offset:1024  lds\n"
"buffer_load_dwordx4 v4, s[20:23], s65 offen offset:2048  lds\n"
"buffer_load_dwordx4 v5, s[20:23], s65 offen offset:3072  lds\n"
"s_mov_b32 m0, 28672\n"
"s_mov_b32 s65, 131584\n"
"buffer_load_dwordx4 v2, s[20:23], s65 offen  lds\n"
"buffer_load_dwordx4 v3, s[20:23], s65 offen offset:1024  lds\n"
"buffer_load_dwordx4 v4, s[20:23], s65 offen offset:2048  lds\n"
"buffer_load_dwordx4 v5, s[20:23], s65 offen offset:3072  lds\n"
"s_waitcnt vmcnt(12)\n"
"s_barrier\n"
"s_mov_b32 m0, 40960\n"
"s_mov_b32 s65, 640\n"
"buffer_load_dwordx4 v2, s[20:23], s65 offen  lds\n"
"buffer_load_dwordx4 v3, s[20:23], s65 offen offset:1024  lds\n"
"buffer_load_dwordx4 v4, s[20:23], s65 offen offset:2048  lds\n"
"buffer_load_dwordx4 v5, s[20:23], s65 offen offset:3072  lds\n"
"s_mov_b32 m0, 45056\n"
"s_mov_b32 s65, 66176\n"
"buffer_load_dwordx4 v2, s[20:23], s65 offen  lds\n"
"buffer_load_dwordx4 v3, s[20:23], s65 offen offset:1024  lds\n"
"buffer_load_dwordx4 v4, s[20:23], s65 offen offset:2048  lds\n"
"buffer_load_dwordx4 v5, s[20:23], s65 offen offset:3072  lds\n"
"s_mov_b32 m0, 49152\n"
"s_mov_b32 s65, 131712\n"
"buffer_load_dwordx4 v2, s[20:23], s65 offen  lds\n"
"buffer_load_dwordx4 v3, s[20:23], s65 offen offset:1024  lds\n"
"buffer_load_dwordx4 v4, s[20:23], s65 offen offset:2048  lds\n"
"buffer_load_dwordx4 v5, s[20:23], s65 offen offset:3072  lds\n"
"s_waitcnt vmcnt(12)\n"
"s_barrier\n"
"s_mov_b32 m0, 0\n"
"s_mov_b32 s65, 768\n"
"buffer_load_dwordx4 v2, s[20:23], s65 offen  lds\n"
"buffer_load_dwordx4 v3, s[20:23], s65 offen offset:1024  lds\n"
"buffer_load_dwordx4 v4, s[20:23], s65 offen offset:2048  lds\n"
"buffer_load_dwordx4 v5, s[20:23], s65 offen offset:3072  lds\n"
"s_mov_b32 m0, 4096\n"
"s_mov_b32 s65, 66304\n"
"buffer_load_dwordx4 v2, s[20:23], s65 offen  lds\n"
"buffer_load_dwordx4 v3, s[20:23], s65 offen offset:1024  lds\n"
"buffer_load_dwordx4 v4, s[20:23], s65 offen offset:2048  lds\n"
"buffer_load_dwordx4 v5, s[20:23], s65 offen offset:3072  lds\n"
"s_mov_b32 m0, 8192\n"
"s_mov_b32 s65, 131840\n"
"buffer_load_dwordx4 v2, s[20:23], s65 offen  lds\n"
"buffer_load_dwordx4 v3, s[20:23], s65 offen offset:1024  lds\n"
"buffer_load_dwordx4 v4, s[20:23], s65 offen offset:2048  lds\n"
"buffer_load_dwordx4 v5, s[20:23], s65 offen offset:3072  lds\n"
"s_waitcnt vmcnt(12)\n"
"s_barrier\n"
"s_mov_b32 m0, 20480\n"
"s_mov_b32 s65, 896\n"
"buffer_load_dwordx4 v2, s[20:23], s65 offen  lds\n"
"buffer_load_dwordx4 v3, s[20:23], s65 offen offset:1024  lds\n"
"buffer_load_dwordx4 v4, s[20:23], s65 offen offset:2048  lds\n"
"buffer_load_dwordx4 v5, s[20:23], s65 offen offset:3072  lds\n"
"s_mov_b32 m0, 24576\n"
"s_mov_b32 s65, 66432\n"
"buffer_load_dwordx4 v2, s[20:23], s65 offen  lds\n"
"buffer_load_dwordx4 v3, s[20:23], s65 offen offset:1024  lds\n"
"buffer_load_dwordx4 v4, s[20:23], s65 offen offset:2048  lds\n"
"buffer_load_dwordx4 v5, s[20:23], s65 offen offset:3072  lds\n"
"s_mov_b32 m0, 28672\n"
"s_mov_b32 s65, 131968\n"
"buffer_load_dwordx4 v2, s[20:23], s65 offen  lds\n"
"buffer_load_dwordx4 v3, s[20:23], s65 offen offset:1024  lds\n"
"buffer_load_dwordx4 v4, s[20:23], s65 offen offset:2048  lds\n"
"buffer_load_dwordx4 v5, s[20:23], s65 offen offset:3072  lds\n"
"s_waitcnt vmcnt(12)\n"
"s_barrier\n"
"s_mov_b32 m0, 40960\n"
"s_mov_b32 s65, 1024\n"
"buffer_load_dwordx4 v2, s[20:23], s65 offen  lds\n"
"buffer_load_dwordx4 v3, s[20:23], s65 offen offset:1024  lds\n"
"buffer_load_dwordx4 v4, s[20:23], s65 offen offset:2048  lds\n"
"buffer_load_dwordx4 v5, s[20:23], s65 offen offset:3072  lds\n"
"s_mov_b32 m0, 45056\n"
"s_mov_b32 s65, 66560\n"
"buffer_load_dwordx4 v2, s[20:23], s65 offen  lds\n"
"buffer_load_dwordx4 v3, s[20:23], s65 offen offset:1024  lds\n"
"buffer_load_dwordx4 v4, s[20:23], s65 offen offset:2048  lds\n"
"buffer_load_dwordx4 v5, s[20:23], s65 offen offset:3072  lds\n"
"s_mov_b32 m0, 49152\n"
"s_mov_b32 s65, 132096\n"
"buffer_load_dwordx4 v2, s[20:23], s65 offen  lds\n"
"buffer_load_dwordx4 v3, s[20:23], s65 offen offset:1024  lds\n"
"buffer_load_dwordx4 v4, s[20:23], s65 offen offset:2048  lds\n"
"buffer_load_dwordx4 v5, s[20:23], s65 offen offset:3072  lds\n"
"s_waitcnt vmcnt(12)\n"
"s_barrier\n"
"s_mov_b32 m0, 0\n"
"s_mov_b32 s65, 1152\n"
"buffer_load_dwordx4 v2, s[20:23], s65 offen  lds\n"
"buffer_load_dwordx4 v3, s[20:23], s65 offen offset:1024  lds\n"
"buffer_load_dwordx4 v4, s[20:23], s65 offen offset:2048  lds\n"
"buffer_load_dwordx4 v5, s[20:23], s65 offen offset:3072  lds\n"
"s_mov_b32 m0, 4096\n"
"s_mov_b32 s65, 66688\n"
"buffer_load_dwordx4 v2, s[20:23], s65 offen  lds\n"
"buffer_load_dwordx4 v3, s[20:23], s65 offen offset:1024  lds\n"
"buffer_load_dwordx4 v4, s[20:23], s65 offen offset:2048  lds\n"
"buffer_load_dwordx4 v5, s[20:23], s65 offen offset:3072  lds\n"
"s_mov_b32 m0, 8192\n"
"s_mov_b32 s65, 132224\n"
"buffer_load_dwordx4 v2, s[20:23], s65 offen  lds\n"
"buffer_load_dwordx4 v3, s[20:23], s65 offen offset:1024  lds\n"
"buffer_load_dwordx4 v4, s[20:23], s65 offen offset:2048  lds\n"
"buffer_load_dwordx4 v5, s[20:23], s65 offen offset:3072  lds\n"
"s_waitcnt vmcnt(12)\n"
"s_barrier\n"
"s_mov_b32 m0, 20480\n"
"s_mov_b32 s65, 1280\n"
"buffer_load_dwordx4 v2, s[20:23], s65 offen  lds\n"
"buffer_load_dwordx4 v3, s[20:23], s65 offen offset:1024  lds\n"
"buffer_load_dwordx4 v4, s[20:23], s65 offen offset:2048  lds\n"
"buffer_load_dwordx4 v5, s[20:23], s65 offen offset:3072  lds\n"
"s_mov_b32 m0, 24576\n"
"s_mov_b32 s65, 66816\n"
"buffer_load_dwordx4 v2, s[20:23], s65 offen  lds\n"
"buffer_load_dwordx4 v3, s[20:23], s65 offen offset:1024  lds\n"
"buffer_load_dwordx4 v4, s[20:23], s65 offen offset:2048  lds\n"
"buffer_load_dwordx4 v5, s[20:23], s65 offen offset:3072  lds\n"
"s_mov_b32 m0, 28672\n"
"s_mov_b32 s65, 132352\n"
"buffer_load_dwordx4 v2, s[20:23], s65 offen  lds\n"
"buffer_load_dwordx4 v3, s[20:23], s65 offen offset:1024  lds\n"
"buffer_load_dwordx4 v4, s[20:23], s65 offen offset:2048  lds\n"
"buffer_load_dwordx4 v5, s[20:23], s65 offen offset:3072  lds\n"
"s_waitcnt vmcnt(12)\n"
"s_barrier\n"
"s_mov_b32 m0, 40960\n"
"s_mov_b32 s65, 1408\n"
"buffer_load_dwordx4 v2, s[20:23], s65 offen  lds\n"
"buffer_load_dwordx4 v3, s[20:23], s65 offen offset:1024  lds\n"
"buffer_load_dwordx4 v4, s[20:23], s65 offen offset:2048  lds\n"
"buffer_load_dwordx4 v5, s[20:23], s65 offen offset:3072  lds\n"
"s_mov_b32 m0, 45056\n"
"s_mov_b32 s65, 66944\n"
"buffer_load_dwordx4 v2, s[20:23], s65 offen  lds\n"
"buffer_load_dwordx4 v3, s[20:23], s65 offen offset:1024  lds\n"
"buffer_load_dwordx4 v4, s[20:23], s65 offen offset:2048  lds\n"
"buffer_load_dwordx4 v5, s[20:23], s65 offen offset:3072  lds\n"
"s_mov_b32 m0, 49152\n"
"s_mov_b32 s65, 132480\n"
"buffer_load_dwordx4 v2, s[20:23], s65 offen  lds\n"
"buffer_load_dwordx4 v3, s[20:23], s65 offen offset:1024  lds\n"
"buffer_load_dwordx4 v4, s[20:23], s65 offen offset:2048  lds\n"
"buffer_load_dwordx4 v5, s[20:23], s65 offen offset:3072  lds\n"
"s_waitcnt vmcnt(12)\n"
"s_barrier\n"
"s_mov_b32 m0, 0\n"
"s_mov_b32 s65, 1536\n"
"buffer_load_dwordx4 v2, s[20:23], s65 offen  lds\n"
"buffer_load_dwordx4 v3, s[20:23], s65 offen offset:1024  lds\n"
"buffer_load_dwordx4 v4, s[20:23], s65 offen offset:2048  lds\n"
"buffer_load_dwordx4 v5, s[20:23], s65 offen offset:3072  lds\n"
"s_mov_b32 m0, 4096\n"
"s_mov_b32 s65, 67072\n"
"buffer_load_dwordx4 v2, s[20:23], s65 offen  lds\n"
"buffer_load_dwordx4 v3, s[20:23], s65 offen offset:1024  lds\n"
"buffer_load_dwordx4 v4, s[20:23], s65 offen offset:2048  lds\n"
"buffer_load_dwordx4 v5, s[20:23], s65 offen offset:3072  lds\n"
"s_mov_b32 m0, 8192\n"
"s_mov_b32 s65, 132608\n"
"buffer_load_dwordx4 v2, s[20:23], s65 offen  lds\n"
"buffer_load_dwordx4 v3, s[20:23], s65 offen offset:1024  lds\n"
"buffer_load_dwordx4 v4, s[20:23], s65 offen offset:2048  lds\n"
"buffer_load_dwordx4 v5, s[20:23], s65 offen offset:3072  lds\n"
"s_waitcnt vmcnt(12)\n"
"s_barrier\n"
"s_mov_b32 m0, 20480\n"
"s_mov_b32 s65, 1664\n"
"buffer_load_dwordx4 v2, s[20:23], s65 offen  lds\n"
"buffer_load_dwordx4 v3, s[20:23], s65 offen offset:1024  lds\n"
"buffer_load_dwordx4 v4, s[20:23], s65 offen offset:2048  lds\n"
"buffer_load_dwordx4 v5, s[20:23], s65 offen offset:3072  lds\n"
"s_mov_b32 m0, 24576\n"
"s_mov_b32 s65, 67200\n"
"buffer_load_dwordx4 v2, s[20:23], s65 offen  lds\n"
"buffer_load_dwordx4 v3, s[20:23], s65 offen offset:1024  lds\n"
"buffer_load_dwordx4 v4, s[20:23], s65 offen offset:2048  lds\n"
"buffer_load_dwordx4 v5, s[20:23], s65 offen offset:3072  lds\n"
"s_mov_b32 m0, 28672\n"
"s_mov_b32 s65, 132736\n"
"buffer_load_dwordx4 v2, s[20:23], s65 offen  lds\n"
"buffer_load_dwordx4 v3, s[20:23], s65 offen offset:1024  lds\n"
"buffer_load_dwordx4 v4, s[20:23], s65 offen offset:2048  lds\n"
"buffer_load_dwordx4 v5, s[20:23], s65 offen offset:3072  lds\n"
"s_waitcnt vmcnt(12)\n"
"s_barrier\n"
"s_mov_b32 m0, 40960\n"
"s_mov_b32 s65, 1792\n"
"buffer_load_dwordx4 v2, s[20:23], s65 offen  lds\n"
"buffer_load_dwordx4 v3, s[20:23], s65 offen offset:1024  lds\n"
"buffer_load_dwordx4 v4, s[20:23], s65 offen offset:2048  lds\n"
"buffer_load_dwordx4 v5, s[20:23], s65 offen offset:3072  lds\n"
"s_mov_b32 m0, 45056\n"
"s_mov_b32 s65, 67328\n"
"buffer_load_dwordx4 v2, s[20:23], s65 offen  lds\n"
"buffer_load_dwordx4 v3, s[20:23], s65 offen offset:1024  lds\n"
"buffer_load_dwordx4 v4, s[20:23], s65 offen offset:2048  lds\n"
"buffer_load_dwordx4 v5, s[20:23], s65 offen offset:3072  lds\n"
"s_mov_b32 m0, 49152\n"
"s_mov_b32 s65, 132864\n"
"buffer_load_dwordx4 v2, s[20:23], s65 offen  lds\n"
"buffer_load_dwordx4 v3, s[20:23], s65 offen offset:1024  lds\n"
"buffer_load_dwordx4 v4, s[20:23], s65 offen offset:2048  lds\n"
"buffer_load_dwordx4 v5, s[20:23], s65 offen offset:3072  lds\n"
"s_waitcnt vmcnt(12)\n"
"s_barrier\n"
"s_mov_b32 m0, 0\n"
"s_mov_b32 s65, 1920\n"
"buffer_load_dwordx4 v2, s[20:23], s65 offen  lds\n"
"buffer_load_dwordx4 v3, s[20:23], s65 offen offset:1024  lds\n"
"buffer_load_dwordx4 v4, s[20:23], s65 offen offset:2048  lds\n"
"buffer_load_dwordx4 v5, s[20:23], s65 offen offset:3072  lds\n"
"s_mov_b32 m0, 4096\n"
"s_mov_b32 s65, 67456\n"
"buffer_load_dwordx4 v2, s[20:23], s65 offen  lds\n"
"buffer_load_dwordx4 v3, s[20:23], s65 offen offset:1024  lds\n"
"buffer_load_dwordx4 v4, s[20:23], s65 offen offset:2048  lds\n"
"buffer_load_dwordx4 v5, s[20:23], s65 offen offset:3072  lds\n"
"s_mov_b32 m0, 8192\n"
"s_mov_b32 s65, 132992\n"
"buffer_load_dwordx4 v2, s[20:23], s65 offen  lds\n"
"buffer_load_dwordx4 v3, s[20:23], s65 offen offset:1024  lds\n"
"buffer_load_dwordx4 v4, s[20:23], s65 offen offset:2048  lds\n"
"buffer_load_dwordx4 v5, s[20:23], s65 offen offset:3072  lds\n"
"s_waitcnt vmcnt(12)\n"
"s_barrier\n"
"s_add_u32 m0, s79, 77824\n"
"s_add_u32 s65, s79, 0\n"
"buffer_load_dwordx4 v14, s[32:35], s65 offen offset:0 nt lds\n"
"buffer_load_dwordx4 v15, s[32:35], s65 offen offset:1024 nt lds\n"
"buffer_load_dwordx4 v16, s[32:35], s65 offen offset:2048 nt lds\n"
"buffer_load_dwordx4 v17, s[32:35], s65 offen offset:3072 nt lds\n"
"s_waitcnt vmcnt(4)\n"
"s_barrier\n"
"s_add_u32 m0, s79, 94208\n"
"s_add_u32 s65, s79, 49152\n"
"buffer_load_dwordx4 v10, s[28:31], s65 offen offset:0 nt lds\n"
"buffer_load_dwordx4 v11, s[28:31], s65 offen offset:1024 nt lds\n"
"buffer_load_dwordx4 v12, s[28:31], s65 offen offset:2048 nt lds\n"
"buffer_load_dwordx4 v13, s[28:31], s65 offen offset:3072 nt lds\n"
"s_barrier\n"
"s_add_u32 m0, s79, 61440\n"
"s_add_u32 s65, s79, 16384\n"
"buffer_load_dwordx4 v14, s[32:35], s65 offen offset:0 nt lds\n"
"buffer_load_dwordx4 v15, s[32:35], s65 offen offset:1024 nt lds\n"
"buffer_load_dwordx4 v16, s[32:35], s65 offen offset:2048 nt lds\n"
"buffer_load_dwordx4 v17, s[32:35], s65 offen offset:3072 nt lds\n"
"s_branch .Lk_M_10_%=\n"
".Lk_b_12_%=:\n"
"s_waitcnt vmcnt(5)\n"
"s_barrier\n"
"s_mov_b32 m0, 53248\n"
"s_mov_b32 s65, 196864\n"
"buffer_load_dwordx4 v2, s[20:23], s65 offen  lds\n"
"buffer_load_dwordx4 v3, s[20:23], s65 offen offset:1024  lds\n"
"buffer_load_dwordx4 v4, s[20:23], s65 offen offset:2048  lds\n"
"buffer_load_dwordx4 v5, s[20:23], s65 offen offset:3072  lds\n"
"s_mov_b32 m0, 57344\n"
"s_mov_b32 s65, 262400\n"
"buffer_load_dwordx4 v2, s[20:23], s65 offen  lds\n"
"buffer_load_dwordx4 v3, s[20:23], s65 offen offset:1024  lds\n"
"buffer_load_dwordx4 v4, s[20:23], s65 offen offset:2048  lds\n"
"buffer_load_dwordx4 v5, s[20:23], s65 offen offset:3072  lds\n"
"s_mov_b32 m0, 94208\n"
"s_mov_b32 s65, 1048576\n"
"buffer_load_dwordx4 v6, s[24:27], s65 offen  lds\n"
"buffer_load_dwordx4 v7, s[24:27], s65 offen offset:1024  lds\n"
"buffer_load_dwordx4 v8, s[24:27], s65 offen offset:2048  lds\n"
"buffer_load_dwordx4 v9, s[24:27], s65 offen offset:3072  lds\n"
"s_waitcnt vmcnt(12)\n"
"s_barrier\n"
"s_mov_b32 m0, 12288\n"
"s_mov_b32 s65, 196992\n"
"buffer_load_dwordx4 v2, s[20:23], s65 offen  lds\n"
"buffer_load_dwordx4 v3, s[20:23], s65 offen offset:1024  lds\n"
"buffer_load_dwordx4 v4, s[20:23], s65 offen offset:2048  lds\n"
"buffer_load_dwordx4 v5, s[20:23], s65 offen offset:3072  lds\n"
"s_mov_b32 m0, 16384\n"
"s_mov_b32 s65, 262528\n"
"buffer_load_dwordx4 v2, s[20:23], s65 offen  lds\n"
"buffer_load_dwordx4 v3, s[20:23], s65 offen offset:1024  lds\n"
"buffer_load_dwordx4 v4, s[20:23], s65 offen offset:2048  lds\n"
"buffer_load_dwordx4 v5, s[20:23], s65 offen offset:3072  lds\n"
"s_mov_b32 m0, 61440\n"
"s_mov_b32 s65, 1572864\n"
"buffer_load_dwordx4 v6, s[24:27], s65 offen  lds\n"
"buffer_load_dwordx4 v7, s[24:27], s65 offen offset:1024  lds\n"
"buffer_load_dwordx4 v8, s[24:27], s65 offen offset:2048  lds\n"
"buffer_load_dwordx4 v9, s[24:27], s65 offen offset:3072  lds\n"
"s_waitcnt vmcnt(12)\n"
"s_barrier\n"
"s_mov_b32 m0, 32768\n"
"s_mov_b32 s65, 197120\n"
"buffer_load_dwordx4 v2, s[20:23], s65 offen  lds\n"
"buffer_load_dwordx4 v3, s[20:23], s65 offen offset:1024  lds\n"
"buffer_load_dwordx4 v4, s[20:23], s65 offen offset:2048  lds\n"
"buffer_load_dwordx4 v5, s[20:23], s65 offen offset:3072  lds\n"
"s_mov_b32 m0, 36864\n"
"s_mov_b32 s65, 262656\n"
"buffer_load_dwordx4 v2, s[20:23], s65 offen  lds\n"
"buffer_load_dwordx4 v3, s[20:23], s65 offen offset:1024  lds\n"
"buffer_load_dwordx4 v4, s[20:23], s65 offen offset:2048  lds\n"
"buffer_load_dwordx4 v5, s[20:23], s65 offen offset:3072  lds\n"
"s_mov_b32 m0, 77824\n"
"s_mov_b32 s65, 2097152\n"
"buffer_load_dwordx4 v6, s[24:27], s65 offen  lds\n"
"buffer_load_dwordx4 v7, s[24:27], s65 offen offset:1024  lds\n"
"buffer_load_dwordx4 v8, s[24:27], s65 offen offset:2048  lds\n"
"buffer_load_dwordx4 v9, s[24:27], s65 offen offset:3072  lds\n"
"s_waitcnt vmcnt(12)\n"
"s_barrier\n"
"s_mov_b32 m0, 53248\n"
"s_mov_b32 s65, 197248\n"
"buffer_load_dwordx4 v2, s[20:23], s65 offen  lds\n"
"buffer_load_dwordx4 v3, s[20:23], s65 offen offset:1024  lds\n"
"buffer_load_dwordx4 v4, s[20:23], s65 offen offset:2048  lds\n"
"buffer_load_dwordx4 v5, s[20:23], s65 offen offset:3072  lds\n"
"s_mov_b32 m0, 57344\n"
"s_mov_b32 s65, 262784\n"
"buffer_load_dwordx4 v2, s[20:23], s65 offen  lds\n"
"buffer_load_dwordx4 v3, s[20:23], s65 offen offset:1024  lds\n"
"buffer_load_dwordx4 v4, s[20:23], s65 offen offset:2048  lds\n"
"buffer_load_dwordx4 v5, s[20:23], s65 offen offset:3072  lds\n"
"s_mov_b32 m0, 94208\n"
"s_mov_b32 s65, 2621440\n"
"buffer_load_dwordx4 v6, s[24:27], s65 offen  lds\n"
"buffer_load_dwordx4 v7, s[24:27], s65 offen offset:1024  lds\n"
"buffer_load_dwordx4 v8, s[24:27], s65 offen offset:2048  lds\n"
"buffer_load_dwordx4 v9, s[24:27], s65 offen offset:3072  lds\n"
"s_waitcnt vmcnt(12)\n"
"s_barrier\n"
"s_mov_b32 m0, 12288\n"
"s_mov_b32 s65, 197376\n"
"buffer_load_dwordx4 v2, s[20:23], s65 offen  lds\n"
"buffer_load_dwordx4 v3, s[20:23], s65 offen offset:1024  lds\n"
"buffer_load_dwordx4 v4, s[20:23], s65 offen offset:2048  lds\n"
"buffer_load_dwordx4 v5, s[20:23], s65 offen offset:3072  lds\n"
"s_mov_b32 m0, 16384\n"
"s_mov_b32 s65, 262912\n"
"buffer_load_dwordx4 v2, s[20:23], s65 offen  lds\n"
"buffer_load_dwordx4 v3, s[20:23], s65 offen offset:1024  lds\n"
"buffer_load_dwordx4 v4, s[20:23], s65 offen offset:2048  lds\n"
"buffer_load_dwordx4 v5, s[20:23], s65 offen offset:3072  lds\n"
"s_mov_b32 m0, 61440\n"
"s_mov_b32 s65, 3145728\n"
"buffer_load_dwordx4 v6, s[24:27], s65 offen  lds\n"
"buffer_load_dwordx4 v7, s[24:27], s65 offen offset:1024  lds\n"
"buffer_load_dwordx4 v8, s[24:27], s65 offen offset:2048  lds\n"
"buffer_load_dwordx4 v9, s[24:27], s65 offen offset:3072  lds\n"
"s_waitcnt vmcnt(12)\n"
"s_barrier\n"
"s_mov_b32 m0, 32768\n"
"s_mov_b32 s65, 197504\n"
"buffer_load_dwordx4 v2, s[20:23], s65 offen  lds\n"
"buffer_load_dwordx4 v3, s[20:23], s65 offen offset:1024  lds\n"
"buffer_load_dwordx4 v4, s[20:23], s65 offen offset:2048  lds\n"
"buffer_load_dwordx4 v5, s[20:23], s65 offen offset:3072  lds\n"
"s_mov_b32 m0, 36864\n"
"s_mov_b32 s65, 263040\n"
"buffer_load_dwordx4 v2, s[20:23], s65 offen  lds\n"
"buffer_load_dwordx4 v3, s[20:23], s65 offen offset:1024  lds\n"
"buffer_load_dwordx4 v4, s[20:23], s65 offen offset:2048  lds\n"
"buffer_load_dwordx4 v5, s[20:23], s65 offen offset:3072  lds\n"
"s_mov_b32 m0, 77824\n"
"s_mov_b32 s65, 3670016\n"
"buffer_load_dwordx4 v6, s[24:27], s65 offen  lds\n"
"buffer_load_dwordx4 v7, s[24:27], s65 offen offset:1024  lds\n"
"buffer_load_dwordx4 v8, s[24:27], s65 offen offset:2048  lds\n"
"buffer_load_dwordx4 v9, s[24:27], s65 offen offset:3072  lds\n"
"s_waitcnt vmcnt(12)\n"
"s_barrier\n"
"s_mov_b32 m0, 53248\n"
"s_mov_b32 s65, 197632\n"
"buffer_load_dwordx4 v2, s[20:23], s65 offen  lds\n"
"buffer_load_dwordx4 v3, s[20:23], s65 offen offset:1024  lds\n"
"buffer_load_dwordx4 v4, s[20:23], s65 offen offset:2048  lds\n"
"buffer_load_dwordx4 v5, s[20:23], s65 offen offset:3072  lds\n"
"s_mov_b32 m0, 57344\n"
"s_mov_b32 s65, 263168\n"
"buffer_load_dwordx4 v2, s[20:23], s65 offen  lds\n"
"buffer_load_dwordx4 v3, s[20:23], s65 offen offset:1024  lds\n"
"buffer_load_dwordx4 v4, s[20:23], s65 offen offset:2048  lds\n"
"buffer_load_dwordx4 v5, s[20:23], s65 offen offset:3072  lds\n"
"s_mov_b32 m0, 94208\n"
"s_mov_b32 s65, 4194304\n"
"buffer_load_dwordx4 v6, s[24:27], s65 offen  lds\n"
"buffer_load_dwordx4 v7, s[24:27], s65 offen offset:1024  lds\n"
"buffer_load_dwordx4 v8, s[24:27], s65 offen offset:2048  lds\n"
"buffer_load_dwordx4 v9, s[24:27], s65 offen offset:3072  lds\n"
"s_waitcnt vmcnt(12)\n"
"s_barrier\n"
"s_mov_b32 m0, 12288\n"
"s_mov_b32 s65, 197760\n"
"buffer_load_dwordx4 v2, s[20:23], s65 offen  lds\n"
"buffer_load_dwordx4 v3, s[20:23], s65 offen offset:1024  lds\n"
"buffer_load_dwordx4 v4, s[20:23], s65 offen offset:2048  lds\n"
"buffer_load_dwordx4 v5, s[20:23], s65 offen offset:3072  lds\n"
"s_mov_b32 m0, 16384\n"
"s_mov_b32 s65, 263296\n"
"buffer_load_dwordx4 v2, s[20:23], s65 offen  lds\n"
"buffer_load_dwordx4 v3, s[20:23], s65 offen offset:1024  lds\n"
"buffer_load_dwordx4 v4, s[20:23], s65 offen offset:2048  lds\n"
"buffer_load_dwordx4 v5, s[20:23], s65 offen offset:3072  lds\n"
"s_mov_b32 m0, 61440\n"
"s_mov_b32 s65, 4718592\n"
"buffer_load_dwordx4 v6, s[24:27], s65 offen  lds\n"
"buffer_load_dwordx4 v7, s[24:27], s65 offen offset:1024  lds\n"
"buffer_load_dwordx4 v8, s[24:27], s65 offen offset:2048  lds\n"
"buffer_load_dwordx4 v9, s[24:27], s65 offen offset:3072  lds\n"
"s_waitcnt vmcnt(12)\n"
"s_barrier\n"
"s_mov_b32 m0, 32768\n"
"s_mov_b32 s65, 197888\n"
"buffer_load_dwordx4 v2, s[20:23], s65 offen  lds\n"
"buffer_load_dwordx4 v3, s[20:23], s65 offen offset:1024  lds\n"
"buffer_load_dwordx4 v4, s[20:23], s65 offen offset:2048  lds\n"
"buffer_load_dwordx4 v5, s[20:23], s65 offen offset:3072  lds\n"
"s_mov_b32 m0, 36864\n"
"s_mov_b32 s65, 263424\n"
"buffer_load_dwordx4 v2, s[20:23], s65 offen  lds\n"
"buffer_load_dwordx4 v3, s[20:23], s65 offen offset:1024  lds\n"
"buffer_load_dwordx4 v4, s[20:23], s65 offen offset:2048  lds\n"
"buffer_load_dwordx4 v5, s[20:23], s65 offen offset:3072  lds\n"
"s_mov_b32 m0, 77824\n"
"s_mov_b32 s65, 5242880\n"
"buffer_load_dwordx4 v6, s[24:27], s65 offen  lds\n"
"buffer_load_dwordx4 v7, s[24:27], s65 offen offset:1024  lds\n"
"buffer_load_dwordx4 v8, s[24:27], s65 offen offset:2048  lds\n"
"buffer_load_dwordx4 v9, s[24:27], s65 offen offset:3072  lds\n"
"s_waitcnt vmcnt(12)\n"
"s_barrier\n"
"s_mov_b32 m0, 53248\n"
"s_mov_b32 s65, 198016\n"
"buffer_load_dwordx4 v2, s[20:23], s65 offen  lds\n"
"buffer_load_dwordx4 v3, s[20:23], s65 offen offset:1024  lds\n"
"buffer_load_dwordx4 v4, s[20:23], s65 offen offset:2048  lds\n"
"buffer_load_dwordx4 v5, s[20:23], s65 offen offset:3072  lds\n"
"s_mov_b32 m0, 57344\n"
"s_mov_b32 s65, 263552\n"
"buffer_load_dwordx4 v2, s[20:23], s65 offen  lds\n"
"buffer_load_dwordx4 v3, s[20:23], s65 offen offset:1024  lds\n"
"buffer_load_dwordx4 v4, s[20:23], s65 offen offset:2048  lds\n"
"buffer_load_dwordx4 v5, s[20:23], s65 offen offset:3072  lds\n"
"s_mov_b32 m0, 94208\n"
"s_mov_b32 s65, 5767168\n"
"buffer_load_dwordx4 v6, s[24:27], s65 offen  lds\n"
"buffer_load_dwordx4 v7, s[24:27], s65 offen offset:1024  lds\n"
"buffer_load_dwordx4 v8, s[24:27], s65 offen offset:2048  lds\n"
"buffer_load_dwordx4 v9, s[24:27], s65 offen offset:3072  lds\n"
"s_waitcnt vmcnt(12)\n"
"s_barrier\n"
"s_mov_b32 m0, 12288\n"
"s_mov_b32 s65, 198144\n"
"buffer_load_dwordx4 v2, s[20:23], s65 offen  lds\n"
"buffer_load_dwordx4 v3, s[20:23], s65 offen offset:1024  lds\n"
"buffer_load_dwordx4 v4, s[20:23], s65 offen offset:2048  lds\n"
"buffer_load_dwordx4 v5, s[20:23], s65 offen offset:3072  lds\n"
"s_mov_b32 m0, 16384\n"
"s_mov_b32 s65, 263680\n"
"buffer_load_dwordx4 v2, s[20:23], s65 offen  lds\n"
"buffer_load_dwordx4 v3, s[20:23], s65 offen offset:1024  lds\n"
"buffer_load_dwordx4 v4, s[20:23], s65 offen offset:2048  lds\n"
"buffer_load_dwordx4 v5, s[20:23], s65 offen offset:3072  lds\n"
"s_mov_b32 m0, 61440\n"
"s_mov_b32 s65, 6291456\n"
"buffer_load_dwordx4 v6, s[24:27], s65 offen  lds\n"
"buffer_load_dwordx4 v7, s[24:27], s65 offen offset:1024  lds\n"
"buffer_load_dwordx4 v8, s[24:27], s65 offen offset:2048  lds\n"
"buffer_load_dwordx4 v9, s[24:27], s65 offen offset:3072  lds\n"
"s_waitcnt vmcnt(12)\n"
"s_barrier\n"
"s_mov_b32 m0, 32768\n"
"s_mov_b32 s65, 198272\n"
"buffer_load_dwordx4 v2, s[20:23], s65 offen  lds\n"
"buffer_load_dwordx4 v3, s[20:23], s65 offen offset:1024  lds\n"
"buffer_load_dwordx4 v4, s[20:23], s65 offen offset:2048  lds\n"
"buffer_load_dwordx4 v5, s[20:23], s65 offen offset:3072  lds\n"
"s_mov_b32 m0, 36864\n"
"s_mov_b32 s65, 263808\n"
"buffer_load_dwordx4 v2, s[20:23], s65 offen  lds\n"
"buffer_load_dwordx4 v3, s[20:23], s65 offen offset:1024  lds\n"
"buffer_load_dwordx4 v4, s[20:23], s65 offen offset:2048  lds\n"
"buffer_load_dwordx4 v5, s[20:23], s65 offen offset:3072  lds\n"
"s_mov_b32 m0, 77824\n"
"s_mov_b32 s65, 6815744\n"
"buffer_load_dwordx4 v6, s[24:27], s65 offen  lds\n"
"buffer_load_dwordx4 v7, s[24:27], s65 offen offset:1024  lds\n"
"buffer_load_dwordx4 v8, s[24:27], s65 offen offset:2048  lds\n"
"buffer_load_dwordx4 v9, s[24:27], s65 offen offset:3072  lds\n"
"s_waitcnt vmcnt(12)\n"
"s_barrier\n"
"s_mov_b32 m0, 53248\n"
"s_mov_b32 s65, 198400\n"
"buffer_load_dwordx4 v2, s[20:23], s65 offen  lds\n"
"buffer_load_dwordx4 v3, s[20:23], s65 offen offset:1024  lds\n"
"buffer_load_dwordx4 v4, s[20:23], s65 offen offset:2048  lds\n"
"buffer_load_dwordx4 v5, s[20:23], s65 offen offset:3072  lds\n"
"s_mov_b32 m0, 57344\n"
"s_mov_b32 s65, 263936\n"
"buffer_load_dwordx4 v2, s[20:23], s65 offen  lds\n"
"buffer_load_dwordx4 v3, s[20:23], s65 offen offset:1024  lds\n"
"buffer_load_dwordx4 v4, s[20:23], s65 offen offset:2048  lds\n"
"buffer_load_dwordx4 v5, s[20:23], s65 offen offset:3072  lds\n"
"s_mov_b32 m0, 94208\n"
"s_mov_b32 s65, 7340032\n"
"buffer_load_dwordx4 v6, s[24:27], s65 offen  lds\n"
"buffer_load_dwordx4 v7, s[24:27], s65 offen offset:1024  lds\n"
"buffer_load_dwordx4 v8, s[24:27], s65 offen offset:2048  lds\n"
"buffer_load_dwordx4 v9, s[24:27], s65 offen offset:3072  lds\n"
"s_waitcnt vmcnt(12)\n"
"s_barrier\n"
"s_mov_b32 m0, 12288\n"
"s_mov_b32 s65, 198528\n"
"buffer_load_dwordx4 v2, s[20:23], s65 offen  lds\n"
"buffer_load_dwordx4 v3, s[20:23], s65 offen offset:1024  lds\n"
"buffer_load_dwordx4 v4, s[20:23], s65 offen offset:2048  lds\n"
"buffer_load_dwordx4 v5, s[20:23], s65 offen offset:3072  lds\n"
"s_mov_b32 m0, 16384\n"
"s_mov_b32 s65, 264064\n"
"buffer_load_dwordx4 v2, s[20:23], s65 offen  lds\n"
"buffer_load_dwordx4 v3, s[20:23], s65 offen offset:1024  lds\n"
"buffer_load_dwordx4 v4, s[20:23], s65 offen offset:2048  lds\n"
"buffer_load_dwordx4 v5, s[20:23], s65 offen offset:3072  lds\n"
"s_mov_b32 m0, 61440\n"
"s_mov_b32 s65, 7864320\n"
"buffer_load_dwordx4 v6, s[24:27], s65 offen  lds\n"
"buffer_load_dwordx4 v7, s[24:27], s65 offen offset:1024  lds\n"
"buffer_load_dwordx4 v8, s[24:27], s65 offen offset:2048  lds\n"
"buffer_load_dwordx4 v9, s[24:27], s65 offen offset:3072  lds\n"
"s_waitcnt vmcnt(12)\n"
"s_barrier\n"
"s_add_u32 m0, s79, 77824\n"
"s_add_u32 s65, s79, 0\n"
"buffer_load_dwordx4 v14, s[32:35], s65 offen offset:0 nt lds\n"
"buffer_load_dwordx4 v15, s[32:35], s65 offen offset:1024 nt lds\n"
"buffer_load_dwordx4 v16, s[32:35], s65 offen offset:2048 nt lds\n"
"buffer_load_dwordx4 v17, s[32:35], s65 offen offset:3072 nt lds\n"
"s_waitcnt vmcnt(4)\n"
"s_barrier\n"
"s_add_u32 m0, s79, 94208\n"
"s_add_u32 s65, s79, 49152\n"
"buffer_load_dwordx4 v10, s[28:31], s65 offen offset:0 nt lds\n"
"buffer_load_dwordx4 v11, s[28:31], s65 offen offset:1024 nt lds\n"
"buffer_load_dwordx4 v12, s[28:31], s65 offen offset:2048 nt lds\n"
"buffer_load_dwordx4 v13, s[28:31], s65 offen offset:3072 nt lds\n"
"s_barrier\n"
"s_add_u32 m0, s79, 61440\n"
"s_add_u32 s65, s79, 16384\n"
"buffer_load_dwordx4 v14, s[32:35], s65 offen offset:0 nt lds\n"
"buffer_load_dwordx4 v15, s[32:35], s65 offen offset:1024 nt lds\n"
"buffer_load_dwordx4 v16, s[32:35], s65 offen offset:2048 nt lds\n"
"buffer_load_dwordx4 v17, s[32:35], s65 offen offset:3072 nt lds\n"
"s_branch .Lk_M_10_%=\n"
".Lk_b_13_%=:\n"
"s_waitcnt vmcnt(5)\n"
"s_barrier\n"
"s_mov_b32 m0, 98304\n"
"s_mov_b32 s65, 1179648\n"
"buffer_load_dwordx4 v6, s[24:27], s65 offen  lds\n"
"buffer_load_dwordx4 v7, s[24:27], s65 offen offset:1024  lds\n"
"buffer_load_dwordx4 v8, s[24:27], s65 offen offset:2048  lds\n"
"buffer_load_dwordx4 v9, s[24:27], s65 offen offset:3072  lds\n"
"s_mov_b32 m0, 102400\n"
"s_mov_b32 s65, 1310720\n"
"buffer_load_dwordx4 v6, s[24:27], s65 offen  lds\n"
"buffer_load_dwordx4 v7, s[24:27], s65 offen offset:1024  lds\n"
"buffer_load_dwordx4 v8, s[24:27], s65 offen offset:2048  lds\n"
"buffer_load_dwordx4 v9, s[24:27], s65 offen offset:3072  lds\n"
"s_mov_b32 m0, 106496\n"
"s_mov_b32 s65, 1441792\n"
"buffer_load_dwordx4 v6, s[24:27], s65 offen  lds\n"
"buffer_load_dwordx4 v7, s[24:27], s65 offen offset:1024  lds\n"
"buffer_load_dwordx4 v8, s[24:27], s65 offen offset:2048  lds\n"
"buffer_load_dwordx4 v9, s[24:27], s65 offen offset:3072  lds\n"
"s_waitcnt vmcnt(12)\n"
"s_barrier\n"
"s_mov_b32 m0, 65536\n"
"s_mov_b32 s65, 1703936\n"
"buffer_load_dwordx4 v6, s[24:27], s65 offen  lds\n"
"buffer_load_dwordx4 v7, s[24:27], s65 offen offset:1024  lds\n"
"buffer_load_dwordx4 v8, s[24:27], s65 offen offset:2048  lds\n"
"buffer_load_dwordx4 v9, s[24:27], s65 offen offset:3072  lds\n"
"s_mov_b32 m0, 69632\n"
"s_mov_b32 s65, 1835008\n"
"buffer_load_dwordx4 v6, s[24:27], s65 offen  lds\n"
"buffer_load_dwordx4 v7, s[24:27], s65 offen offset:1024  lds\n"
"buffer_load_dwordx4 v8, s[24:27], s65 offen offset:2048  lds\n"
"buffer_load_dwordx4 v9, s[24:27], s65 offen offset:3072  lds\n"
"s_mov_b32 m0, 73728\n"
"s_mov_b32 s65, 1966080\n"
"buffer_load_dwordx4 v6, s[24:27], s65 offen  lds\n"
"buffer_load_dwordx4 v7, s[24:27], s65 offen offset:1024  lds\n"
"buffer_load_dwordx4 v8, s[24:27], s65 offen offset:2048  lds\n"
"buffer_load_dwordx4 v9, s[24:27], s65 offen offset:3072  lds\n"
"s_waitcnt vmcnt(12)\n"
"s_barrier\n"
"s_mov_b32 m0, 81920\n"
"s_mov_b32 s65, 2228224\n"
"buffer_load_dwordx4 v6, s[24:27], s65 offen  lds\n"
"buffer_load_dwordx4 v7, s[24:27], s65 offen offset:1024  lds\n"
"buffer_load_dwordx4 v8, s[24:27], s65 offen offset:2048  lds\n"
"buffer_load_dwordx4 v9, s[24:27], s65 offen offset:3072  lds\n"
"s_mov_b32 m0, 86016\n"
"s_mov_b32 s65, 2359296\n"
"buffer_load_dwordx4 v6, s[24:27], s65 offen  lds\n"
"buffer_load_dwordx4 v7, s[24:27], s65 offen offset:1024  lds\n"
"buffer_load_dwordx4 v8, s[24:27], s65 offen offset:2048  lds\n"
"buffer_load_dwordx4 v9, s[24:27], s65 offen offset:3072  lds\n"
"s_mov_b32 m0, 90112\n"
"s_mov_b32 s65, 2490368\n"
"buffer_load_dwordx4 v6, s[24:27], s65 offen  lds\n"
"buffer_load_dwordx4 v7, s[24:27], s65 offen offset:1024  lds\n"
"buffer_load_dwordx4 v8, s[24:27], s65 offen offset:2048  lds\n"
"buffer_load_dwordx4 v9, s[24:27], s65 offen offset:3072  lds\n"
"s_waitcnt vmcnt(12)\n"
"s_barrier\n"
"s_mov_b32 m0, 98304\n"
"s_mov_b32 s65, 2752512\n"
"buffer_load_dwordx4 v6, s[24:27], s65 offen  lds\n"
"buffer_load_dwordx4 v7, s[24:27], s65 offen offset:1024  lds\n"
"buffer_load_dwordx4 v8, s[24:27], s65 offen offset:2048  lds\n"
"buffer_load_dwordx4 v9, s[24:27], s65 offen offset:3072  lds\n"
"s_mov_b32 m0, 102400\n"
"s_mov_b32 s65, 2883584\n"
"buffer_load_dwordx4 v6, s[24:27], s65 offen  lds\n"
"buffer_load_dwordx4 v7, s[24:27], s65 offen offset:1024  lds\n"
"buffer_load_dwordx4 v8, s[24:27], s65 offen offset:2048  lds\n"
"buffer_load_dwordx4 v9, s[24:27], s65 offen offset:3072  lds\n"
"s_mov_b32 m0, 106496\n"
"s_mov_b32 s65, 3014656\n"
"buffer_load_dwordx4 v6, s[24:27], s65 offen  lds\n"
"buffer_load_dwordx4 v7, s[24:27], s65 offen offset:1024  lds\n"
"buffer_load_dwordx4 v8, s[24:27], s65 offen offset:2048  lds\n"
"buffer_load_dwordx4 v9, s[24:27], s65 offen offset:3072  lds\n"
"s_waitcnt vmcnt(12)\n"
"s_barrier\n"
"s_mov_b32 m0, 65536\n"
"s_mov_b32 s65, 3276800\n"
"buffer_load_dwordx4 v6, s[24:27], s65 offen  lds\n"
"buffer_load_dwordx4 v7, s[24:27], s65 offen offset:1024  lds\n"
"buffer_load_dwordx4 v8, s[24:27], s65 offen offset:2048  lds\n"
"buffer_load_dwordx4 v9, s[24:27], s65 offen offset:3072  lds\n"
"s_mov_b32 m0, 69632\n"
"s_mov_b32 s65, 3407872\n"
"buffer_load_dwordx4 v6, s[24:27], s65 offen  lds\n"
"buffer_load_dwordx4 v7, s[24:27], s65 offen offset:1024  lds\n"
"buffer_load_dwordx4 v8, s[24:27], s65 offen offset:2048  lds\n"
"buffer_load_dwordx4 v9, s[24:27], s65 offen offset:3072  lds\n"
"s_mov_b32 m0, 73728\n"
"s_mov_b32 s65, 3538944\n"
"buffer_load_dwordx4 v6, s[24:27], s65 offen  lds\n"
"buffer_load_dwordx4 v7, s[24:27], s65 offen offset:1024  lds\n"
"buffer_load_dwordx4 v8, s[24:27], s65 offen offset:2048  lds\n"
"buffer_load_dwordx4 v9, s[24:27], s65 offen offset:3072  lds\n"
"s_waitcnt vmcnt(12)\n"
"s_barrier\n"
"s_mov_b32 m0, 81920\n"
"s_mov_b32 s65, 3801088\n"
"buffer_load_dwordx4 v6, s[24:27], s65 offen  lds\n"
"buffer_load_dwordx4 v7, s[24:27], s65 offen offset:1024  lds\n"
"buffer_load_dwordx4 v8, s[24:27], s65 offen offset:2048  lds\n"
"buffer_load_dwordx4 v9, s[24:27], s65 offen offset:3072  lds\n"
"s_mov_b32 m0, 86016\n"
"s_mov_b32 s65, 3932160\n"
"buffer_load_dwordx4 v6, s[24:27], s65 offen  lds\n"
"buffer_load_dwordx4 v7, s[24:27], s65 offen offset:1024  lds\n"
"buffer_load_dwordx4 v8, s[24:27], s65 offen offset:2048  lds\n"
"buffer_load_dwordx4 v9, s[24:27], s65 offen offset:3072  lds\n"
"s_mov_b32 m0, 90112\n"
"s_mov_b32 s65, 4063232\n"
"buffer_load_dwordx4 v6, s[24:27], s65 offen  lds\n"
"buffer_load_dwordx4 v7, s[24:27], s65 offen offset:1024  lds\n"
"buffer_load_dwordx4 v8, s[24:27], s65 offen offset:2048  lds\n"
"buffer_load_dwordx4 v9, s[24:27], s65 offen offset:3072  lds\n"
"s_waitcnt vmcnt(12)\n"
"s_barrier\n"
"s_mov_b32 m0, 98304\n"
"s_mov_b32 s65, 4325376\n"
"buffer_load_dwordx4 v6, s[24:27], s65 offen  lds\n"
"buffer_load_dwordx4 v7, s[24:27], s65 offen offset:1024  lds\n"
"buffer_load_dwordx4 v8, s[24:27], s65 offen offset:2048  lds\n"
"buffer_load_dwordx4 v9, s[24:27], s65 offen offset:3072  lds\n"
"s_mov_b32 m0, 102400\n"
"s_mov_b32 s65, 4456448\n"
"buffer_load_dwordx4 v6, s[24:27], s65 offen  lds\n"
"buffer_load_dwordx4 v7, s[24:27], s65 offen offset:1024  lds\n"
"buffer_load_dwordx4 v8, s[24:27], s65 offen offset:2048  lds\n"
"buffer_load_dwordx4 v9, s[24:27], s65 offen offset:3072  lds\n"
"s_mov_b32 m0, 106496\n"
"s_mov_b32 s65, 4587520\n"
"buffer_load_dwordx4 v6, s[24:27], s65 offen  lds\n"
"buffer_load_dwordx4 v7, s[24:27], s65 offen offset:1024  lds\n"
"buffer_load_dwordx4 v8, s[24:27], s65 offen offset:2048  lds\n"
"buffer_load_dwordx4 v9, s[24:27], s65 offen offset:3072  lds\n"
"s_waitcnt vmcnt(12)\n"
"s_barrier\n"
"s_mov_b32 m0, 65536\n"
"s_mov_b32 s65, 4849664\n"
"buffer_load_dwordx4 v6, s[24:27], s65 offen  lds\n"
"buffer_load_dwordx4 v7, s[24:27], s65 offen offset:1024  lds\n"
"buffer_load_dwordx4 v8, s[24:27], s65 offen offset:2048  lds\n"
"buffer_load_dwordx4 v9, s[24:27], s65 offen offset:3072  lds\n"
"s_mov_b32 m0, 69632\n"
"s_mov_b32 s65, 4980736\n"
"buffer_load_dwordx4 v6, s[24:27], s65 offen  lds\n"
"buffer_load_dwordx4 v7, s[24:27], s65 offen offset:1024  lds\n"
"buffer_load_dwordx4 v8, s[24:27], s65 offen offset:2048  lds\n"
"buffer_load_dwordx4 v9, s[24:27], s65 offen offset:3072  lds\n"
"s_mov_b32 m0, 73728\n"
"s_mov_b32 s65, 5111808\n"
"buffer_load_dwordx4 v6, s[24:27], s65 offen  lds\n"
"buffer_load_dwordx4 v7, s[24:27], s65 offen offset:1024  lds\n"
"buffer_load_dwordx4 v8, s[24:27], s65 offen offset:2048  lds\n"
"buffer_load_dwordx4 v9, s[24:27], s65 offen offset:3072  lds\n"
"s_waitcnt vmcnt(12)\n"
"s_barrier\n"
"s_mov_b32 m0, 81920\n"
"s_mov_b32 s65, 5373952\n"
"buffer_load_dwordx4 v6, s[24:27], s65 offen  lds\n"
"buffer_load_dwordx4 v7, s[24:27], s65 offen offset:1024  lds\n"
"buffer_load_dwordx4 v8, s[24:27], s65 offen offset:2048  lds\n"
"buffer_load_dwordx4 v9, s[24:27], s65 offen offset:3072  lds\n"
"s_mov_b32 m0, 86016\n"
"s_mov_b32 s65, 5505024\n"
"buffer_load_dwordx4 v6, s[24:27], s65 offen  lds\n"
"buffer_load_dwordx4 v7, s[24:27], s65 offen offset:1024  lds\n"
"buffer_load_dwordx4 v8, s[24:27], s65 offen offset:2048  lds\n"
"buffer_load_dwordx4 v9, s[24:27], s65 offen offset:3072  lds\n"
"s_mov_b32 m0, 90112\n"
"s_mov_b32 s65, 5636096\n"
"buffer_load_dwordx4 v6, s[24:27], s65 offen  lds\n"
"buffer_load_dwordx4 v7, s[24:27], s65 offen offset:1024  lds\n"
"buffer_load_dwordx4 v8, s[24:27], s65 offen offset:2048  lds\n"
"buffer_load_dwordx4 v9, s[24:27], s65 offen offset:3072  lds\n"
"s_waitcnt vmcnt(12)\n"
"s_barrier\n"
"s_mov_b32 m0, 98304\n"
"s_mov_b32 s65, 5898240\n"
"buffer_load_dwordx4 v6, s[24:27], s65 offen  lds\n"
"buffer_load_dwordx4 v7, s[24:27], s65 offen offset:1024  lds\n"
"buffer_load_dwordx4 v8, s[24:27], s65 offen offset:2048  lds\n"
"buffer_load_dwordx4 v9, s[24:27], s65 offen offset:3072  lds\n"
"s_mov_b32 m0, 102400\n"
"s_mov_b32 s65, 6029312\n"
"buffer_load_dwordx4 v6, s[24:27], s65 offen  lds\n"
"buffer_load_dwordx4 v7, s[24:27], s65 offen offset:1024  lds\n"
"buffer_load_dwordx4 v8, s[24:27], s65 offen offset:2048  lds\n"
"buffer_load_dwordx4 v9, s[24:27], s65 offen offset:3072  lds\n"
"s_mov_b32 m0, 106496\n"
"s_mov_b32 s65, 6160384\n"
"buffer_load_dwordx4 v6, s[24:27], s65 offen  lds\n"
"buffer_load_dwordx4 v7, s[24:27], s65 offen offset:1024  lds\n"
"buffer_load_dwordx4 v8, s[24:27], s65 offen offset:2048  lds\n"
"buffer_load_dwordx4 v9, s[24:27], s65 offen offset:3072  lds\n"
"s_waitcnt vmcnt(12)\n"
"s_barrier\n"
"s_mov_b32 m0, 65536\n"
"s_mov_b32 s65, 6422528\n"
"buffer_load_dwordx4 v6, s[24:27], s65 offen  lds\n"
"buffer_load_dwordx4 v7, s[24:27], s65 offen offset:1024  lds\n"
"buffer_load_dwordx4 v8, s[24:27], s65 offen offset:2048  lds\n"
"buffer_load_dwordx4 v9, s[24:27], s65 offen offset:3072  lds\n"
"s_mov_b32 m0, 69632\n"
"s_mov_b32 s65, 6553600\n"
"buffer_load_dwordx4 v6, s[24:27], s65 offen  lds\n"
"buffer_load_dwordx4 v7, s[24:27], s65 offen offset:1024  lds\n"
"buffer_load_dwordx4 v8, s[24:27], s65 offen offset:2048  lds\n"
"buffer_load_dwordx4 v9, s[24:27], s65 offen offset:3072  lds\n"
"s_mov_b32 m0, 73728\n"
"s_mov_b32 s65, 6684672\n"
"buffer_load_dwordx4 v6, s[24:27], s65 offen  lds\n"
"buffer_load_dwordx4 v7, s[24:27], s65 offen offset:1024  lds\n"
"buffer_load_dwordx4 v8, s[24:27], s65 offen offset:2048  lds\n"
"buffer_load_dwordx4 v9, s[24:27], s65 offen offset:3072  lds\n"
"s_waitcnt vmcnt(12)\n"
"s_barrier\n"
"s_mov_b32 m0, 81920\n"
"s_mov_b32 s65, 6946816\n"
"buffer_load_dwordx4 v6, s[24:27], s65 offen  lds\n"
"buffer_load_dwordx4 v7, s[24:27], s65 offen offset:1024  lds\n"
"buffer_load_dwordx4 v8, s[24:27], s65 offen offset:2048  lds\n"
"buffer_load_dwordx4 v9, s[24:27], s65 offen offset:3072  lds\n"
"s_mov_b32 m0, 86016\n"
"s_mov_b32 s65, 7077888\n"
"buffer_load_dwordx4 v6, s[24:27], s65 offen  lds\n"
"buffer_load_dwordx4 v7, s[24:27], s65 offen offset:1024  lds\n"
"buffer_load_dwordx4 v8, s[24:27], s65 offen offset:2048  lds\n"
"buffer_load_dwordx4 v9, s[24:27], s65 offen offset:3072  lds\n"
"s_mov_b32 m0, 90112\n"
"s_mov_b32 s65, 7208960\n"
"buffer_load_dwordx4 v6, s[24:27], s65 offen  lds\n"
"buffer_load_dwordx4 v7, s[24:27], s65 offen offset:1024  lds\n"
"buffer_load_dwordx4 v8, s[24:27], s65 offen offset:2048  lds\n"
"buffer_load_dwordx4 v9, s[24:27], s65 offen offset:3072  lds\n"
"s_waitcnt vmcnt(12)\n"
"s_barrier\n"
"s_mov_b32 m0, 98304\n"
"s_mov_b32 s65, 7471104\n"
"buffer_load_dwordx4 v6, s[24:27], s65 offen  lds\n"
"buffer_load_dwordx4 v7, s[24:27], s65 offen offset:1024  lds\n"
"buffer_load_dwordx4 v8, s[24:27], s65 offen offset:2048  lds\n"
"buffer_load_dwordx4 v9, s[24:27], s65 offen offset:3072  lds\n"
"s_mov_b32 m0, 102400\n"
"s_mov_b32 s65, 7602176\n"
"buffer_load_dwordx4 v6, s[24:27], s65 offen  lds\n"
"buffer_load_dwordx4 v7, s[24:27], s65 offen offset:1024  lds\n"
"buffer_load_dwordx4 v8, s[24:27], s65 offen offset:2048  lds\n"
"buffer_load_dwordx4 v9, s[24:27], s65 offen offset:3072  lds\n"
"s_mov_b32 m0, 106496\n"
"s_mov_b32 s65, 7733248\n"
"buffer_load_dwordx4 v6, s[24:27], s65 offen  lds\n"
"buffer_load_dwordx4 v7, s[24:27], s65 offen offset:1024  lds\n"
"buffer_load_dwordx4 v8, s[24:27], s65 offen offset:2048  lds\n"
"buffer_load_dwordx4 v9, s[24:27], s65 offen offset:3072  lds\n"
"s_waitcnt vmcnt(12)\n"
"s_barrier\n"
"s_mov_b32 m0, 65536\n"
"s_mov_b32 s65, 7995392\n"
"buffer_load_dwordx4 v6, s[24:27], s65 offen  lds\n"
"buffer_load_dwordx4 v7, s[24:27], s65 offen offset:1024  lds\n"
"buffer_load_dwordx4 v8, s[24:27], s65 offen offset:2048  lds\n"
"buffer_load_dwordx4 v9, s[24:27], s65 offen offset:3072  lds\n"
"s_mov_b32 m0, 69632\n"
"s_mov_b32 s65, 8126464\n"
"buffer_load_dwordx4 v6, s[24:27], s65 offen  lds\n"
"buffer_load_dwordx4 v7, s[24:27], s65 offen offset:1024  lds\n"
"buffer_load_dwordx4 v8, s[24:27], s65 offen offset:2048  lds\n"
"buffer_load_dwordx4 v9, s[24:27], s65 offen offset:3072  lds\n"
"s_mov_b32 m0, 73728\n"
"s_mov_b32 s65, 8257536\n"
"buffer_load_dwordx4 v6, s[24:27], s65 offen  lds\n"
"buffer_load_dwordx4 v7, s[24:27], s65 offen offset:1024  lds\n"
"buffer_load_dwordx4 v8, s[24:27], s65 offen offset:2048  lds\n"
"buffer_load_dwordx4 v9, s[24:27], s65 offen offset:3072  lds\n"
"s_waitcnt vmcnt(12)\n"
"s_barrier\n"
"s_add_u32 m0, s79, 77824\n"
"s_add_u32 s65, s79, 0\n"
"buffer_load_dwordx4 v14, s[32:35], s65 offen offset:0 nt lds\n"
"buffer_load_dwordx4 v15, s[32:35], s65 offen offset:1024 nt lds\n"
"buffer_load_dwordx4 v16, s[32:35], s65 offen offset:2048 nt lds\n"
"buffer_load_dwordx4 v17, s[32:35], s65 offen offset:3072 nt lds\n"
"s_waitcnt vmcnt(4)\n"
"s_barrier\n"
"s_add_u32 m0, s79, 94208\n"
"s_add_u32 s65, s79, 49152\n"
"buffer_load_dwordx4 v10, s[28:31], s65 offen offset:0 nt lds\n"
"buffer_load_dwordx4 v11, s[28:31], s65 offen offset:1024 nt lds\n"
"buffer_load_dwordx4 v12, s[28:31], s65 offen offset:2048 nt lds\n"
"buffer_load_dwordx4 v13, s[28:31], s65 offen offset:3072 nt lds\n"
"s_barrier\n"
"s_add_u32 m0, s79, 61440\n"
"s_add_u32 s65, s79, 16384\n"
"buffer_load_dwordx4 v14, s[32:35], s65 offen offset:0 nt lds\n"
"buffer_load_dwordx4 v15, s[32:35], s65 offen offset:1024 nt lds\n"
"buffer_load_dwordx4 v16, s[32:35], s65 offen offset:2048 nt lds\n"
"buffer_load_dwordx4 v17, s[32:35], s65 offen offset:3072 nt lds\n"
"s_branch .Lk_M_10_%=\n"
".Lk_b_14_%=:\n"
"s_and_b32 s63, s59, 1\n"
"s_lshl_b32 s63, s63, 18\n"
"s_mov_b32 s69, s63\n"
"s_xor_b32 s61, s60, 0\n"
"s_lshl_b32 s61, s61, 13\n"
"s_lshl_b32 s62, s57, 11\n"
"s_add_u32 s61, s61, s62\n"
"s_mov_b64 s[72:73], s[10:11]\n"
"s_add_u32 s72, s72, s61\n"
"s_addc_u32 s73, s73, 0\n"
"v_lshrrev_b32 v40, 4, v0\n"
"v_and_b32 v53, 15, v0\n"
"v_lshl_add_u32 v53, v53, 7, s63\n"
"v_lshl_add_u32 v50, v40, 19, v53\n"
"s_lshl_b32 s61, s57, 5\n"
"s_add_u32 s61, s61, s60\n"
"s_mul_i32 s61, s61, 10240\n"
"s_lshr_b32 s62, s55, 2\n"
"s_mul_i32 s62, s62, 1310720\n"
"s_add_u32 s61, s61, s62\n"
"s_mov_b64 s[74:75], s[4:5]\n"
"s_add_u32 s74, s74, s61\n"
"s_addc_u32 s75, s75, 0\n"
"v_lshrrev_b32 v40, 2, v0\n"
"v_and_b32 v41, 3, v0\n"
"v_lshlrev_b32 v41, 7, v41\n"
"v_lshl_add_u32 v51, v40, 11, v41\n"
"s_mov_b64 s[62:63], s[10:11]\n"
"s_lshr_b32 s62, s62, 8\n"
"s_sub_u32 s62, 1, s62\n"
"s_and_b32 s62, s62, 3\n"
"s_lshl_b32 s62, s62, 8\n"
"s_xor_b32 s69, s69, 0x40000\n"
"s_add_u32 s62, s62, s69\n"
"v_lshrrev_b32 v40, 2, v0\n"
"v_and_b32 v41, 2, v0\n"
"v_lshlrev_b32 v41, 9, v41\n"
"v_lshl_add_u32 v40, v40, 19, v41\n"
"v_and_b32 v41, 1, v0\n"
"v_lshl_add_u32 v40, v41, 7, v40\n"
"v_add_u32 v49, s62, v40\n"
"v_mov_b32 v52, 0\n"
"s_mov_b64 exec, 1\n"
"s_mov_b32 s65, 0\n"
"buffer_load_dword v55, v52, s[28:31], s65 offen\n"
"s_mov_b32 s65, 0\n"
"buffer_load_dword v55, v52, s[32:35], s65 offen\n"
"s_mov_b32 s65, 0\n"
"buffer_load_dword v55, v52, s[36:39], s65 offen\n"
"s_mov_b32 s65, 2097152\n"
"buffer_load_dword v55, v52, s[24:27], s65 offen\n"
"s_mov_b32 s65, 4194304\n"
"buffer_load_dword v55, v52, s[24:27], s65 offen\n"
"s_mov_b32 s65, 6291456\n"
"buffer_load_dword v55, v52, s[24:27], s65 offen\n"
"s_mov_b32 s65, 65536\n"
"buffer_load_dword v55, v52, s[28:31], s65 offen\n"
"s_mov_b32 s65, 65536\n"
"buffer_load_dword v55, v52, s[32:35], s65 offen\n"
"s_mov_b32 s65, 2095104\n"
"buffer_load_dword v55, v52, s[36:39], s65 offen\n"
"s_mov_b32 s65, 1048576\n"
"buffer_load_dword v55, v52, s[36:39], s65 offen\n"
"s_mov_b32 s65, 8380416\n"
"buffer_load_dword v55, v52, s[24:27], s65 offen\n"
"s_mov_b64 exec, -1\n"
"s_lshl_b32 s61, s59, 9\n"
"v_min_u32 v40, 48, v0\n"
"v_lshlrev_b32 v40, 2, v40\n"
"v_mov_b32 v41, 256\n"
"v_cmp_eq_u32 vcc, 192, v40\n"
"s_nop 1\n"
"v_cndmask_b32 v40, v40, v41, vcc\n"
"v_add_u32 v40, s61, v40\n"
"global_load_dword v47, v40, s[52:53] sc1\n"
"s_lshl_b32 s61, s59, 9\n"
"v_min_u32 v40, 11, v0\n"
"s_add_u32 s62, s61, 144\n"
"v_lshl_add_u32 v39, v40, 2, s62\n"
"s_and_b32 s61, s59, 31\n"
"v_lshl_or_b32 v41, v40, 5, s61\n"
"s_and_b32 s62, s59, 0xf9\n"
"v_and_b32 v42, 3, v40\n"
"v_lshl_or_b32 v42, v42, 1, s62\n"
"v_cmp_gt_u32 vcc, 8, v40\n"
"s_add_u32 s61, s57, 8\n"
"v_mov_b32 v40, s58\n"
"v_cndmask_b32 v41, v42, v41, vcc\n"
"v_mov_b32 v42, s61\n"
"v_lshlrev_b32 v36, 9, v41\n"
"v_cndmask_b32 v42, v42, v40, vcc\n"
"v_lshl_add_u32 v37, v42, 2, v36\n"
"v_add_u32 v52, 1048576, v53\n"
"s_mov_b64 exec, 0xffff\n"
"global_load_dword v55, v52, s[72:73]\n"
"s_mov_b64 exec, -1\n"
"v_add_u32 v52, 1572864, v53\n"
"s_mov_b64 exec, 0xffff\n"
"global_load_dword v55, v52, s[72:73]\n"
"s_mov_b64 exec, -1\n"
"s_and_b32 s71, s60, 1\n"
"s_add_u32 s71, s71, 3\n"
"s_mov_b32 s68, 0\n"
"s_mov_b32 s69, 0\n"
"s_waitcnt vmcnt(19)\n"
"s_barrier\n"
"s_sleep 5\n"
"v_add_u32 v52, 2097152, v50\n"
"global_load_dword v55, v52, s[72:73]\n"
"s_waitcnt vmcnt(15)\n"
"s_barrier\n"
"v_lshlrev_b32 v43, 2, v0\n"
"v_add_u32 v43, 162816, v43\n"
"v_mov_b32 v44, 0\n"
"ds_write_b32 v43, v44\n"
"v_mov_b32 v45, 1\n"
"s_waitcnt vmcnt(3)\n"
"v_and_b32 v42, 63, v47\n"
"v_lshlrev_b32 v42, 2, v42\n"
"v_add_u32 v42, 162816, v42\n"
"ds_write_b32 v42, v45\n"
"ds_read_b32 v45, v43\n"
"s_waitcnt lgkmcnt(0)\n"
"v_cmp_eq_u32 vcc, 0, v45\n"
"s_nop 0\n"
"s_ff1_i32_b64 s70, vcc\n"
"s_lshl_b32 s61, s59, 9\n"
"v_mov_b32 v45, 0\n"
"v_mov_b32 v44, s70\n"
"v_mov_b32 v43, 162368\n"
"ds_write_b64 v43, v[44:45]\n"
"v_and_b32 v48, 31, v0\n"
"v_lshl_add_u32 v48, v48, 2, v43\n"
"ds_write_b32 v48, v45 offset:16\n"
"v_mov_b32 v46, s61\n"
"s_barrier\n"
"s_mov_b64 exec, 1\n"
"global_store_dword v46, v44, s[52:53] offset:256 sc1\n"
"s_mov_b64 exec, -1\n"
"s_sleep 5\n"
"v_add_u32 v52, 4194304, v50\n"
"global_load_dword v55, v52, s[72:73]\n"
"s_barrier\n"
"s_barrier\n"
"s_waitcnt vmcnt(1)\n"
"s_cmp_lg_u32 s71, 3\n"
"s_cbranch_scc1 .Lk_k_15_%=\n"
"s_sleep 5\n"
"s_add_u32 m0, s68, 110592\n"
"s_add_u32 s65, s68, 0\n"
"buffer_load_dwordx4 v10, s[28:31], s65 offen nt lds\n"
"buffer_load_dwordx4 v11, s[28:31], s65 offen offset:1024 nt lds\n"
"buffer_load_dwordx4 v12, s[28:31], s65 offen offset:2048 nt lds\n"
"buffer_load_dwordx4 v13, s[28:31], s65 offen offset:3072 nt lds\n"
"s_add_u32 m0, s68, 114688\n"
"s_add_u32 s65, s68, 4096\n"
"buffer_load_dwordx4 v10, s[28:31], s65 offen nt lds\n"
"buffer_load_dwordx4 v11, s[28:31], s65 offen offset:1024 nt lds\n"
"buffer_load_dwordx4 v12, s[28:31], s65 offen offset:2048 nt lds\n"
"buffer_load_dwordx4 v13, s[28:31], s65 offen offset:3072 nt lds\n"
"s_add_u32 m0, s68, 118784\n"
"s_add_u32 s65, s68, 8192\n"
"buffer_load_dwordx4 v10, s[28:31], s65 offen nt lds\n"
"buffer_load_dwordx4 v11, s[28:31], s65 offen offset:1024 nt lds\n"
"buffer_load_dwordx4 v12, s[28:31], s65 offen offset:2048 nt lds\n"
"buffer_load_dwordx4 v13, s[28:31], s65 offen offset:3072 nt lds\n"
"s_add_u32 m0, s68, 122880\n"
"s_add_u32 s65, s68, 12288\n"
"buffer_load_dwordx4 v10, s[28:31], s65 offen nt lds\n"
"buffer_load_dwordx4 v11, s[28:31], s65 offen offset:1024 nt lds\n"
"buffer_load_dwordx4 v12, s[28:31], s65 offen offset:2048 nt lds\n"
"buffer_load_dwordx4 v13, s[28:31], s65 offen offset:3072 nt lds\n"
"s_add_u32 s68, s68, 16384\n"
"s_add_u32 s71, s71, 3\n"
"s_cmp_lt_u32 s68, 49152\n"
"s_cselect_b32 s71, s71, 99\n"
".Lk_k_15_%=:\n"
"s_barrier\n"
"s_sleep 5\n"
"v_add_u32 v52, 6291456, v50\n"
"global_load_dword v55, v52, s[72:73]\n"
"s_cmp_lg_u32 s71, 4\n"
"s_cbranch_scc1 .Lk_k_18_%=\n"
"s_add_u32 m0, s68, 110592\n"
"s_add_u32 s65, s68, 0\n"
"buffer_load_dwordx4 v10, s[28:31], s65 offen nt lds\n"
"buffer_load_dwordx4 v11, s[28:31], s65 offen offset:1024 nt lds\n"
"buffer_load_dwordx4 v12, s[28:31], s65 offen offset:2048 nt lds\n"
"buffer_load_dwordx4 v13, s[28:31], s65 offen offset:3072 nt lds\n"
"s_add_u32 m0, s68, 114688\n"
"s_add_u32 s65, s68, 4096\n"
"buffer_load_dwordx4 v10, s[28:31], s65 offen nt lds\n"
"buffer_load_dwordx4 v11, s[28:31], s65 offen offset:1024 nt lds\n"
"buffer_load_dwordx4 v12, s[28:31], s65 offen offset:2048 nt lds\n"
"buffer_load_dwordx4 v13, s[28:31], s65 offen offset:3072 nt lds\n"
"s_add_u32 m0, s68, 118784\n"
"s_add_u32 s65, s68, 8192\n"
"buffer_load_dwordx4 v10, s[28:31], s65 offen nt lds\n"
"buffer_load_dwordx4 v11, s[28:31], s65 offen offset:1024 nt lds\n"
"buffer_load_dwordx4 v12, s[28:31], s65 offen offset:2048 nt lds\n"
"buffer_load_dwordx4 v13, s[28:31], s65 offen offset:3072 nt lds\n"
"s_add_u32 m0, s68, 122880\n"
"s_add_u32 s65, s68, 12288\n"
"buffer_load_dwordx4 v10, s[28:31], s65 offen nt lds\n"
"buffer_load_dwordx4 v11, s[28:31], s65 offen offset:1024 nt lds\n"
"buffer_load_dwordx4 v12, s[28:31], s65 offen offset:2048 nt lds\n"
"buffer_load_dwordx4 v13, s[28:31], s65 offen offset:3072 nt lds\n"
"s_add_u32 s68, s68, 16384\n"
"s_add_u32 s71, s71, 3\n"
"s_cmp_lt_u32 s68, 49152\n"
"s_cselect_b32 s71, s71, 99\n"
".Lk_k_18_%=:\n"
"s_barrier\n"
"s_cmp_lg_u32 s71, 5\n"
"s_cbranch_scc1 .Lk_k_21_%=\n"
"s_sleep 5\n"
"s_add_u32 m0, s68, 110592\n"
"s_add_u32 s65, s68, 0\n"
"buffer_load_dwordx4 v10, s[28:31], s65 offen nt lds\n"
"buffer_load_dwordx4 v11, s[28:31], s65 offen offset:1024 nt lds\n"
"buffer_load_dwordx4 v12, s[28:31], s65 offen offset:2048 nt lds\n"
"buffer_load_dwordx4 v13, s[28:31], s65 offen offset:3072 nt lds\n"
"s_add_u32 m0, s68, 114688\n"
"s_add_u32 s65, s68, 4096\n"
"buffer_load_dwordx4 v10, s[28:31], s65 offen nt lds\n"
"buffer_load_dwordx4 v11, s[28:31], s65 offen offset:1024 nt lds\n"
"buffer_load_dwordx4 v12, s[28:31], s65 offen offset:2048 nt lds\n"
"buffer_load_dwordx4 v13, s[28:31], s65 offen offset:3072 nt lds\n"
"s_add_u32 m0, s68, 118784\n"
"s_add_u32 s65, s68, 8192\n"
"buffer_load_dwordx4 v10, s[28:31], s65 offen nt lds\n"
"buffer_load_dwordx4 v11, s[28:31], s65 offen offset:1024 nt lds\n"
"buffer_load_dwordx4 v12, s[28:31], s65 offen offset:2048 nt lds\n"
"buffer_load_dwordx4 v13, s[28:31], s65 offen offset:3072 nt lds\n"
"s_add_u32 m0, s68, 122880\n"
"s_add_u32 s65, s68, 12288\n"
"buffer_load_dwordx4 v10, s[28:31], s65 offen nt lds\n"
"buffer_load_dwordx4 v11, s[28:31], s65 offen offset:1024 nt lds\n"
"buffer_load_dwordx4 v12, s[28:31], s65 offen offset:2048 nt lds\n"
"buffer_load_dwordx4 v13, s[28:31], s65 offen offset:3072 nt lds\n"
"s_add_u32 s68, s68, 16384\n"
"s_add_u32 s71, s71, 3\n"
"s_cmp_lt_u32 s68, 49152\n"
"s_cselect_b32 s71, s71, 99\n"
".Lk_k_21_%=:\n"
"s_barrier\n"
"s_cmp_lg_u32 s71, 6\n"
"s_cbranch_scc1 .Lk_k_24_%=\n"
"s_sleep 5\n"
"s_add_u32 m0, s68, 110592\n"
"s_add_u32 s65, s68, 0\n"
"buffer_load_dwordx4 v10, s[28:31], s65 offen nt lds\n"
"buffer_load_dwordx4 v11, s[28:31], s65 offen offset:1024 nt lds\n"
"buffer_load_dwordx4 v12, s[28:31], s65 offen offset:2048 nt lds\n"
"buffer_load_dwordx4 v13, s[28:31], s65 offen offset:3072 nt lds\n"
"s_add_u32 m0, s68, 114688\n"
"s_add_u32 s65, s68, 4096\n"
"buffer_load_dwordx4 v10, s[28:31], s65 offen nt lds\n"
"buffer_load_dwordx4 v11, s[28:31], s65 offen offset:1024 nt lds\n"
"buffer_load_dwordx4 v12, s[28:31], s65 offen offset:2048 nt lds\n"
"buffer_load_dwordx4 v13, s[28:31], s65 offen offset:3072 nt lds\n"
"s_add_u32 m0, s68, 118784\n"
"s_add_u32 s65, s68, 8192\n"
"buffer_load_dwordx4 v10, s[28:31], s65 offen nt lds\n"
"buffer_load_dwordx4 v11, s[28:31], s65 offen offset:1024 nt lds\n"
"buffer_load_dwordx4 v12, s[28:31], s65 offen offset:2048 nt lds\n"
"buffer_load_dwordx4 v13, s[28:31], s65 offen offset:3072 nt lds\n"
"s_add_u32 m0, s68, 122880\n"
"s_add_u32 s65, s68, 12288\n"
"buffer_load_dwordx4 v10, s[28:31], s65 offen nt lds\n"
"buffer_load_dwordx4 v11, s[28:31], s65 offen offset:1024 nt lds\n"
"buffer_load_dwordx4 v12, s[28:31], s65 offen offset:2048 nt lds\n"
"buffer_load_dwordx4 v13, s[28:31], s65 offen offset:3072 nt lds\n"
"s_add_u32 s68, s68, 16384\n"
"s_add_u32 s71, s71, 3\n"
"s_cmp_lt_u32 s68, 49152\n"
"s_cselect_b32 s71, s71, 99\n"
".Lk_k_24_%=:\n"
"s_barrier\n"
"s_cmp_lg_u32 s71, 7\n"
"s_cbranch_scc1 .Lk_k_27_%=\n"
"s_sleep 5\n"
"s_add_u32 m0, s68, 110592\n"
"s_add_u32 s65, s68, 0\n"
"buffer_load_dwordx4 v10, s[28:31], s65 offen nt lds\n"
"buffer_load_dwordx4 v11, s[28:31], s65 offen offset:1024 nt lds\n"
"buffer_load_dwordx4 v12, s[28:31], s65 offen offset:2048 nt lds\n"
"buffer_load_dwordx4 v13, s[28:31], s65 offen offset:3072 nt lds\n"
"s_add_u32 m0, s68, 114688\n"
"s_add_u32 s65, s68, 4096\n"
"buffer_load_dwordx4 v10, s[28:31], s65 offen nt lds\n"
"buffer_load_dwordx4 v11, s[28:31], s65 offen offset:1024 nt lds\n"
"buffer_load_dwordx4 v12, s[28:31], s65 offen offset:2048 nt lds\n"
"buffer_load_dwordx4 v13, s[28:31], s65 offen offset:3072 nt lds\n"
"s_add_u32 m0, s68, 118784\n"
"s_add_u32 s65, s68, 8192\n"
"buffer_load_dwordx4 v10, s[28:31], s65 offen nt lds\n"
"buffer_load_dwordx4 v11, s[28:31], s65 offen offset:1024 nt lds\n"
"buffer_load_dwordx4 v12, s[28:31], s65 offen offset:2048 nt lds\n"
"buffer_load_dwordx4 v13, s[28:31], s65 offen offset:3072 nt lds\n"
"s_add_u32 m0, s68, 122880\n"
"s_add_u32 s65, s68, 12288\n"
"buffer_load_dwordx4 v10, s[28:31], s65 offen nt lds\n"
"buffer_load_dwordx4 v11, s[28:31], s65 offen offset:1024 nt lds\n"
"buffer_load_dwordx4 v12, s[28:31], s65 offen offset:2048 nt lds\n"
"buffer_load_dwordx4 v13, s[28:31], s65 offen offset:3072 nt lds\n"
"s_add_u32 s68, s68, 16384\n"
"s_add_u32 s71, s71, 3\n"
"s_cmp_lt_u32 s68, 49152\n"
"s_cselect_b32 s71, s71, 99\n"
".Lk_k_27_%=:\n"
"s_barrier\n"
"s_cmp_lg_u32 s71, 8\n"
"s_cbranch_scc1 .Lk_k_30_%=\n"
"s_sleep 5\n"
"s_add_u32 m0, s68, 110592\n"
"s_add_u32 s65, s68, 0\n"
"buffer_load_dwordx4 v10, s[28:31], s65 offen nt lds\n"
"buffer_load_dwordx4 v11, s[28:31], s65 offen offset:1024 nt lds\n"
"buffer_load_dwordx4 v12, s[28:31], s65 offen offset:2048 nt lds\n"
"buffer_load_dwordx4 v13, s[28:31], s65 offen offset:3072 nt lds\n"
"s_add_u32 m0, s68, 114688\n"
"s_add_u32 s65, s68, 4096\n"
"buffer_load_dwordx4 v10, s[28:31], s65 offen nt lds\n"
"buffer_load_dwordx4 v11, s[28:31], s65 offen offset:1024 nt lds\n"
"buffer_load_dwordx4 v12, s[28:31], s65 offen offset:2048 nt lds\n"
"buffer_load_dwordx4 v13, s[28:31], s65 offen offset:3072 nt lds\n"
"s_add_u32 m0, s68, 118784\n"
"s_add_u32 s65, s68, 8192\n"
"buffer_load_dwordx4 v10, s[28:31], s65 offen nt lds\n"
"buffer_load_dwordx4 v11, s[28:31], s65 offen offset:1024 nt lds\n"
"buffer_load_dwordx4 v12, s[28:31], s65 offen offset:2048 nt lds\n"
"buffer_load_dwordx4 v13, s[28:31], s65 offen offset:3072 nt lds\n"
"s_add_u32 m0, s68, 122880\n"
"s_add_u32 s65, s68, 12288\n"
"buffer_load_dwordx4 v10, s[28:31], s65 offen nt lds\n"
"buffer_load_dwordx4 v11, s[28:31], s65 offen offset:1024 nt lds\n"
"buffer_load_dwordx4 v12, s[28:31], s65 offen offset:2048 nt lds\n"
"buffer_load_dwordx4 v13, s[28:31], s65 offen offset:3072 nt lds\n"
"s_add_u32 s68, s68, 16384\n"
"s_add_u32 s71, s71, 3\n"
"s_cmp_lt_u32 s68, 49152\n"
"s_cselect_b32 s71, s71, 99\n"
".Lk_k_30_%=:\n"
"s_barrier\n"
"s_cmp_lg_u32 s71, 9\n"
"s_cbranch_scc1 .Lk_k_33_%=\n"
"s_sleep 5\n"
"s_add_u32 m0, s68, 110592\n"
"s_add_u32 s65, s68, 0\n"
"buffer_load_dwordx4 v10, s[28:31], s65 offen nt lds\n"
"buffer_load_dwordx4 v11, s[28:31], s65 offen offset:1024 nt lds\n"
"buffer_load_dwordx4 v12, s[28:31], s65 offen offset:2048 nt lds\n"
"buffer_load_dwordx4 v13, s[28:31], s65 offen offset:3072 nt lds\n"
"s_add_u32 m0, s68, 114688\n"
"s_add_u32 s65, s68, 4096\n"
"buffer_load_dwordx4 v10, s[28:31], s65 offen nt lds\n"
"buffer_load_dwordx4 v11, s[28:31], s65 offen offset:1024 nt lds\n"
"buffer_load_dwordx4 v12, s[28:31], s65 offen offset:2048 nt lds\n"
"buffer_load_dwordx4 v13, s[28:31], s65 offen offset:3072 nt lds\n"
"s_add_u32 m0, s68, 118784\n"
"s_add_u32 s65, s68, 8192\n"
"buffer_load_dwordx4 v10, s[28:31], s65 offen nt lds\n"
"buffer_load_dwordx4 v11, s[28:31], s65 offen offset:1024 nt lds\n"
"buffer_load_dwordx4 v12, s[28:31], s65 offen offset:2048 nt lds\n"
"buffer_load_dwordx4 v13, s[28:31], s65 offen offset:3072 nt lds\n"
"s_add_u32 m0, s68, 122880\n"
"s_add_u32 s65, s68, 12288\n"
"buffer_load_dwordx4 v10, s[28:31], s65 offen nt lds\n"
"buffer_load_dwordx4 v11, s[28:31], s65 offen offset:1024 nt lds\n"
"buffer_load_dwordx4 v12, s[28:31], s65 offen offset:2048 nt lds\n"
"buffer_load_dwordx4 v13, s[28:31], s65 offen offset:3072 nt lds\n"
"s_add_u32 s68, s68, 16384\n"
"s_add_u32 s71, s71, 3\n"
"s_cmp_lt_u32 s68, 49152\n"
"s_cselect_b32 s71, s71, 99\n"
".Lk_k_33_%=:\n"
"s_barrier\n"
"global_load_dword v38, v36, s[52:53] offset:256 sc1\n"
"s_cmp_lg_u32 s71, 10\n"
"s_cbranch_scc1 .Lk_k_36_%=\n"
"s_sleep 5\n"
"s_add_u32 m0, s68, 110592\n"
"s_add_u32 s65, s68, 0\n"
"buffer_load_dwordx4 v10, s[28:31], s65 offen nt lds\n"
"buffer_load_dwordx4 v11, s[28:31], s65 offen offset:1024 nt lds\n"
"buffer_load_dwordx4 v12, s[28:31], s65 offen offset:2048 nt lds\n"
"buffer_load_dwordx4 v13, s[28:31], s65 offen offset:3072 nt lds\n"
"s_add_u32 m0, s68, 114688\n"
"s_add_u32 s65, s68, 4096\n"
"buffer_load_dwordx4 v10, s[28:31], s65 offen nt lds\n"
"buffer_load_dwordx4 v11, s[28:31], s65 offen offset:1024 nt lds\n"
"buffer_load_dwordx4 v12, s[28:31], s65 offen offset:2048 nt lds\n"
"buffer_load_dwordx4 v13, s[28:31], s65 offen offset:3072 nt lds\n"
"s_add_u32 m0, s68, 118784\n"
"s_add_u32 s65, s68, 8192\n"
"buffer_load_dwordx4 v10, s[28:31], s65 offen nt lds\n"
"buffer_load_dwordx4 v11, s[28:31], s65 offen offset:1024 nt lds\n"
"buffer_load_dwordx4 v12, s[28:31], s65 offen offset:2048 nt lds\n"
"buffer_load_dwordx4 v13, s[28:31], s65 offen offset:3072 nt lds\n"
"s_add_u32 m0, s68, 122880\n"
"s_add_u32 s65, s68, 12288\n"
"buffer_load_dwordx4 v10, s[28:31], s65 offen nt lds\n"
"buffer_load_dwordx4 v11, s[28:31], s65 offen offset:1024 nt lds\n"
"buffer_load_dwordx4 v12, s[28:31], s65 offen offset:2048 nt lds\n"
"buffer_load_dwordx4 v13, s[28:31], s65 offen offset:3072 nt lds\n"
"s_add_u32 s68, s68, 16384\n"
"s_add_u32 s71, s71, 3\n"
"s_cmp_lt_u32 s68, 49152\n"
"s_cselect_b32 s71, s71, 99\n"
".Lk_k_36_%=:\n"
"s_barrier\n"
"s_barrier\n"
"s_mov_b64 exec, 0xfff\n"
"s_waitcnt vmcnt(0)\n"
"global_store_dword v37, v38, s[52:53] offset:144 sc1\n"
"s_mov_b64 exec, -1\n"
"s_barrier\n"
"s_add_u32 m0, s79, 77824\n"
"s_add_u32 s65, s79, 0\n"
"buffer_load_dwordx4 v14, s[32:35], s65 offen offset:0 nt lds\n"
"buffer_load_dwordx4 v15, s[32:35], s65 offen offset:1024 nt lds\n"
"buffer_load_dwordx4 v16, s[32:35], s65 offen offset:2048 nt lds\n"
"buffer_load_dwordx4 v17, s[32:35], s65 offen offset:3072 nt lds\n"
"s_barrier\n"
"s_add_u32 m0, s79, 94208\n"
"s_add_u32 s65, s79, 49152\n"
"buffer_load_dwordx4 v10, s[28:31], s65 offen offset:0 nt lds\n"
"buffer_load_dwordx4 v11, s[28:31], s65 offen offset:1024 nt lds\n"
"buffer_load_dwordx4 v12, s[28:31], s65 offen offset:2048 nt lds\n"
"buffer_load_dwordx4 v13, s[28:31], s65 offen offset:3072 nt lds\n"
"s_waitcnt vmcnt(8)\n"
"s_barrier\n"
"s_add_u32 m0, s79, 61440\n"
"s_add_u32 s65, s79, 16384\n"
"buffer_load_dwordx4 v14, s[32:35], s65 offen offset:0 nt lds\n"
"buffer_load_dwordx4 v15, s[32:35], s65 offen offset:1024 nt lds\n"
"buffer_load_dwordx4 v16, s[32:35], s65 offen offset:2048 nt lds\n"
"buffer_load_dwordx4 v17, s[32:35], s65 offen offset:3072 nt lds\n"
"s_waitcnt lgkmcnt(0)\n"
".Lk_M_10_%=:\n"
"s_nop 4\n"
"s_barrier\n"
"v_lshrrev_b32 v42, 4, v0\n"
"v_and_b32 v41, 15, v0\n"
"v_lshlrev_b32 v43, 1, v42\n"
"v_xor_b32 v41, v41, v43\n"
"v_lshlrev_b32 v41, 4, v41\n"
"v_mul_u32_u24 v43, 2048, v42\n"
"v_add_u32 v6, v43, v41\n"
"v_xor_b32 v8, 128, v6\n"
"v_add_u32 v7, 7168, v6\n"
"v_add_u32 v9, 21504, v8\n"
"v_add_u32 v8, 14336, v8\n"
"v_mov_b32 v40, 162368\n"
"ds_read_b32 v40, v40\n"
"s_waitcnt lgkmcnt(0)\n"
"v_readfirstlane_b32 s70, v40\n"
"s_mov_b32 s68, 0\n"
"s_add_u32 s65, s79, 65536\n"
"buffer_load_dwordx4 v[56:59], v10, s[28:31], s65 offen offset:0 nt\n"
"buffer_load_dwordx4 v[60:63], v11, s[28:31], s65 offen offset:1024 nt\n"
"buffer_load_dwordx4 v[64:67], v12, s[28:31], s65 offen offset:2048 nt\n"
"buffer_load_dwordx4 v[68:71], v13, s[28:31], s65 offen offset:3072 nt\n"
"s_mov_b32 s20, 0x98765410\n"
"s_mov_b32 s21, 0xba32fedc\n"
"s_cmp_eq_u32 s58, 1\n"
"s_cselect_b32 s20, 0x98765432, s20\n"
"s_cselect_b32 s21, 0xba10fedc, s21\n"
"s_cmp_eq_u32 s58, 2\n"
"s_cselect_b32 s20, 0xdc987654, s20\n"
"s_cselect_b32 s21, 0x32ba10fe, s21\n"
"s_cmp_eq_u32 s58, 3\n"
"s_cselect_b32 s20, 0xfedc9876, s20\n"
"s_cselect_b32 s21, 0x32ba5410, s21\n"
"s_cmp_eq_u32 s58, 4\n"
"s_cselect_b32 s20, 0x10fedc98, s20\n"
"s_cselect_b32 s21, 0x32ba7654, s21\n"
"s_cmp_eq_u32 s58, 5\n"
"s_cselect_b32 s20, 0x10fedcba, s20\n"
"s_cselect_b32 s21, 0x32987654, s21\n"
"s_cmp_eq_u32 s58, 6\n"
"s_cselect_b32 s20, 0x5410fedc, s20\n"
"s_cselect_b32 s21, 0xba329876, s21\n"
"s_cmp_eq_u32 s58, 7\n"
"s_cselect_b32 s20, 0x765410fe, s20\n"
"s_cselect_b32 s21, 0xba32dc98, s21\n"
"s_waitcnt vmcnt(12)\n"
"s_barrier\n"
"ds_read_b64_tr_b16 v[136:137], v24 offset:16384\n"
"ds_read_b64_tr_b16 v[138:139], v24 offset:20480\n"
"ds_read_b128 v[96:99], v19 offset:0\n"
"ds_read_b128 v[100:103], v19 offset:1024\n"
"ds_read_b128 v[104:107], v19 offset:2048\n"
"ds_read_b128 v[108:111], v19 offset:3072\n"
"ds_read_b128 v[112:115], v19 offset:4096\n"
"ds_read_b64_tr_b16 v[140:141], v25 offset:16384\n"
"ds_read_b64_tr_b16 v[142:143], v25 offset:20480\n"
"s_waitcnt lgkmcnt(6)\n"
"v_mfma_f32_16x16x32_bf16 v[176:179], v[136:139], v[96:99], 0\n"
"ds_read_b64_tr_b16 v[144:145], v26 offset:16384\n"
"ds_read_b64_tr_b16 v[146:147], v26 offset:20480\n"
"s_waitcnt lgkmcnt(7)\n"
"v_mfma_f32_16x16x32_bf16 v[180:183], v[136:139], v[100:103], 0\n"
"s_add_u32 m0, s79, 126976\n"
"s_add_u32 s65, s79, 32768\n"
"s_waitcnt vmcnt(5)\n"
"buffer_load_dwordx4 v14, s[32:35], s65 offen offset:0 nt lds\n"
"s_waitcnt lgkmcnt(6)\n"
"v_mfma_f32_16x16x32_bf16 v[184:187], v[136:139], v[104:107], 0\n"
"ds_read_b64_tr_b16 v[148:149], v27 offset:16384\n"
"ds_read_b64_tr_b16 v[150:151], v27 offset:20480\n"
"s_waitcnt lgkmcnt(7)\n"
"v_mfma_f32_16x16x32_bf16 v[188:191], v[136:139], v[108:111], 0\n"
"s_waitcnt vmcnt(5)\n"
"buffer_load_dwordx4 v15, s[32:35], s65 offen offset:1024 nt lds\n"
"s_waitcnt lgkmcnt(6)\n"
"v_mfma_f32_16x16x32_bf16 v[192:195], v[136:139], v[112:115], 0\n"
"s_waitcnt lgkmcnt(4)\n"
"v_mfma_f32_16x16x32_bf16 v[196:199], v[140:143], v[96:99], 0\n"
"ds_read_b64_tr_b16 v[136:137], v24 offset:24576\n"
"ds_read_b64_tr_b16 v[138:139], v24 offset:28672\n"
"s_waitcnt vmcnt(5)\n"
"buffer_load_dwordx4 v16, s[32:35], s65 offen offset:2048 nt lds\n"
"v_mfma_f32_16x16x32_bf16 v[200:203], v[140:143], v[100:103], 0\n"
"ds_read_b128 v[116:119], v19 offset:5120\n"
"v_mfma_f32_16x16x32_bf16 v[204:207], v[140:143], v[104:107], 0\n"
"ds_read_b128 v[120:123], v19 offset:6144\n"
"v_mfma_f32_16x16x32_bf16 v[208:211], v[140:143], v[108:111], 0\n"
"ds_read_b128 v[124:127], v19 offset:7168\n"
"s_waitcnt vmcnt(5)\n"
"buffer_load_dwordx4 v17, s[32:35], s65 offen offset:3072 nt lds\n"
"v_mfma_f32_16x16x32_bf16 v[212:215], v[140:143], v[112:115], 0\n"
"ds_read_b128 v[128:131], v19 offset:8192\n"
"s_waitcnt lgkmcnt(8)\n"
"v_mfma_f32_16x16x32_bf16 v[216:219], v[144:147], v[96:99], 0\n"
"ds_read_b128 v[132:135], v19 offset:9216\n"
"s_add_u32 m0, s79, 20480\n"
"s_add_u32 s65, s79, 81920\n"
"s_waitcnt vmcnt(5)\n"
"buffer_load_dwordx4 v10, s[28:31], s65 offen offset:0 nt lds\n"
"v_mfma_f32_16x16x32_bf16 v[220:223], v[144:147], v[100:103], 0\n"
"ds_read_b64_tr_b16 v[140:141], v25 offset:24576\n"
"ds_read_b64_tr_b16 v[142:143], v25 offset:28672\n"
"v_mfma_f32_16x16x32_bf16 v[224:227], v[144:147], v[104:107], 0\n"
"v_mfma_f32_16x16x32_bf16 v[228:231], v[144:147], v[108:111], 0\n"
"s_waitcnt vmcnt(5)\n"
"buffer_load_dwordx4 v11, s[28:31], s65 offen offset:1024 nt lds\n"
"v_mfma_f32_16x16x32_bf16 v[232:235], v[144:147], v[112:115], 0\n"
"s_waitcnt lgkmcnt(9)\n"
"v_mfma_f32_16x16x32_bf16 v[236:239], v[148:151], v[96:99], 0\n"
"ds_read_b64_tr_b16 v[144:145], v26 offset:24576\n"
"ds_read_b64_tr_b16 v[146:147], v26 offset:28672\n"
"ds_write_b128 v33, v[56:59] offset:0\n"
"v_mfma_f32_16x16x32_bf16 v[240:243], v[148:151], v[100:103], 0\n"
"v_mfma_f32_16x16x32_bf16 v[244:247], v[148:151], v[104:107], 0\n"
"ds_write_b128 v33, v[60:63] offset:1024\n"
"v_mfma_f32_16x16x32_bf16 v[248:251], v[148:151], v[108:111], 0\n"
"v_mfma_f32_16x16x32_bf16 v[252:255], v[148:151], v[112:115], 0\n"
"s_waitcnt lgkmcnt(10)\n"
"v_mfma_f32_16x16x32_bf16 v[176:179], v[136:139], v[116:119], v[176:179]\n"
"ds_read_b64_tr_b16 v[148:149], v27 offset:24576\n"
"ds_read_b64_tr_b16 v[150:151], v27 offset:28672\n"
"ds_write_b128 v33, v[64:67] offset:2048\n"
"s_waitcnt lgkmcnt(12)\n"
"v_mfma_f32_16x16x32_bf16 v[180:183], v[136:139], v[120:123], v[180:183]\n"
"s_waitcnt lgkmcnt(11)\n"
"v_mfma_f32_16x16x32_bf16 v[184:187], v[136:139], v[124:127], v[184:187]\n"
"ds_write_b128 v33, v[68:71] offset:3072\n"
"s_waitcnt lgkmcnt(11)\n"
"v_mfma_f32_16x16x32_bf16 v[188:191], v[136:139], v[128:131], v[188:191]\n"
"s_waitcnt lgkmcnt(10)\n"
"v_mfma_f32_16x16x32_bf16 v[192:195], v[136:139], v[132:135], v[192:195]\n"
"s_waitcnt lgkmcnt(8)\n"
"v_mfma_f32_16x16x32_bf16 v[196:199], v[140:143], v[116:119], v[196:199]\n"
"v_mfma_f32_16x16x32_bf16 v[200:203], v[140:143], v[120:123], v[200:203]\n"
"v_mfma_f32_16x16x32_bf16 v[204:207], v[140:143], v[124:127], v[204:207]\n"
"v_mfma_f32_16x16x32_bf16 v[208:211], v[140:143], v[128:131], v[208:211]\n"
"v_mfma_f32_16x16x32_bf16 v[212:215], v[140:143], v[132:135], v[212:215]\n"
"s_waitcnt lgkmcnt(0)\n"
"s_barrier\n"
"v_mfma_f32_16x16x32_bf16 v[216:219], v[144:147], v[116:119], v[216:219]\n"
"ds_read_b64_tr_b16 v[136:137], v24 offset:0\n"
"ds_read_b64_tr_b16 v[138:139], v24 offset:4096\n"
"v_mfma_f32_16x16x32_bf16 v[220:223], v[144:147], v[120:123], v[220:223]\n"
"ds_read_b128 v[96:99], v19 offset:40960\n"
"ds_read_b128 v[100:103], v19 offset:41984\n"
"s_add_u32 m0, s79, 20480\n"
"s_add_u32 s65, s79, 81920\n"
"s_waitcnt vmcnt(5)\n"
"buffer_load_dwordx4 v12, s[28:31], s65 offen offset:2048 nt lds\n"
"v_mfma_f32_16x16x32_bf16 v[224:227], v[144:147], v[124:127], v[224:227]\n"
"ds_read_b128 v[104:107], v19 offset:43008\n"
"ds_read_b128 v[108:111], v19 offset:44032\n"
"v_mfma_f32_16x16x32_bf16 v[228:231], v[144:147], v[128:131], v[228:231]\n"
"ds_read_b128 v[112:115], v19 offset:45056\n"
"ds_read_b64_tr_b16 v[140:141], v25 offset:0\n"
"v_mfma_f32_16x16x32_bf16 v[232:235], v[144:147], v[132:135], v[232:235]\n"
"ds_read_b64_tr_b16 v[142:143], v25 offset:4096\n"
"v_mfma_f32_16x16x32_bf16 v[236:239], v[148:151], v[116:119], v[236:239]\n"
"v_mfma_f32_16x16x32_bf16 v[240:243], v[148:151], v[120:123], v[240:243]\n"
"s_waitcnt vmcnt(5)\n"
"buffer_load_dwordx4 v13, s[28:31], s65 offen offset:3072 nt lds\n"
"v_mfma_f32_16x16x32_bf16 v[244:247], v[148:151], v[124:127], v[244:247]\n"
"v_mfma_f32_16x16x32_bf16 v[248:251], v[148:151], v[128:131], v[248:251]\n"
"v_mfma_f32_16x16x32_bf16 v[252:255], v[148:151], v[132:135], v[252:255]\n"
"s_waitcnt lgkmcnt(6)\n"
"v_mfma_f32_16x16x32_bf16 v[176:179], v[136:139], v[96:99], v[176:179]\n"
"ds_read_b64_tr_b16 v[144:145], v26 offset:0\n"
"ds_read_b64_tr_b16 v[146:147], v26 offset:4096\n"
"s_waitcnt lgkmcnt(7)\n"
"v_mfma_f32_16x16x32_bf16 v[180:183], v[136:139], v[100:103], v[180:183]\n"
"s_waitcnt lgkmcnt(6)\n"
"v_mfma_f32_16x16x32_bf16 v[184:187], v[136:139], v[104:107], v[184:187]\n"
"ds_read_b64_tr_b16 v[148:149], v27 offset:0\n"
"ds_read_b64_tr_b16 v[150:151], v27 offset:4096\n"
"s_add_u32 m0, s79, 143360\n"
"s_add_u32 s65, s79, 49152\n"
"s_waitcnt vmcnt(5)\n"
"buffer_load_dwordx4 v14, s[32:35], s65 offen offset:0 nt lds\n"
"s_waitcnt lgkmcnt(7)\n"
"v_mfma_f32_16x16x32_bf16 v[188:191], v[136:139], v[108:111], v[188:191]\n"
"s_waitcnt lgkmcnt(6)\n"
"v_mfma_f32_16x16x32_bf16 v[192:195], v[136:139], v[112:115], v[192:195]\n"
"s_waitcnt lgkmcnt(4)\n"
"v_mfma_f32_16x16x32_bf16 v[196:199], v[140:143], v[96:99], v[196:199]\n"
"ds_read_b64_tr_b16 v[136:137], v24 offset:8192\n"
"ds_read_b64_tr_b16 v[138:139], v24 offset:12288\n"
"v_mfma_f32_16x16x32_bf16 v[200:203], v[140:143], v[100:103], v[200:203]\n"
"ds_read_b128 v[116:119], v19 offset:46080\n"
"v_mfma_f32_16x16x32_bf16 v[204:207], v[140:143], v[104:107], v[204:207]\n"
"ds_read_b128 v[120:123], v19 offset:47104\n"
"v_mfma_f32_16x16x32_bf16 v[208:211], v[140:143], v[108:111], v[208:211]\n"
"ds_read_b128 v[124:127], v19 offset:48128\n"
"s_waitcnt vmcnt(5)\n"
"buffer_load_dwordx4 v15, s[32:35], s65 offen offset:1024 nt lds\n"
"v_mfma_f32_16x16x32_bf16 v[212:215], v[140:143], v[112:115], v[212:215]\n"
"ds_read_b128 v[128:131], v19 offset:49152\n"
"s_waitcnt lgkmcnt(8)\n"
"v_mfma_f32_16x16x32_bf16 v[216:219], v[144:147], v[96:99], v[216:219]\n"
"ds_read_b128 v[132:135], v19 offset:50176\n"
"v_mfma_f32_16x16x32_bf16 v[220:223], v[144:147], v[100:103], v[220:223]\n"
"ds_read_b64_tr_b16 v[140:141], v25 offset:8192\n"
"ds_read_b64_tr_b16 v[142:143], v25 offset:12288\n"
"v_mfma_f32_16x16x32_bf16 v[224:227], v[144:147], v[104:107], v[224:227]\n"
"v_mfma_f32_16x16x32_bf16 v[228:231], v[144:147], v[108:111], v[228:231]\n"
"s_waitcnt vmcnt(5)\n"
"buffer_load_dwordx4 v16, s[32:35], s65 offen offset:2048 nt lds\n"
"v_mfma_f32_16x16x32_bf16 v[232:235], v[144:147], v[112:115], v[232:235]\n"
"s_waitcnt lgkmcnt(9)\n"
"v_mfma_f32_16x16x32_bf16 v[236:239], v[148:151], v[96:99], v[236:239]\n"
"ds_read_b64_tr_b16 v[144:145], v26 offset:8192\n"
"ds_read_b64_tr_b16 v[146:147], v26 offset:12288\n"
"v_mfma_f32_16x16x32_bf16 v[240:243], v[148:151], v[100:103], v[240:243]\n"
"v_mfma_f32_16x16x32_bf16 v[244:247], v[148:151], v[104:107], v[244:247]\n"
"v_mfma_f32_16x16x32_bf16 v[248:251], v[148:151], v[108:111], v[248:251]\n"
"v_mfma_f32_16x16x32_bf16 v[252:255], v[148:151], v[112:115], v[252:255]\n"
"s_waitcnt vmcnt(5)\n"
"buffer_load_dwordx4 v17, s[32:35], s65 offen offset:3072 nt lds\n"
"s_waitcnt lgkmcnt(8)\n"
"v_mfma_f32_16x16x32_bf16 v[176:179], v[136:139], v[116:119], v[176:179]\n"
"ds_read_b64_tr_b16 v[148:149], v27 offset:8192\n"
"ds_read_b64_tr_b16 v[150:151], v27 offset:12288\n"
"s_waitcnt lgkmcnt(9)\n"
"v_mfma_f32_16x16x32_bf16 v[180:183], v[136:139], v[120:123], v[180:183]\n"
"s_waitcnt lgkmcnt(8)\n"
"v_mfma_f32_16x16x32_bf16 v[184:187], v[136:139], v[124:127], v[184:187]\n"
"s_waitcnt lgkmcnt(7)\n"
"v_mfma_f32_16x16x32_bf16 v[188:191], v[136:139], v[128:131], v[188:191]\n"
"s_waitcnt lgkmcnt(6)\n"
"v_mfma_f32_16x16x32_bf16 v[192:195], v[136:139], v[132:135], v[192:195]\n"
"s_waitcnt lgkmcnt(4)\n"
"v_mfma_f32_16x16x32_bf16 v[196:199], v[140:143], v[116:119], v[196:199]\n"
"v_mfma_f32_16x16x32_bf16 v[200:203], v[140:143], v[120:123], v[200:203]\n"
"v_mfma_f32_16x16x32_bf16 v[204:207], v[140:143], v[124:127], v[204:207]\n"
"v_mfma_f32_16x16x32_bf16 v[208:211], v[140:143], v[128:131], v[208:211]\n"
"v_mfma_f32_16x16x32_bf16 v[212:215], v[140:143], v[132:135], v[212:215]\n"
"s_waitcnt lgkmcnt(0)\n"
"s_barrier\n"
"v_mfma_f32_16x16x32_bf16 v[216:219], v[144:147], v[116:119], v[216:219]\n"
"ds_read_b64_tr_b16 v[136:137], v20 offset:16384\n"
"ds_read_b64_tr_b16 v[138:139], v20 offset:20480\n"
"v_mfma_f32_16x16x32_bf16 v[220:223], v[144:147], v[120:123], v[220:223]\n"
"ds_read_b128 v[96:99], v19 offset:0\n"
"ds_read_b128 v[100:103], v19 offset:1024\n"
"s_add_u32 m0, s79, 77824\n"
"s_add_u32 s65, s79, 98304\n"
"s_waitcnt vmcnt(5)\n"
"buffer_load_dwordx4 v10, s[28:31], s65 offen offset:0 nt lds\n"
"v_mfma_f32_16x16x32_bf16 v[224:227], v[144:147], v[124:127], v[224:227]\n"
"ds_read_b128 v[104:107], v19 offset:2048\n"
"ds_read_b128 v[108:111], v19 offset:3072\n"
"v_mfma_f32_16x16x32_bf16 v[228:231], v[144:147], v[128:131], v[228:231]\n"
"ds_read_b128 v[112:115], v19 offset:4096\n"
"ds_read_b64_tr_b16 v[140:141], v21 offset:16384\n"
"v_mfma_f32_16x16x32_bf16 v[232:235], v[144:147], v[132:135], v[232:235]\n"
"ds_read_b64_tr_b16 v[142:143], v21 offset:20480\n"
"v_mfma_f32_16x16x32_bf16 v[236:239], v[148:151], v[116:119], v[236:239]\n"
"v_mfma_f32_16x16x32_bf16 v[240:243], v[148:151], v[120:123], v[240:243]\n"
"s_waitcnt vmcnt(5)\n"
"buffer_load_dwordx4 v11, s[28:31], s65 offen offset:1024 nt lds\n"
"v_mfma_f32_16x16x32_bf16 v[244:247], v[148:151], v[124:127], v[244:247]\n"
"v_mfma_f32_16x16x32_bf16 v[248:251], v[148:151], v[128:131], v[248:251]\n"
"v_mfma_f32_16x16x32_bf16 v[252:255], v[148:151], v[132:135], v[252:255]\n"
"s_waitcnt lgkmcnt(6)\n"
"v_mfma_f32_16x16x32_bf16 v[176:179], v[136:139], v[96:99], v[176:179]\n"
"ds_read_b64_tr_b16 v[144:145], v22 offset:16384\n"
"ds_read_b64_tr_b16 v[146:147], v22 offset:20480\n"
"s_waitcnt lgkmcnt(7)\n"
"v_mfma_f32_16x16x32_bf16 v[180:183], v[136:139], v[100:103], v[180:183]\n"
"s_waitcnt lgkmcnt(6)\n"
"v_mfma_f32_16x16x32_bf16 v[184:187], v[136:139], v[104:107], v[184:187]\n"
"ds_read_b64_tr_b16 v[148:149], v23 offset:16384\n"
"ds_read_b64_tr_b16 v[150:151], v23 offset:20480\n"
"s_waitcnt vmcnt(5)\n"
"buffer_load_dwordx4 v12, s[28:31], s65 offen offset:2048 nt lds\n"
"s_waitcnt lgkmcnt(7)\n"
"v_mfma_f32_16x16x32_bf16 v[188:191], v[136:139], v[108:111], v[188:191]\n"
"s_waitcnt lgkmcnt(6)\n"
"v_mfma_f32_16x16x32_bf16 v[192:195], v[136:139], v[112:115], v[192:195]\n"
"s_waitcnt lgkmcnt(4)\n"
"v_mfma_f32_16x16x32_bf16 v[196:199], v[140:143], v[96:99], v[196:199]\n"
"ds_read_b64_tr_b16 v[136:137], v20 offset:24576\n"
"ds_read_b64_tr_b16 v[138:139], v20 offset:28672\n"
"v_mfma_f32_16x16x32_bf16 v[200:203], v[140:143], v[100:103], v[200:203]\n"
"ds_read_b128 v[116:119], v19 offset:5120\n"
"v_mfma_f32_16x16x32_bf16 v[204:207], v[140:143], v[104:107], v[204:207]\n"
"ds_read_b128 v[120:123], v19 offset:6144\n"
"v_mfma_f32_16x16x32_bf16 v[208:211], v[140:143], v[108:111], v[208:211]\n"
"ds_read_b128 v[124:127], v19 offset:7168\n"
"s_waitcnt vmcnt(5)\n"
"buffer_load_dwordx4 v13, s[28:31], s65 offen offset:3072 nt lds\n"
"v_mfma_f32_16x16x32_bf16 v[212:215], v[140:143], v[112:115], v[212:215]\n"
"ds_read_b128 v[128:131], v19 offset:8192\n"
"s_waitcnt lgkmcnt(8)\n"
"v_mfma_f32_16x16x32_bf16 v[216:219], v[144:147], v[96:99], v[216:219]\n"
"ds_read_b128 v[132:135], v19 offset:9216\n"
"v_mfma_f32_16x16x32_bf16 v[220:223], v[144:147], v[100:103], v[220:223]\n"
"ds_read_b64_tr_b16 v[140:141], v21 offset:24576\n"
"ds_read_b64_tr_b16 v[142:143], v21 offset:28672\n"
"v_mfma_f32_16x16x32_bf16 v[224:227], v[144:147], v[104:107], v[224:227]\n"
"v_mfma_f32_16x16x32_bf16 v[228:231], v[144:147], v[108:111], v[228:231]\n"
"s_add_u32 m0, s79, 94208\n"
"s_add_u32 s65, s79, 65536\n"
"s_waitcnt vmcnt(5)\n"
"buffer_load_dwordx4 v14, s[32:35], s65 offen offset:0 nt lds\n"
"v_mfma_f32_16x16x32_bf16 v[232:235], v[144:147], v[112:115], v[232:235]\n"
"s_waitcnt lgkmcnt(9)\n"
"v_mfma_f32_16x16x32_bf16 v[236:239], v[148:151], v[96:99], v[236:239]\n"
"ds_read_b64_tr_b16 v[144:145], v22 offset:24576\n"
"ds_read_b64_tr_b16 v[146:147], v22 offset:28672\n"
"v_mfma_f32_16x16x32_bf16 v[240:243], v[148:151], v[100:103], v[240:243]\n"
"v_mfma_f32_16x16x32_bf16 v[244:247], v[148:151], v[104:107], v[244:247]\n"
"v_mfma_f32_16x16x32_bf16 v[248:251], v[148:151], v[108:111], v[248:251]\n"
"v_mfma_f32_16x16x32_bf16 v[252:255], v[148:151], v[112:115], v[252:255]\n"
"s_waitcnt vmcnt(5)\n"
"buffer_load_dwordx4 v15, s[32:35], s65 offen offset:1024 nt lds\n"
"s_waitcnt lgkmcnt(8)\n"
"v_mfma_f32_16x16x32_bf16 v[176:179], v[136:139], v[116:119], v[176:179]\n"
"ds_read_b64_tr_b16 v[148:149], v23 offset:24576\n"
"ds_read_b64_tr_b16 v[150:151], v23 offset:28672\n"
"s_waitcnt lgkmcnt(9)\n"
"v_mfma_f32_16x16x32_bf16 v[180:183], v[136:139], v[120:123], v[180:183]\n"
"s_waitcnt lgkmcnt(8)\n"
"v_mfma_f32_16x16x32_bf16 v[184:187], v[136:139], v[124:127], v[184:187]\n"
"s_waitcnt lgkmcnt(7)\n"
"v_mfma_f32_16x16x32_bf16 v[188:191], v[136:139], v[128:131], v[188:191]\n"
"s_waitcnt lgkmcnt(6)\n"
"v_mfma_f32_16x16x32_bf16 v[192:195], v[136:139], v[132:135], v[192:195]\n"
"s_waitcnt lgkmcnt(4)\n"
"v_mfma_f32_16x16x32_bf16 v[196:199], v[140:143], v[116:119], v[196:199]\n"
"v_mfma_f32_16x16x32_bf16 v[200:203], v[140:143], v[120:123], v[200:203]\n"
"v_mfma_f32_16x16x32_bf16 v[204:207], v[140:143], v[124:127], v[204:207]\n"
"v_mfma_f32_16x16x32_bf16 v[208:211], v[140:143], v[128:131], v[208:211]\n"
"v_mfma_f32_16x16x32_bf16 v[212:215], v[140:143], v[132:135], v[212:215]\n"
"s_waitcnt lgkmcnt(0)\n"
"s_barrier\n"
"v_mfma_f32_16x16x32_bf16 v[216:219], v[144:147], v[116:119], v[216:219]\n"
"ds_read_b64_tr_b16 v[136:137], v20 offset:32768\n"
"ds_read_b64_tr_b16 v[138:139], v20 offset:36864\n"
"v_mfma_f32_16x16x32_bf16 v[220:223], v[144:147], v[120:123], v[220:223]\n"
"ds_read_b128 v[96:99], v19 offset:40960\n"
"ds_read_b128 v[100:103], v19 offset:41984\n"
"s_add_u32 m0, s79, 94208\n"
"s_add_u32 s65, s79, 65536\n"
"s_waitcnt vmcnt(5)\n"
"buffer_load_dwordx4 v16, s[32:35], s65 offen offset:2048 nt lds\n"
"v_mfma_f32_16x16x32_bf16 v[224:227], v[144:147], v[124:127], v[224:227]\n"
"ds_read_b128 v[104:107], v19 offset:43008\n"
"ds_read_b128 v[108:111], v19 offset:44032\n"
"v_mfma_f32_16x16x32_bf16 v[228:231], v[144:147], v[128:131], v[228:231]\n"
"ds_read_b128 v[112:115], v19 offset:45056\n"
"ds_read_b64_tr_b16 v[140:141], v21 offset:32768\n"
"v_mfma_f32_16x16x32_bf16 v[232:235], v[144:147], v[132:135], v[232:235]\n"
"ds_read_b64_tr_b16 v[142:143], v21 offset:36864\n"
"v_mfma_f32_16x16x32_bf16 v[236:239], v[148:151], v[116:119], v[236:239]\n"
"v_mfma_f32_16x16x32_bf16 v[240:243], v[148:151], v[120:123], v[240:243]\n"
"s_waitcnt vmcnt(5)\n"
"buffer_load_dwordx4 v17, s[32:35], s65 offen offset:3072 nt lds\n"
"v_mfma_f32_16x16x32_bf16 v[244:247], v[148:151], v[124:127], v[244:247]\n"
"v_mfma_f32_16x16x32_bf16 v[248:251], v[148:151], v[128:131], v[248:251]\n"
"v_mfma_f32_16x16x32_bf16 v[252:255], v[148:151], v[132:135], v[252:255]\n"
"s_waitcnt lgkmcnt(6)\n"
"v_mfma_f32_16x16x32_bf16 v[176:179], v[136:139], v[96:99], v[176:179]\n"
"ds_read_b64_tr_b16 v[144:145], v22 offset:32768\n"
"ds_read_b64_tr_b16 v[146:147], v22 offset:36864\n"
"s_waitcnt lgkmcnt(7)\n"
"v_mfma_f32_16x16x32_bf16 v[180:183], v[136:139], v[100:103], v[180:183]\n"
"s_waitcnt lgkmcnt(6)\n"
"v_mfma_f32_16x16x32_bf16 v[184:187], v[136:139], v[104:107], v[184:187]\n"
"ds_read_b64_tr_b16 v[148:149], v23 offset:32768\n"
"ds_read_b64_tr_b16 v[150:151], v23 offset:36864\n"
"s_add_u32 m0, s79, 61440\n"
"s_add_u32 s65, s79, 114688\n"
"s_waitcnt vmcnt(5)\n"
"buffer_load_dwordx4 v10, s[28:31], s65 offen offset:0 nt lds\n"
"s_waitcnt lgkmcnt(7)\n"
"v_mfma_f32_16x16x32_bf16 v[188:191], v[136:139], v[108:111], v[188:191]\n"
"s_waitcnt lgkmcnt(6)\n"
"v_mfma_f32_16x16x32_bf16 v[192:195], v[136:139], v[112:115], v[192:195]\n"
"s_waitcnt lgkmcnt(4)\n"
"v_mfma_f32_16x16x32_bf16 v[196:199], v[140:143], v[96:99], v[196:199]\n"
"ds_read_b64_tr_b16 v[136:137], v20 offset:40960\n"
"ds_read_b64_tr_b16 v[138:139], v20 offset:45056\n"
"v_mfma_f32_16x16x32_bf16 v[200:203], v[140:143], v[100:103], v[200:203]\n"
"ds_read_b128 v[116:119], v19 offset:46080\n"
"v_mfma_f32_16x16x32_bf16 v[204:207], v[140:143], v[104:107], v[204:207]\n"
"ds_read_b128 v[120:123], v19 offset:47104\n"
"v_mfma_f32_16x16x32_bf16 v[208:211], v[140:143], v[108:111], v[208:211]\n"
"ds_read_b128 v[124:127], v19 offset:48128\n"
"s_waitcnt vmcnt(5)\n"
"buffer_load_dwordx4 v11, s[28:31], s65 offen offset:1024 nt lds\n"
"v_mfma_f32_16x16x32_bf16 v[212:215], v[140:143], v[112:115], v[212:215]\n"
"ds_read_b128 v[128:131], v19 offset:49152\n"
"s_waitcnt lgkmcnt(8)\n"
"v_mfma_f32_16x16x32_bf16 v[216:219], v[144:147], v[96:99], v[216:219]\n"
"ds_read_b128 v[132:135], v19 offset:50176\n"
"v_mfma_f32_16x16x32_bf16 v[220:223], v[144:147], v[100:103], v[220:223]\n"
"ds_read_b64_tr_b16 v[140:141], v21 offset:40960\n"
"ds_read_b64_tr_b16 v[142:143], v21 offset:45056\n"
"v_mfma_f32_16x16x32_bf16 v[224:227], v[144:147], v[104:107], v[224:227]\n"
"v_mfma_f32_16x16x32_bf16 v[228:231], v[144:147], v[108:111], v[228:231]\n"
"s_waitcnt vmcnt(5)\n"
"buffer_load_dwordx4 v12, s[28:31], s65 offen offset:2048 nt lds\n"
"v_mfma_f32_16x16x32_bf16 v[232:235], v[144:147], v[112:115], v[232:235]\n"
"s_waitcnt lgkmcnt(9)\n"
"v_mfma_f32_16x16x32_bf16 v[236:239], v[148:151], v[96:99], v[236:239]\n"
"ds_read_b64_tr_b16 v[144:145], v22 offset:40960\n"
"ds_read_b64_tr_b16 v[146:147], v22 offset:45056\n"
"v_mfma_f32_16x16x32_bf16 v[240:243], v[148:151], v[100:103], v[240:243]\n"
"v_mfma_f32_16x16x32_bf16 v[244:247], v[148:151], v[104:107], v[244:247]\n"
"v_mfma_f32_16x16x32_bf16 v[248:251], v[148:151], v[108:111], v[248:251]\n"
"v_mfma_f32_16x16x32_bf16 v[252:255], v[148:151], v[112:115], v[252:255]\n"
"s_waitcnt vmcnt(5)\n"
"buffer_load_dwordx4 v13, s[28:31], s65 offen offset:3072 nt lds\n"
"s_waitcnt lgkmcnt(8)\n"
"v_mfma_f32_16x16x32_bf16 v[176:179], v[136:139], v[116:119], v[176:179]\n"
"ds_read_b64_tr_b16 v[148:149], v23 offset:40960\n"
"ds_read_b64_tr_b16 v[150:151], v23 offset:45056\n"
"s_waitcnt lgkmcnt(9)\n"
"v_mfma_f32_16x16x32_bf16 v[180:183], v[136:139], v[120:123], v[180:183]\n"
"s_waitcnt lgkmcnt(8)\n"
"v_mfma_f32_16x16x32_bf16 v[184:187], v[136:139], v[124:127], v[184:187]\n"
"s_waitcnt lgkmcnt(7)\n"
"v_mfma_f32_16x16x32_bf16 v[188:191], v[136:139], v[128:131], v[188:191]\n"
"s_waitcnt lgkmcnt(6)\n"
"v_mfma_f32_16x16x32_bf16 v[192:195], v[136:139], v[132:135], v[192:195]\n"
"s_waitcnt lgkmcnt(4)\n"
"v_mfma_f32_16x16x32_bf16 v[196:199], v[140:143], v[116:119], v[196:199]\n"
"v_mfma_f32_16x16x32_bf16 v[200:203], v[140:143], v[120:123], v[200:203]\n"
"v_mfma_f32_16x16x32_bf16 v[204:207], v[140:143], v[124:127], v[204:207]\n"
"v_mfma_f32_16x16x32_bf16 v[208:211], v[140:143], v[128:131], v[208:211]\n"
"v_mfma_f32_16x16x32_bf16 v[212:215], v[140:143], v[132:135], v[212:215]\n"
"s_waitcnt vmcnt(4)\n"
"s_waitcnt lgkmcnt(0)\n"
"s_barrier\n"
"v_mfma_f32_16x16x32_bf16 v[216:219], v[144:147], v[116:119], v[216:219]\n"
"ds_read_b64_tr_b16 v[136:137], v24 offset:32768\n"
"ds_read_b64_tr_b16 v[138:139], v24 offset:36864\n"
"v_mfma_f32_16x16x32_bf16 v[220:223], v[144:147], v[120:123], v[220:223]\n"
"ds_read_b128 v[96:99], v19 offset:0\n"
"ds_read_b128 v[100:103], v19 offset:1024\n"
"s_add_u32 m0, s79, 110592\n"
"s_add_u32 s65, s79, 81920\n"
"buffer_load_dwordx4 v14, s[32:35], s65 offen offset:0 nt lds\n"
"v_mfma_f32_16x16x32_bf16 v[224:227], v[144:147], v[124:127], v[224:227]\n"
"ds_read_b128 v[104:107], v19 offset:2048\n"
"ds_read_b128 v[108:111], v19 offset:3072\n"
"v_mfma_f32_16x16x32_bf16 v[228:231], v[144:147], v[128:131], v[228:231]\n"
"ds_read_b128 v[112:115], v19 offset:4096\n"
"ds_read_b64_tr_b16 v[140:141], v25 offset:32768\n"
"v_mfma_f32_16x16x32_bf16 v[232:235], v[144:147], v[132:135], v[232:235]\n"
"ds_read_b64_tr_b16 v[142:143], v25 offset:36864\n"
"v_mfma_f32_16x16x32_bf16 v[236:239], v[148:151], v[116:119], v[236:239]\n"
"v_mfma_f32_16x16x32_bf16 v[240:243], v[148:151], v[120:123], v[240:243]\n"
"buffer_load_dwordx4 v15, s[32:35], s65 offen offset:1024 nt lds\n"
"v_mfma_f32_16x16x32_bf16 v[244:247], v[148:151], v[124:127], v[244:247]\n"
"v_mfma_f32_16x16x32_bf16 v[248:251], v[148:151], v[128:131], v[248:251]\n"
"v_mfma_f32_16x16x32_bf16 v[252:255], v[148:151], v[132:135], v[252:255]\n"
"s_waitcnt lgkmcnt(6)\n"
"v_mfma_f32_16x16x32_bf16 v[176:179], v[136:139], v[96:99], v[176:179]\n"
"ds_read_b64_tr_b16 v[144:145], v26 offset:32768\n"
"ds_read_b64_tr_b16 v[146:147], v26 offset:36864\n"
"s_waitcnt lgkmcnt(7)\n"
"v_mfma_f32_16x16x32_bf16 v[180:183], v[136:139], v[100:103], v[180:183]\n"
"s_waitcnt lgkmcnt(6)\n"
"v_mfma_f32_16x16x32_bf16 v[184:187], v[136:139], v[104:107], v[184:187]\n"
"ds_read_b64_tr_b16 v[148:149], v27 offset:32768\n"
"ds_read_b64_tr_b16 v[150:151], v27 offset:36864\n"
"s_waitcnt vmcnt(5)\n"
"buffer_load_dwordx4 v16, s[32:35], s65 offen offset:2048 nt lds\n"
"s_waitcnt lgkmcnt(7)\n"
"v_mfma_f32_16x16x32_bf16 v[188:191], v[136:139], v[108:111], v[188:191]\n"
"s_waitcnt lgkmcnt(6)\n"
"v_mfma_f32_16x16x32_bf16 v[192:195], v[136:139], v[112:115], v[192:195]\n"
"s_waitcnt lgkmcnt(4)\n"
"v_mfma_f32_16x16x32_bf16 v[196:199], v[140:143], v[96:99], v[196:199]\n"
"ds_read_b64_tr_b16 v[136:137], v24 offset:40960\n"
"ds_read_b64_tr_b16 v[138:139], v24 offset:45056\n"
"v_mfma_f32_16x16x32_bf16 v[200:203], v[140:143], v[100:103], v[200:203]\n"
"ds_read_b128 v[116:119], v19 offset:5120\n"
"v_mfma_f32_16x16x32_bf16 v[204:207], v[140:143], v[104:107], v[204:207]\n"
"ds_read_b128 v[120:123], v19 offset:6144\n"
"v_mfma_f32_16x16x32_bf16 v[208:211], v[140:143], v[108:111], v[208:211]\n"
"ds_read_b128 v[124:127], v19 offset:7168\n"
"s_waitcnt vmcnt(5)\n"
"buffer_load_dwordx4 v17, s[32:35], s65 offen offset:3072 nt lds\n"
"v_mfma_f32_16x16x32_bf16 v[212:215], v[140:143], v[112:115], v[212:215]\n"
"ds_read_b128 v[128:131], v19 offset:8192\n"
"s_waitcnt lgkmcnt(8)\n"
"v_mfma_f32_16x16x32_bf16 v[216:219], v[144:147], v[96:99], v[216:219]\n"
"ds_read_b128 v[132:135], v19 offset:9216\n"
"v_mfma_f32_16x16x32_bf16 v[220:223], v[144:147], v[100:103], v[220:223]\n"
"ds_read_b64_tr_b16 v[140:141], v25 offset:40960\n"
"ds_read_b64_tr_b16 v[142:143], v25 offset:45056\n"
"v_mfma_f32_16x16x32_bf16 v[224:227], v[144:147], v[104:107], v[224:227]\n"
"v_mfma_f32_16x16x32_bf16 v[228:231], v[144:147], v[108:111], v[228:231]\n"
"s_add_u32 m0, s79, 126976\n"
"s_add_u32 s65, s79, 98304\n"
"s_waitcnt vmcnt(5)\n"
"buffer_load_dwordx4 v14, s[32:35], s65 offen offset:0 nt lds\n"
"v_mfma_f32_16x16x32_bf16 v[232:235], v[144:147], v[112:115], v[232:235]\n"
"s_waitcnt lgkmcnt(9)\n"
"v_mfma_f32_16x16x32_bf16 v[236:239], v[148:151], v[96:99], v[236:239]\n"
"ds_read_b64_tr_b16 v[144:145], v26 offset:40960\n"
"ds_read_b64_tr_b16 v[146:147], v26 offset:45056\n"
"v_mfma_f32_16x16x32_bf16 v[240:243], v[148:151], v[100:103], v[240:243]\n"
"v_mfma_f32_16x16x32_bf16 v[244:247], v[148:151], v[104:107], v[244:247]\n"
"v_mfma_f32_16x16x32_bf16 v[248:251], v[148:151], v[108:111], v[248:251]\n"
"v_mfma_f32_16x16x32_bf16 v[252:255], v[148:151], v[112:115], v[252:255]\n"
"s_waitcnt vmcnt(5)\n"
"buffer_load_dwordx4 v15, s[32:35], s65 offen offset:1024 nt lds\n"
"s_waitcnt lgkmcnt(8)\n"
"v_mfma_f32_16x16x32_bf16 v[176:179], v[136:139], v[116:119], v[176:179]\n"
"ds_read_b64_tr_b16 v[148:149], v27 offset:40960\n"
"ds_read_b64_tr_b16 v[150:151], v27 offset:45056\n"
"s_waitcnt lgkmcnt(9)\n"
"v_mfma_f32_16x16x32_bf16 v[180:183], v[136:139], v[120:123], v[180:183]\n"
"s_waitcnt lgkmcnt(8)\n"
"v_mfma_f32_16x16x32_bf16 v[184:187], v[136:139], v[124:127], v[184:187]\n"
"s_waitcnt lgkmcnt(7)\n"
"v_mfma_f32_16x16x32_bf16 v[188:191], v[136:139], v[128:131], v[188:191]\n"
"s_waitcnt lgkmcnt(6)\n"
"v_mfma_f32_16x16x32_bf16 v[192:195], v[136:139], v[132:135], v[192:195]\n"
"s_waitcnt lgkmcnt(4)\n"
"v_mfma_f32_16x16x32_bf16 v[196:199], v[140:143], v[116:119], v[196:199]\n"
"v_mfma_f32_16x16x32_bf16 v[200:203], v[140:143], v[120:123], v[200:203]\n"
"v_mfma_f32_16x16x32_bf16 v[204:207], v[140:143], v[124:127], v[204:207]\n"
"v_mfma_f32_16x16x32_bf16 v[208:211], v[140:143], v[128:131], v[208:211]\n"
"v_mfma_f32_16x16x32_bf16 v[212:215], v[140:143], v[132:135], v[212:215]\n"
"s_waitcnt vmcnt(2)\n"
"s_waitcnt lgkmcnt(0)\n"
"s_barrier\n"
"v_mfma_f32_16x16x32_bf16 v[216:219], v[144:147], v[116:119], v[216:219]\n"
"ds_read_b64_tr_b16 v[136:137], v20 offset:0\n"
"ds_read_b64_tr_b16 v[138:139], v20 offset:4096\n"
"v_mfma_f32_16x16x32_bf16 v[220:223], v[144:147], v[120:123], v[220:223]\n"
"ds_read_b128 v[96:99], v19 offset:40960\n"
"ds_read_b128 v[100:103], v19 offset:41984\n"
"s_add_u32 m0, s79, 126976\n"
"s_add_u32 s65, s79, 98304\n"
"buffer_load_dwordx4 v16, s[32:35], s65 offen offset:2048 nt lds\n"
"v_mfma_f32_16x16x32_bf16 v[224:227], v[144:147], v[124:127], v[224:227]\n"
"ds_read_b128 v[104:107], v19 offset:43008\n"
"ds_read_b128 v[108:111], v19 offset:44032\n"
"v_mfma_f32_16x16x32_bf16 v[228:231], v[144:147], v[128:131], v[228:231]\n"
"ds_read_b128 v[112:115], v19 offset:45056\n"
"ds_read_b64_tr_b16 v[140:141], v21 offset:0\n"
"buffer_load_dwordx4 v17, s[32:35], s65 offen offset:3072 nt lds\n"
"v_mfma_f32_16x16x32_bf16 v[232:235], v[144:147], v[132:135], v[232:235]\n"
"ds_read_b64_tr_b16 v[142:143], v21 offset:4096\n"
"v_mfma_f32_16x16x32_bf16 v[236:239], v[148:151], v[116:119], v[236:239]\n"
"s_add_u32 m0, s79, 143360\n"
"s_add_u32 s65, s79, 114688\n"
"buffer_load_dwordx4 v14, s[32:35], s65 offen offset:0 nt lds\n"
"v_mfma_f32_16x16x32_bf16 v[240:243], v[148:151], v[120:123], v[240:243]\n"
"v_mfma_f32_16x16x32_bf16 v[244:247], v[148:151], v[124:127], v[244:247]\n"
"buffer_load_dwordx4 v15, s[32:35], s65 offen offset:1024 nt lds\n"
"v_mfma_f32_16x16x32_bf16 v[248:251], v[148:151], v[128:131], v[248:251]\n"
"v_mfma_f32_16x16x32_bf16 v[252:255], v[148:151], v[132:135], v[252:255]\n"
"s_waitcnt lgkmcnt(6)\n"
"v_mfma_f32_16x16x32_bf16 v[176:179], v[136:139], v[96:99], v[176:179]\n"
"ds_read_b64_tr_b16 v[144:145], v22 offset:0\n"
"ds_read_b64_tr_b16 v[146:147], v22 offset:4096\n"
"s_waitcnt vmcnt(5)\n"
"buffer_load_dwordx4 v16, s[32:35], s65 offen offset:2048 nt lds\n"
"s_waitcnt lgkmcnt(7)\n"
"v_mfma_f32_16x16x32_bf16 v[180:183], v[136:139], v[100:103], v[180:183]\n"
"s_waitcnt lgkmcnt(6)\n"
"v_mfma_f32_16x16x32_bf16 v[184:187], v[136:139], v[104:107], v[184:187]\n"
"ds_read_b64_tr_b16 v[148:149], v23 offset:0\n"
"ds_read_b64_tr_b16 v[150:151], v23 offset:4096\n"
"s_waitcnt vmcnt(5)\n"
"buffer_load_dwordx4 v17, s[32:35], s65 offen offset:3072 nt lds\n"
"s_waitcnt lgkmcnt(7)\n"
"v_mfma_f32_16x16x32_bf16 v[188:191], v[136:139], v[108:111], v[188:191]\n"
"s_waitcnt lgkmcnt(6)\n"
"v_mfma_f32_16x16x32_bf16 v[192:195], v[136:139], v[112:115], v[192:195]\n"
"s_bfe_u32 s61, s20, 262148\n"
"s_lshl_b32 s61, s61, 17\n"
"s_lshl_b32 s62, s78, 15\n"
"s_add_u32 s65, s61, s62\n"
"s_add_u32 m0, s79, 77824\n"
"s_nop 0\n"
"buffer_load_dwordx4 v6, s[36:39], s65 offen offset:0  lds\n"
"s_waitcnt lgkmcnt(4)\n"
"v_mfma_f32_16x16x32_bf16 v[196:199], v[140:143], v[96:99], v[196:199]\n"
"ds_read_b64_tr_b16 v[136:137], v20 offset:8192\n"
"ds_read_b64_tr_b16 v[138:139], v20 offset:12288\n"
"v_mfma_f32_16x16x32_bf16 v[200:203], v[140:143], v[100:103], v[200:203]\n"
"ds_read_b128 v[116:119], v19 offset:46080\n"
"buffer_load_dwordx4 v7, s[36:39], s65 offen offset:1024  lds\n"
"v_mfma_f32_16x16x32_bf16 v[204:207], v[140:143], v[104:107], v[204:207]\n"
"ds_read_b128 v[120:123], v19 offset:47104\n"
"v_mfma_f32_16x16x32_bf16 v[208:211], v[140:143], v[108:111], v[208:211]\n"
"ds_read_b128 v[124:127], v19 offset:48128\n"
"v_mfma_f32_16x16x32_bf16 v[212:215], v[140:143], v[112:115], v[212:215]\n"
"ds_read_b128 v[128:131], v19 offset:49152\n"
"buffer_load_dwordx4 v8, s[36:39], s65 offen offset:2048  lds\n"
"s_waitcnt lgkmcnt(8)\n"
"v_mfma_f32_16x16x32_bf16 v[216:219], v[144:147], v[96:99], v[216:219]\n"
"ds_read_b128 v[132:135], v19 offset:50176\n"
"v_mfma_f32_16x16x32_bf16 v[220:223], v[144:147], v[100:103], v[220:223]\n"
"ds_read_b64_tr_b16 v[140:141], v21 offset:8192\n"
"ds_read_b64_tr_b16 v[142:143], v21 offset:12288\n"
"buffer_load_dwordx4 v9, s[36:39], s65 offen offset:3072  lds\n"
"v_mfma_f32_16x16x32_bf16 v[224:227], v[144:147], v[104:107], v[224:227]\n"
"v_mfma_f32_16x16x32_bf16 v[228:231], v[144:147], v[108:111], v[228:231]\n"
"s_bfe_u32 s61, s20, 262152\n"
"s_lshl_b32 s61, s61, 17\n"
"s_lshl_b32 s62, s78, 15\n"
"s_add_u32 s65, s61, s62\n"
"s_add_u32 m0, s79, 94208\n"
"s_nop 0\n"
"buffer_load_dwordx4 v6, s[36:39], s65 offen offset:0  lds\n"
"v_mfma_f32_16x16x32_bf16 v[232:235], v[144:147], v[112:115], v[232:235]\n"
"s_waitcnt lgkmcnt(9)\n"
"v_mfma_f32_16x16x32_bf16 v[236:239], v[148:151], v[96:99], v[236:239]\n"
"ds_read_b64_tr_b16 v[144:145], v22 offset:8192\n"
"ds_read_b64_tr_b16 v[146:147], v22 offset:12288\n"
"buffer_load_dwordx4 v7, s[36:39], s65 offen offset:1024  lds\n"
"v_mfma_f32_16x16x32_bf16 v[240:243], v[148:151], v[100:103], v[240:243]\n"
"v_mfma_f32_16x16x32_bf16 v[244:247], v[148:151], v[104:107], v[244:247]\n"
"v_mfma_f32_16x16x32_bf16 v[248:251], v[148:151], v[108:111], v[248:251]\n"
"buffer_load_dwordx4 v8, s[36:39], s65 offen offset:2048  lds\n"
"v_mfma_f32_16x16x32_bf16 v[252:255], v[148:151], v[112:115], v[252:255]\n"
"s_waitcnt lgkmcnt(8)\n"
"v_mfma_f32_16x16x32_bf16 v[176:179], v[136:139], v[116:119], v[176:179]\n"
"ds_read_b64_tr_b16 v[148:149], v23 offset:8192\n"
"ds_read_b64_tr_b16 v[150:151], v23 offset:12288\n"
"buffer_load_dwordx4 v9, s[36:39], s65 offen offset:3072  lds\n"
"s_waitcnt lgkmcnt(9)\n"
"v_mfma_f32_16x16x32_bf16 v[180:183], v[136:139], v[120:123], v[180:183]\n"
"s_waitcnt lgkmcnt(8)\n"
"v_mfma_f32_16x16x32_bf16 v[184:187], v[136:139], v[124:127], v[184:187]\n"
"s_getpc_b64 s[74:75]\n"
".Lk_pc_39_%=:\n"
"s_add_u32 s74, s74, .Lk_start_%=-.Lk_pc_39_%=\n"
"s_addc_u32 s75, s75, -1\n"
"s_mov_b32 s62, .Lk_end_%=-.Lk_start_%=-4\n"
"s_lshl_b32 s61, s60, 7\n"
"s_lshl_b32 s63, s78, 14\n"
"s_add_u32 s61, s61, s63\n"
"v_lshl_add_u32 v52, v0, 12, s61\n"
"v_min_u32 v52, s62, v52\n"
"v_and_b32 v52, -4, v52\n"
"s_mov_b64 exec, 0xf\n"
"global_load_dword v55, v52, s[74:75]\n"
"s_mov_b64 exec, -1\n"
"s_waitcnt lgkmcnt(7)\n"
"v_mfma_f32_16x16x32_bf16 v[188:191], v[136:139], v[128:131], v[188:191]\n"
"s_waitcnt lgkmcnt(6)\n"
"v_mfma_f32_16x16x32_bf16 v[192:195], v[136:139], v[132:135], v[192:195]\n"
"s_waitcnt lgkmcnt(4)\n"
"v_mfma_f32_16x16x32_bf16 v[196:199], v[140:143], v[116:119], v[196:199]\n"
"v_mfma_f32_16x16x32_bf16 v[200:203], v[140:143], v[120:123], v[200:203]\n"
"v_mfma_f32_16x16x32_bf16 v[204:207], v[140:143], v[124:127], v[204:207]\n"
"v_mfma_f32_16x16x32_bf16 v[208:211], v[140:143], v[128:131], v[208:211]\n"
"v_mfma_f32_16x16x32_bf16 v[212:215], v[140:143], v[132:135], v[212:215]\n"
"s_waitcnt vmcnt(13)\n"
"s_waitcnt lgkmcnt(0)\n"
"s_barrier\n"
"v_mfma_f32_16x16x32_bf16 v[216:219], v[144:147], v[116:119], v[216:219]\n"
"ds_read_b64_tr_b16 v[136:137], v20 offset:16384\n"
"ds_read_b64_tr_b16 v[138:139], v20 offset:20480\n"
"v_mfma_f32_16x16x32_bf16 v[220:223], v[144:147], v[120:123], v[220:223]\n"
"ds_read_b128 v[96:99], v19 offset:0\n"
"ds_read_b128 v[100:103], v19 offset:1024\n"
"s_bfe_u32 s61, s20, 262144\n"
"s_lshl_b32 s61, s61, 17\n"
"s_lshl_b32 s62, s78, 15\n"
"s_add_u32 s65, s61, s62\n"
"s_add_u32 m0, s79, 61440\n"
"s_nop 0\n"
"buffer_load_dwordx4 v6, s[36:39], s65 offen offset:0  lds\n"
"v_mfma_f32_16x16x32_bf16 v[224:227], v[144:147], v[124:127], v[224:227]\n"
"ds_read_b128 v[104:107], v19 offset:2048\n"
"ds_read_b128 v[108:111], v19 offset:3072\n"
"v_mfma_f32_16x16x32_bf16 v[228:231], v[144:147], v[128:131], v[228:231]\n"
"ds_read_b128 v[112:115], v19 offset:4096\n"
"ds_read_b64_tr_b16 v[140:141], v21 offset:16384\n"
"v_mfma_f32_16x16x32_bf16 v[232:235], v[144:147], v[132:135], v[232:235]\n"
"ds_read_b64_tr_b16 v[142:143], v21 offset:20480\n"
"buffer_load_dwordx4 v7, s[36:39], s65 offen offset:1024  lds\n"
"v_mfma_f32_16x16x32_bf16 v[236:239], v[148:151], v[116:119], v[236:239]\n"
"v_mfma_f32_16x16x32_bf16 v[240:243], v[148:151], v[120:123], v[240:243]\n"
"v_mfma_f32_16x16x32_bf16 v[244:247], v[148:151], v[124:127], v[244:247]\n"
"v_mfma_f32_16x16x32_bf16 v[248:251], v[148:151], v[128:131], v[248:251]\n"
"buffer_load_dwordx4 v8, s[36:39], s65 offen offset:2048  lds\n"
"v_mfma_f32_16x16x32_bf16 v[252:255], v[148:151], v[132:135], v[252:255]\n"
"s_waitcnt lgkmcnt(6)\n"
"v_mfma_f32_16x16x32_bf16 v[176:179], v[136:139], v[96:99], v[176:179]\n"
"ds_read_b64_tr_b16 v[144:145], v22 offset:16384\n"
"ds_read_b64_tr_b16 v[146:147], v22 offset:20480\n"
"s_waitcnt lgkmcnt(7)\n"
"v_mfma_f32_16x16x32_bf16 v[180:183], v[136:139], v[100:103], v[180:183]\n"
"s_waitcnt lgkmcnt(6)\n"
"v_mfma_f32_16x16x32_bf16 v[184:187], v[136:139], v[104:107], v[184:187]\n"
"ds_read_b64_tr_b16 v[148:149], v23 offset:16384\n"
"ds_read_b64_tr_b16 v[150:151], v23 offset:20480\n"
"buffer_load_dwordx4 v9, s[36:39], s65 offen offset:3072  lds\n"
"s_waitcnt lgkmcnt(7)\n"
"v_mfma_f32_16x16x32_bf16 v[188:191], v[136:139], v[108:111], v[188:191]\n"
"s_waitcnt lgkmcnt(6)\n"
"v_mfma_f32_16x16x32_bf16 v[192:195], v[136:139], v[112:115], v[192:195]\n"
"s_waitcnt lgkmcnt(4)\n"
"v_mfma_f32_16x16x32_bf16 v[196:199], v[140:143], v[96:99], v[196:199]\n"
"ds_read_b64_tr_b16 v[136:137], v20 offset:24576\n"
"ds_read_b64_tr_b16 v[138:139], v20 offset:28672\n"
"v_mfma_f32_16x16x32_bf16 v[200:203], v[140:143], v[100:103], v[200:203]\n"
"ds_read_b128 v[116:119], v19 offset:5120\n"
"s_bfe_u32 s61, s20, 262156\n"
"s_lshl_b32 s61, s61, 17\n"
"s_lshl_b32 s62, s78, 15\n"
"s_add_u32 s65, s61, s62\n"
"s_add_u32 m0, s79, 110592\n"
"s_nop 0\n"
"buffer_load_dwordx4 v6, s[36:39], s65 offen offset:0  lds\n"
"v_mfma_f32_16x16x32_bf16 v[204:207], v[140:143], v[104:107], v[204:207]\n"
"ds_read_b128 v[120:123], v19 offset:6144\n"
"v_mfma_f32_16x16x32_bf16 v[208:211], v[140:143], v[108:111], v[208:211]\n"
"ds_read_b128 v[124:127], v19 offset:7168\n"
"v_mfma_f32_16x16x32_bf16 v[212:215], v[140:143], v[112:115], v[212:215]\n"
"ds_read_b128 v[128:131], v19 offset:8192\n"
"buffer_load_dwordx4 v7, s[36:39], s65 offen offset:1024  lds\n"
"s_waitcnt lgkmcnt(8)\n"
"v_mfma_f32_16x16x32_bf16 v[216:219], v[144:147], v[96:99], v[216:219]\n"
"ds_read_b128 v[132:135], v19 offset:9216\n"
"v_mfma_f32_16x16x32_bf16 v[220:223], v[144:147], v[100:103], v[220:223]\n"
"ds_read_b64_tr_b16 v[140:141], v21 offset:24576\n"
"ds_read_b64_tr_b16 v[142:143], v21 offset:28672\n"
"v_mfma_f32_16x16x32_bf16 v[224:227], v[144:147], v[104:107], v[224:227]\n"
"v_mfma_f32_16x16x32_bf16 v[228:231], v[144:147], v[108:111], v[228:231]\n"
"buffer_load_dwordx4 v8, s[36:39], s65 offen offset:2048  lds\n"
"v_mfma_f32_16x16x32_bf16 v[232:235], v[144:147], v[112:115], v[232:235]\n"
"s_waitcnt lgkmcnt(9)\n"
"v_mfma_f32_16x16x32_bf16 v[236:239], v[148:151], v[96:99], v[236:239]\n"
"ds_read_b64_tr_b16 v[144:145], v22 offset:24576\n"
"ds_read_b64_tr_b16 v[146:147], v22 offset:28672\n"
"v_mfma_f32_16x16x32_bf16 v[240:243], v[148:151], v[100:103], v[240:243]\n"
"v_mfma_f32_16x16x32_bf16 v[244:247], v[148:151], v[104:107], v[244:247]\n"
"buffer_load_dwordx4 v9, s[36:39], s65 offen offset:3072  lds\n"
"v_mfma_f32_16x16x32_bf16 v[248:251], v[148:151], v[108:111], v[248:251]\n"
"v_mfma_f32_16x16x32_bf16 v[252:255], v[148:151], v[112:115], v[252:255]\n"
"s_waitcnt lgkmcnt(8)\n"
"v_mfma_f32_16x16x32_bf16 v[176:179], v[136:139], v[116:119], v[176:179]\n"
"ds_read_b64_tr_b16 v[148:149], v23 offset:24576\n"
"ds_read_b64_tr_b16 v[150:151], v23 offset:28672\n"
"s_waitcnt lgkmcnt(9)\n"
"v_mfma_f32_16x16x32_bf16 v[180:183], v[136:139], v[120:123], v[180:183]\n"
"global_load_dword v55, v35, s[76:77]\n"
"s_waitcnt lgkmcnt(8)\n"
"v_mfma_f32_16x16x32_bf16 v[184:187], v[136:139], v[124:127], v[184:187]\n"
"s_waitcnt lgkmcnt(7)\n"
"v_mfma_f32_16x16x32_bf16 v[188:191], v[136:139], v[128:131], v[188:191]\n"
"s_waitcnt lgkmcnt(6)\n"
"v_mfma_f32_16x16x32_bf16 v[192:195], v[136:139], v[132:135], v[192:195]\n"
"s_waitcnt lgkmcnt(4)\n"
"v_mfma_f32_16x16x32_bf16 v[196:199], v[140:143], v[116:119], v[196:199]\n"
"v_mfma_f32_16x16x32_bf16 v[200:203], v[140:143], v[120:123], v[200:203]\n"
"v_mfma_f32_16x16x32_bf16 v[204:207], v[140:143], v[124:127], v[204:207]\n"
"v_mfma_f32_16x16x32_bf16 v[208:211], v[140:143], v[128:131], v[208:211]\n"
"v_mfma_f32_16x16x32_bf16 v[212:215], v[140:143], v[132:135], v[212:215]\n"
"s_waitcnt vmcnt(18)\n"
"s_waitcnt lgkmcnt(0)\n"
"s_barrier\n"
"s_setprio 2\n"
"ds_read2_b32 v[40:41], v32 offset0:0 offset1:160\n"
"ds_read2_b32 v[42:43], v32 offset0:32 offset1:192\n"
"ds_read2_b32 v[44:45], v32 offset0:64 offset1:224\n"
"ds_read_b32 v46, v32 offset:384\n"
"ds_read_b32 v47, v32 offset:1024\n"
"ds_read_b32 v48, v32 offset:512\n"
"ds_read_b32 v49, v32 offset:1152\n"
"s_lshl_b32 s61, s78, 2\n"
"s_add_u32 s61, s61, 162384\n"
"v_mov_b32 v54, s61\n"
"v_mfma_f32_16x16x32_bf16 v[216:219], v[144:147], v[116:119], v[216:219]\n"
"ds_read_b128 v[96:99], v19 offset:40960\n"
"ds_read_b64_tr_b16 v[56:57], v20 offset:32768\n"
"v_mfma_f32_16x16x32_bf16 v[220:223], v[144:147], v[120:123], v[220:223]\n"
"ds_read_b64_tr_b16 v[58:59], v20 offset:36864\n"
"ds_read_b64_tr_b16 v[60:61], v21 offset:32768\n"
"v_mfma_f32_16x16x32_bf16 v[224:227], v[144:147], v[124:127], v[224:227]\n"
"ds_read_b64_tr_b16 v[62:63], v21 offset:36864\n"
"ds_read_b64_tr_b16 v[64:65], v22 offset:32768\n"
"v_mfma_f32_16x16x32_bf16 v[228:231], v[144:147], v[128:131], v[228:231]\n"
"ds_read_b64_tr_b16 v[66:67], v22 offset:36864\n"
"ds_read_b64_tr_b16 v[68:69], v23 offset:32768\n"
"s_waitcnt lgkmcnt(14)\n"
"v_add_f32 v40, v40, v41\n"
"s_waitcnt lgkmcnt(13)\n"
"v_add_f32 v42, v42, v43\n"
"v_mfma_f32_16x16x32_bf16 v[232:235], v[144:147], v[132:135], v[232:235]\n"
"ds_read_b64_tr_b16 v[70:71], v23 offset:36864\n"
"ds_read_b128 v[152:155], v19 offset:46080\n"
"s_waitcnt lgkmcnt(14)\n"
"v_add_f32 v44, v44, v45\n"
"s_waitcnt lgkmcnt(12)\n"
"v_add_f32 v46, v46, v47\n"
"v_mfma_f32_16x16x32_bf16 v[236:239], v[148:151], v[116:119], v[236:239]\n"
"ds_read_b64_tr_b16 v[72:73], v20 offset:40960\n"
"ds_read_b64_tr_b16 v[74:75], v20 offset:45056\n"
"s_waitcnt lgkmcnt(12)\n"
"v_add_f32 v48, v48, v49\n"
"v_mov_b32 v41, v40\n"
"v_mfma_f32_16x16x32_bf16 v[240:243], v[148:151], v[120:123], v[240:243]\n"
"ds_read_b64_tr_b16 v[76:77], v21 offset:40960\n"
"ds_read_b64_tr_b16 v[78:79], v21 offset:45056\n"
"v_mov_b32 v43, v42\n"
"v_mov_b32 v45, v44\n"
"v_mfma_f32_16x16x32_bf16 v[244:247], v[148:151], v[124:127], v[244:247]\n"
"ds_read_b64_tr_b16 v[80:81], v22 offset:40960\n"
"ds_read_b64_tr_b16 v[82:83], v22 offset:45056\n"
"v_mov_b32 v47, v46\n"
"v_mov_b32 v49, v48\n"
"v_mfma_f32_16x16x32_bf16 v[248:251], v[148:151], v[128:131], v[248:251]\n"
"ds_read_b64_tr_b16 v[84:85], v23 offset:40960\n"
"ds_read_b64_tr_b16 v[86:87], v23 offset:45056\n"
"v_permlane16_swap_b32 v40, v41\n"
"v_permlane16_swap_b32 v42, v43\n"
"v_mfma_f32_16x16x32_bf16 v[252:255], v[148:151], v[132:135], v[252:255]\n"
"v_permlane16_swap_b32 v44, v45\n"
"v_permlane16_swap_b32 v46, v47\n"
"s_waitcnt lgkmcnt(15)\n"
"v_mfma_f32_16x16x32_bf16 v[176:179], v[56:59], v[96:99], v[176:179]\n"
"ds_read_b128 v[100:103], v19 offset:41984\n"
"v_permlane16_swap_b32 v48, v49\n"
"v_add_f32 v40, v40, v41\n"
"v_add_f32 v42, v42, v43\n"
"v_add_f32 v44, v44, v45\n"
"s_waitcnt lgkmcnt(14)\n"
"v_mfma_f32_16x16x32_bf16 v[196:199], v[60:63], v[96:99], v[196:199]\n"
"ds_read_b128 v[156:159], v19 offset:47104\n"
"v_add_f32 v46, v46, v47\n"
"v_add_f32 v48, v48, v49\n"
"v_add_f32 v50, v40, v42\n"
"v_add_f32 v51, v44, v46\n"
"s_waitcnt lgkmcnt(13)\n"
"v_mfma_f32_16x16x32_bf16 v[216:219], v[64:67], v[96:99], v[216:219]\n"
"ds_read_b128 v[104:107], v19 offset:43008\n"
"v_min3_f32 v52, v40, v42, v44\n"
"v_add_f32 v50, v50, v48\n"
"v_min3_f32 v52, v52, v46, v48\n"
"v_add_f32 v50, v50, v51\n"
"s_waitcnt lgkmcnt(12)\n"
"v_mfma_f32_16x16x32_bf16 v[236:239], v[68:71], v[96:99], v[236:239]\n"
"ds_read_b128 v[160:163], v19 offset:48128\n"
"s_mov_b32 s61, 0x2b800000\n"
"s_mov_b32 s62, 0x53800000\n"
"v_cmp_nle_f32 vcc, s61, v52\n"
"v_cmp_nge_f32 s[74:75], s62, v50\n"
"s_waitcnt lgkmcnt(10)\n"
"v_mfma_f32_16x16x32_bf16 v[176:179], v[72:75], v[152:155], v[176:179]\n"
"ds_read_b128 v[108:111], v19 offset:44032\n"
"v_mov_b32 v50, 162372\n"
"v_mov_b32 v51, 1\n"
"s_or_b64 vcc, vcc, s[74:75]\n"
"s_cbranch_scc0 .Lk_ok_40_%=\n"
"ds_write_b32 v50, v51\n"
"s_waitcnt lgkmcnt(0)\n"
".Lk_ok_40_%=:\n"
"s_waitcnt lgkmcnt(9)\n"
"v_mfma_f32_16x16x32_bf16 v[196:199], v[76:79], v[152:155], v[196:199]\n"
"ds_read_b128 v[164:167], v19 offset:49152\n"
"v_rcp_f32 v40, v40\n"
"v_rcp_f32 v42, v42\n"
"v_rcp_f32 v44, v44\n"
"v_rcp_f32 v46, v46\n"
"s_waitcnt lgkmcnt(8)\n"
"v_mfma_f32_16x16x32_bf16 v[216:219], v[80:83], v[152:155], v[216:219]\n"
"ds_read_b128 v[112:115], v19 offset:45056\n"
"v_rcp_f32 v48, v48\n"
"s_waitcnt lgkmcnt(7)\n"
"v_mfma_f32_16x16x32_bf16 v[236:239], v[84:87], v[152:155], v[236:239]\n"
"ds_read_b128 v[168:171], v19 offset:50176\n"
"s_waitcnt lgkmcnt(7)\n"
"v_mfma_f32_16x16x32_bf16 v[180:183], v[56:59], v[100:103], v[180:183]\n"
"s_barrier\n"
"ds_read_b32 v51, v50\n"
"v_mfma_f32_16x16x32_bf16 v[200:203], v[60:63], v[100:103], v[200:203]\n"
"v_mul_f32 v176, v40, v176\n"
"v_mul_f32 v177, v40, v177\n"
"v_mul_f32 v178, v40, v178\n"
"v_mul_f32 v179, v40, v179\n"
"v_mfma_f32_16x16x32_bf16 v[220:223], v[64:67], v[100:103], v[220:223]\n"
"v_mul_f32 v196, v40, v196\n"
"v_mul_f32 v197, v40, v197\n"
"v_mul_f32 v198, v40, v198\n"
"v_mul_f32 v199, v40, v199\n"
"v_mfma_f32_16x16x32_bf16 v[240:243], v[68:71], v[100:103], v[240:243]\n"
"v_cvt_pk_bf16_f32 v176, v176, v177\n"
"v_cvt_pk_bf16_f32 v177, v178, v179\n"
"v_mul_f32 v216, v40, v216\n"
"v_mul_f32 v217, v40, v217\n"
"s_waitcnt lgkmcnt(7)\n"
"v_mfma_f32_16x16x32_bf16 v[180:183], v[72:75], v[156:159], v[180:183]\n"
"ds_write_b64 v28, v[176:177] offset:0\n"
"v_mul_f32 v218, v40, v218\n"
"v_mul_f32 v219, v40, v219\n"
"v_cvt_pk_bf16_f32 v196, v196, v197\n"
"v_mfma_f32_16x16x32_bf16 v[200:203], v[76:79], v[156:159], v[200:203]\n"
"v_cvt_pk_bf16_f32 v197, v198, v199\n"
"v_mul_f32 v236, v40, v236\n"
"v_mul_f32 v237, v40, v237\n"
"ds_write_b64 v29, v[196:197] offset:0\n"
"v_mfma_f32_16x16x32_bf16 v[220:223], v[80:83], v[156:159], v[220:223]\n"
"s_waitcnt lgkmcnt(2)\n"
"v_readfirstlane_b32 s61, v51\n"
"s_nop 0\n"
"s_cmp_lg_u32 s61, 0\n"
"s_cbranch_scc1 .Lk_SB_5_%=\n"
"v_mul_f32 v238, v40, v238\n"
"v_mul_f32 v239, v40, v239\n"
"v_cvt_pk_bf16_f32 v216, v216, v217\n"
"v_cvt_pk_bf16_f32 v217, v218, v219\n"
"v_mfma_f32_16x16x32_bf16 v[240:243], v[84:87], v[156:159], v[240:243]\n"
"v_cvt_pk_bf16_f32 v236, v236, v237\n"
"ds_write_b64 v30, v[216:217] offset:0\n"
"v_cvt_pk_bf16_f32 v237, v238, v239\n"
"ds_write_b64 v31, v[236:237] offset:0\n"
"v_mov_b32 v53, 1\n"
"ds_write_b32 v54, v53\n"
"v_mfma_f32_16x16x32_bf16 v[184:187], v[56:59], v[104:107], v[184:187]\n"
"v_mfma_f32_16x16x32_bf16 v[204:207], v[60:63], v[104:107], v[204:207]\n"
"v_mul_f32 v180, v42, v180\n"
"v_mul_f32 v181, v42, v181\n"
"v_mul_f32 v182, v42, v182\n"
"v_mul_f32 v183, v42, v183\n"
"v_mfma_f32_16x16x32_bf16 v[224:227], v[64:67], v[104:107], v[224:227]\n"
"v_mul_f32 v200, v42, v200\n"
"v_mul_f32 v201, v42, v201\n"
"v_mul_f32 v202, v42, v202\n"
"v_mul_f32 v203, v42, v203\n"
"v_mfma_f32_16x16x32_bf16 v[244:247], v[68:71], v[104:107], v[244:247]\n"
"v_cvt_pk_bf16_f32 v180, v180, v181\n"
"v_cvt_pk_bf16_f32 v181, v182, v183\n"
"v_mul_f32 v220, v42, v220\n"
"v_mul_f32 v221, v42, v221\n"
"v_mfma_f32_16x16x32_bf16 v[184:187], v[72:75], v[160:163], v[184:187]\n"
"ds_write_b64 v28, v[180:181] offset:2048\n"
"v_mul_f32 v222, v42, v222\n"
"v_mul_f32 v223, v42, v223\n"
"v_cvt_pk_bf16_f32 v200, v200, v201\n"
"v_mfma_f32_16x16x32_bf16 v[204:207], v[76:79], v[160:163], v[204:207]\n"
"v_cvt_pk_bf16_f32 v201, v202, v203\n"
"v_mul_f32 v240, v42, v240\n"
"v_mul_f32 v241, v42, v241\n"
"ds_write_b64 v29, v[200:201] offset:2048\n"
"v_mfma_f32_16x16x32_bf16 v[224:227], v[80:83], v[160:163], v[224:227]\n"
"v_mul_f32 v242, v42, v242\n"
"v_mul_f32 v243, v42, v243\n"
"v_cvt_pk_bf16_f32 v220, v220, v221\n"
"v_cvt_pk_bf16_f32 v221, v222, v223\n"
"v_mfma_f32_16x16x32_bf16 v[244:247], v[84:87], v[160:163], v[244:247]\n"
"v_cvt_pk_bf16_f32 v240, v240, v241\n"
"ds_write_b64 v30, v[220:221] offset:2048\n"
"v_cvt_pk_bf16_f32 v241, v242, v243\n"
"ds_write_b64 v31, v[240:241] offset:2048\n"
"v_mov_b32 v53, 2\n"
"ds_write_b32 v54, v53\n"
"v_mfma_f32_16x16x32_bf16 v[188:191], v[56:59], v[108:111], v[188:191]\n"
"v_mfma_f32_16x16x32_bf16 v[208:211], v[60:63], v[108:111], v[208:211]\n"
"v_mul_f32 v184, v44, v184\n"
"v_mul_f32 v185, v44, v185\n"
"v_mul_f32 v186, v44, v186\n"
"v_mul_f32 v187, v44, v187\n"
"v_mfma_f32_16x16x32_bf16 v[228:231], v[64:67], v[108:111], v[228:231]\n"
"v_mul_f32 v204, v44, v204\n"
"v_mul_f32 v205, v44, v205\n"
"v_mul_f32 v206, v44, v206\n"
"v_mul_f32 v207, v44, v207\n"
"v_mfma_f32_16x16x32_bf16 v[248:251], v[68:71], v[108:111], v[248:251]\n"
"v_cvt_pk_bf16_f32 v184, v184, v185\n"
"v_cvt_pk_bf16_f32 v185, v186, v187\n"
"v_mul_f32 v224, v44, v224\n"
"v_mul_f32 v225, v44, v225\n"
"v_mfma_f32_16x16x32_bf16 v[188:191], v[72:75], v[164:167], v[188:191]\n"
"ds_write_b64 v28, v[184:185] offset:4096\n"
"v_mul_f32 v226, v44, v226\n"
"v_mul_f32 v227, v44, v227\n"
"v_cvt_pk_bf16_f32 v204, v204, v205\n"
"v_mfma_f32_16x16x32_bf16 v[208:211], v[76:79], v[164:167], v[208:211]\n"
"v_cvt_pk_bf16_f32 v205, v206, v207\n"
"v_mul_f32 v244, v44, v244\n"
"v_mul_f32 v245, v44, v245\n"
"ds_write_b64 v29, v[204:205] offset:4096\n"
"v_mfma_f32_16x16x32_bf16 v[228:231], v[80:83], v[164:167], v[228:231]\n"
"v_mul_f32 v246, v44, v246\n"
"v_mul_f32 v247, v44, v247\n"
"v_cvt_pk_bf16_f32 v224, v224, v225\n"
"v_cvt_pk_bf16_f32 v225, v226, v227\n"
"v_mfma_f32_16x16x32_bf16 v[248:251], v[84:87], v[164:167], v[248:251]\n"
"v_cvt_pk_bf16_f32 v244, v244, v245\n"
"ds_write_b64 v30, v[224:225] offset:4096\n"
"v_cvt_pk_bf16_f32 v245, v246, v247\n"
"ds_write_b64 v31, v[244:245] offset:4096\n"
"v_mov_b32 v53, 3\n"
"ds_write_b32 v54, v53\n"
"v_mfma_f32_16x16x32_bf16 v[192:195], v[56:59], v[112:115], v[192:195]\n"
"v_mfma_f32_16x16x32_bf16 v[212:215], v[60:63], v[112:115], v[212:215]\n"
"v_mul_f32 v188, v46, v188\n"
"v_mul_f32 v189, v46, v189\n"
"v_mul_f32 v190, v46, v190\n"
"v_mul_f32 v191, v46, v191\n"
"v_mfma_f32_16x16x32_bf16 v[232:235], v[64:67], v[112:115], v[232:235]\n"
"v_mul_f32 v208, v46, v208\n"
"v_mul_f32 v209, v46, v209\n"
"v_mul_f32 v210, v46, v210\n"
"v_mul_f32 v211, v46, v211\n"
"v_mfma_f32_16x16x32_bf16 v[252:255], v[68:71], v[112:115], v[252:255]\n"
"v_cvt_pk_bf16_f32 v188, v188, v189\n"
"v_cvt_pk_bf16_f32 v189, v190, v191\n"
"v_mul_f32 v228, v46, v228\n"
"v_mul_f32 v229, v46, v229\n"
"v_mfma_f32_16x16x32_bf16 v[192:195], v[72:75], v[168:171], v[192:195]\n"
"ds_write_b64 v28, v[188:189] offset:6144\n"
"v_mul_f32 v230, v46, v230\n"
"v_mul_f32 v231, v46, v231\n"
"v_cvt_pk_bf16_f32 v208, v208, v209\n"
"v_mfma_f32_16x16x32_bf16 v[212:215], v[76:79], v[168:171], v[212:215]\n"
"v_cvt_pk_bf16_f32 v209, v210, v211\n"
"v_mul_f32 v248, v46, v248\n"
"v_mul_f32 v249, v46, v249\n"
"ds_write_b64 v29, v[208:209] offset:6144\n"
"v_mfma_f32_16x16x32_bf16 v[232:235], v[80:83], v[168:171], v[232:235]\n"
"v_mul_f32 v250, v46, v250\n"
"v_mul_f32 v251, v46, v251\n"
"v_cvt_pk_bf16_f32 v228, v228, v229\n"
"v_cvt_pk_bf16_f32 v229, v230, v231\n"
"v_mfma_f32_16x16x32_bf16 v[252:255], v[84:87], v[168:171], v[252:255]\n"
"v_cvt_pk_bf16_f32 v248, v248, v249\n"
"ds_write_b64 v30, v[228:229] offset:6144\n"
"v_cvt_pk_bf16_f32 v249, v250, v251\n"
"ds_write_b64 v31, v[248:249] offset:6144\n"
"v_mov_b32 v53, 4\n"
"ds_write_b32 v54, v53\n"
"v_mul_f32 v192, v48, v192\n"
"v_mul_f32 v193, v48, v193\n"
"v_mul_f32 v194, v48, v194\n"
"v_mul_f32 v195, v48, v195\n"
"v_mul_f32 v212, v48, v212\n"
"v_mul_f32 v213, v48, v213\n"
"v_mul_f32 v214, v48, v214\n"
"v_mul_f32 v215, v48, v215\n"
"v_cvt_pk_bf16_f32 v192, v192, v193\n"
"v_cvt_pk_bf16_f32 v193, v194, v195\n"
"v_mul_f32 v232, v48, v232\n"
"v_mul_f32 v233, v48, v233\n"
"ds_write_b64 v28, v[192:193] offset:8192\n"
"v_mul_f32 v234, v48, v234\n"
"v_mul_f32 v235, v48, v235\n"
"v_cvt_pk_bf16_f32 v212, v212, v213\n"
"v_cvt_pk_bf16_f32 v213, v214, v215\n"
"v_mul_f32 v252, v48, v252\n"
"v_mul_f32 v253, v48, v253\n"
"ds_write_b64 v29, v[212:213] offset:8192\n"
"v_mul_f32 v254, v48, v254\n"
"v_mul_f32 v255, v48, v255\n"
"v_cvt_pk_bf16_f32 v232, v232, v233\n"
"v_cvt_pk_bf16_f32 v233, v234, v235\n"
"v_cvt_pk_bf16_f32 v252, v252, v253\n"
"ds_write_b64 v30, v[232:233] offset:8192\n"
"v_cvt_pk_bf16_f32 v253, v254, v255\n"
"ds_write_b64 v31, v[252:253] offset:8192\n"
".Lk_JB_7_%=:\n"
"s_waitcnt lgkmcnt(0)\n"
"s_waitcnt vmcnt(5)\n"
"s_setprio 0\n"
"s_barrier\n"
"v_mov_b32 v42, 162432\n"
"s_cmp_lg_u32 s78, 0\n"
"s_cbranch_scc1 .Lk_fo_41_%=\n"
"s_bfe_u32 s73, s20, 0x40008\n"
"s_bfe_u32 s72, s20, 0x4000c\n"
"s_mov_b32 s74, 0\n"
"s_bfe_u32 s61, s20, 0x40008\n"
"s_lshl_b32 s61, 3, s61\n"
"s_or_b32 s74, s74, s61\n"
"s_mov_b32 s71, 0\n"
".Lk_p_43_%=:\n"
"s_andn2_b32 s61, s74, s68\n"
"s_cbranch_scc0 .Lk_g_44_%=\n"
"global_load_dword v41, v34, s[52:53] sc1\n"
"s_waitcnt vmcnt(0)\n"
"v_cmp_eq_u32 vcc, s70, v41\n"
"s_nop 0\n"
"s_lshr_b32 s61, vcc_lo, 16\n"
"s_and_b32 s68, s61, vcc_lo\n"
"s_add_u32 s71, s71, 1\n"
"s_cmp_lt_u32 s71, 400000\n"
"s_cbranch_scc1 .Lk_p_43_%=\n"
".Lk_g_44_%=:\n"
"v_mov_b32 v41, 4\n"
"ds_write_b32 v42, v41\n"
"s_mul_i32 s61, s73, 20480\n"
"s_mul_i32 s62, s78, 5120\n"
"s_add_u32 s61, s61, s62\n"
"s_add_u32 m0, s62, 40960\n"
"s_add_u32 s65, s61, 0\n"
"buffer_load_dwordx4 v18, s[40:43], s65 offen sc1 lds\n"
"buffer_load_dwordx4 v18, s[40:43], s65 offen offset:1024 sc1 lds\n"
"buffer_load_dwordx4 v18, s[40:43], s65 offen offset:2048 sc1 lds\n"
"buffer_load_dwordx4 v18, s[40:43], s65 offen offset:3072 sc1 lds\n"
"s_add_u32 m0, s62, 45056\n"
"s_add_u32 s65, s61, 4096\n"
"buffer_load_dwordx4 v18, s[40:43], s65 offen sc1 lds\n"
"s_mul_i32 s61, s72, 20480\n"
"s_mul_i32 s62, s78, 5120\n"
"s_add_u32 s61, s61, s62\n"
"s_add_u32 m0, s62, 126976\n"
"s_add_u32 s65, s61, 0\n"
"buffer_load_dwordx4 v18, s[40:43], s65 offen sc1 lds\n"
"buffer_load_dwordx4 v18, s[40:43], s65 offen offset:1024 sc1 lds\n"
"buffer_load_dwordx4 v18, s[40:43], s65 offen offset:2048 sc1 lds\n"
"buffer_load_dwordx4 v18, s[40:43], s65 offen offset:3072 sc1 lds\n"
"s_add_u32 m0, s62, 131072\n"
"s_add_u32 s65, s61, 4096\n"
"buffer_load_dwordx4 v18, s[40:43], s65 offen sc1 lds\n"
"global_load_dword v43, v34, s[52:53] sc1\n"
"s_waitcnt vmcnt(6)\n"
"s_barrier\n"
"s_waitcnt vmcnt(0)\n"
"v_cmp_eq_u32 vcc, s70, v43\n"
"s_nop 0\n"
"s_lshr_b32 s61, vcc_lo, 16\n"
"s_and_b32 s68, s61, vcc_lo\n"
"s_bfe_u32 s72, s20, 0x40010\n"
"s_mov_b32 s74, 0\n"
"s_bfe_u32 s61, s20, 0x40010\n"
"s_lshl_b32 s61, 3, s61\n"
"s_or_b32 s74, s74, s61\n"
"s_bfe_u32 s61, s20, 0x40018\n"
"s_lshl_b32 s61, 3, s61\n"
"s_or_b32 s74, s74, s61\n"
"s_bfe_u32 s61, s21, 0x40000\n"
"s_lshl_b32 s61, 3, s61\n"
"s_or_b32 s74, s74, s61\n"
"s_bfe_u32 s61, s21, 0x40008\n"
"s_lshl_b32 s61, 3, s61\n"
"s_or_b32 s74, s74, s61\n"
"s_mov_b32 s71, 0\n"
".Lk_p_45_%=:\n"
"s_andn2_b32 s61, s74, s68\n"
"s_cbranch_scc0 .Lk_g_46_%=\n"
"global_load_dword v41, v34, s[52:53] sc1\n"
"s_waitcnt vmcnt(0)\n"
"v_cmp_eq_u32 vcc, s70, v41\n"
"s_nop 0\n"
"s_lshr_b32 s61, vcc_lo, 16\n"
"s_and_b32 s68, s61, vcc_lo\n"
"s_add_u32 s71, s71, 1\n"
"s_cmp_lt_u32 s71, 400000\n"
"s_cbranch_scc1 .Lk_p_45_%=\n"
".Lk_g_46_%=:\n"
"v_mov_b32 v41, 12\n"
"ds_write_b32 v42, v41\n"
"s_mul_i32 s61, s72, 20480\n"
"s_mul_i32 s62, s78, 5120\n"
"s_add_u32 s61, s61, s62\n"
"s_add_u32 m0, s62, 0\n"
"s_add_u32 s65, s61, 0\n"
"buffer_load_dwordx4 v18, s[40:43], s65 offen sc1 lds\n"
"buffer_load_dwordx4 v18, s[40:43], s65 offen offset:1024 sc1 lds\n"
"buffer_load_dwordx4 v18, s[40:43], s65 offen offset:2048 sc1 lds\n"
"buffer_load_dwordx4 v18, s[40:43], s65 offen offset:3072 sc1 lds\n"
"s_add_u32 m0, s62, 4096\n"
"s_add_u32 s65, s61, 4096\n"
"buffer_load_dwordx4 v18, s[40:43], s65 offen sc1 lds\n"
"s_lshl_b32 s61, s72, 17\n"
"s_lshl_b32 s62, s78, 15\n"
"s_add_u32 s61, s61, s62\n"
"s_add_u32 m0, s79, 61440\n"
"s_add_u32 s65, s61, 0\n"
"buffer_load_dwordx4 v6, s[36:39], s65 offen  lds\n"
"buffer_load_dwordx4 v7, s[36:39], s65 offen offset:1024  lds\n"
"buffer_load_dwordx4 v8, s[36:39], s65 offen offset:2048  lds\n"
"buffer_load_dwordx4 v9, s[36:39], s65 offen offset:3072  lds\n"
"s_waitcnt lgkmcnt(0)\n"
"s_barrier\n"
"s_bfe_u32 s72, s20, 0x40014\n"
"s_mul_i32 s61, s72, 20480\n"
"s_mul_i32 s62, s78, 5120\n"
"s_add_u32 s61, s61, s62\n"
"s_add_u32 m0, s62, 20480\n"
"s_add_u32 s65, s61, 0\n"
"buffer_load_dwordx4 v18, s[40:43], s65 offen sc1 lds\n"
"buffer_load_dwordx4 v18, s[40:43], s65 offen offset:1024 sc1 lds\n"
"buffer_load_dwordx4 v18, s[40:43], s65 offen offset:2048 sc1 lds\n"
"buffer_load_dwordx4 v18, s[40:43], s65 offen offset:3072 sc1 lds\n"
"s_add_u32 m0, s62, 24576\n"
"s_add_u32 s65, s61, 4096\n"
"buffer_load_dwordx4 v18, s[40:43], s65 offen sc1 lds\n"
"s_lshl_b32 s61, s72, 17\n"
"s_lshl_b32 s62, s78, 15\n"
"s_add_u32 s61, s61, s62\n"
"s_add_u32 m0, s79, 77824\n"
"s_add_u32 s65, s61, 0\n"
"buffer_load_dwordx4 v6, s[36:39], s65 offen  lds\n"
"buffer_load_dwordx4 v7, s[36:39], s65 offen offset:1024  lds\n"
"buffer_load_dwordx4 v8, s[36:39], s65 offen offset:2048  lds\n"
"buffer_load_dwordx4 v9, s[36:39], s65 offen offset:3072  lds\n"
"s_waitcnt vmcnt(9)\n"
"s_barrier\n"
"s_bfe_u32 s72, s20, 0x40018\n"
"s_mul_i32 s61, s72, 20480\n"
"s_mul_i32 s62, s78, 5120\n"
"s_add_u32 s61, s61, s62\n"
"s_add_u32 m0, s62, 40960\n"
"s_add_u32 s65, s61, 0\n"
"buffer_load_dwordx4 v18, s[40:43], s65 offen sc1 lds\n"
"buffer_load_dwordx4 v18, s[40:43], s65 offen offset:1024 sc1 lds\n"
"buffer_load_dwordx4 v18, s[40:43], s65 offen offset:2048 sc1 lds\n"
"buffer_load_dwordx4 v18, s[40:43], s65 offen offset:3072 sc1 lds\n"
"s_add_u32 m0, s62, 45056\n"
"s_add_u32 s65, s61, 4096\n"
"buffer_load_dwordx4 v18, s[40:43], s65 offen sc1 lds\n"
"s_lshl_b32 s61, s72, 17\n"
"s_lshl_b32 s62, s78, 15\n"
"s_add_u32 s61, s61, s62\n"
"s_add_u32 m0, s79, 94208\n"
"s_add_u32 s65, s61, 0\n"
"buffer_load_dwordx4 v6, s[36:39], s65 offen  lds\n"
"buffer_load_dwordx4 v7, s[36:39], s65 offen offset:1024  lds\n"
"buffer_load_dwordx4 v8, s[36:39], s65 offen offset:2048  lds\n"
"buffer_load_dwordx4 v9, s[36:39], s65 offen offset:3072  lds\n"
"s_waitcnt vmcnt(9)\n"
"s_barrier\n"
"s_bfe_u32 s72, s20, 0x4001c\n"
"s_mul_i32 s61, s72, 20480\n"
"s_mul_i32 s62, s78, 5120\n"
"s_add_u32 s61, s61, s62\n"
"s_add_u32 m0, s62, 126976\n"
"s_add_u32 s65, s61, 0\n"
"buffer_load_dwordx4 v18, s[40:43], s65 offen sc1 lds\n"
"buffer_load_dwordx4 v18, s[40:43], s65 offen offset:1024 sc1 lds\n"
"buffer_load_dwordx4 v18, s[40:43], s65 offen offset:2048 sc1 lds\n"
"buffer_load_dwordx4 v18, s[40:43], s65 offen offset:3072 sc1 lds\n"
"s_add_u32 m0, s62, 131072\n"
"s_add_u32 s65, s61, 4096\n"
"buffer_load_dwordx4 v18, s[40:43], s65 offen sc1 lds\n"
"s_lshl_b32 s61, s72, 17\n"
"s_lshl_b32 s62, s78, 15\n"
"s_add_u32 s61, s61, s62\n"
"s_add_u32 m0, s79, 110592\n"
"s_add_u32 s65, s61, 0\n"
"buffer_load_dwordx4 v6, s[36:39], s65 offen  lds\n"
"buffer_load_dwordx4 v7, s[36:39], s65 offen offset:1024  lds\n"
"buffer_load_dwordx4 v8, s[36:39], s65 offen offset:2048  lds\n"
"buffer_load_dwordx4 v9, s[36:39], s65 offen offset:3072  lds\n"
"s_waitcnt vmcnt(9)\n"
"s_barrier\n"
"s_bfe_u32 s72, s21, 0x40000\n"
"s_mul_i32 s61, s72, 20480\n"
"s_mul_i32 s62, s78, 5120\n"
"s_add_u32 s61, s61, s62\n"
"s_add_u32 m0, s62, 0\n"
"s_add_u32 s65, s61, 0\n"
"buffer_load_dwordx4 v18, s[40:43], s65 offen sc1 lds\n"
"buffer_load_dwordx4 v18, s[40:43], s65 offen offset:1024 sc1 lds\n"
"buffer_load_dwordx4 v18, s[40:43], s65 offen offset:2048 sc1 lds\n"
"buffer_load_dwordx4 v18, s[40:43], s65 offen offset:3072 sc1 lds\n"
"s_add_u32 m0, s62, 4096\n"
"s_add_u32 s65, s61, 4096\n"
"buffer_load_dwordx4 v18, s[40:43], s65 offen sc1 lds\n"
"s_lshl_b32 s61, s72, 17\n"
"s_lshl_b32 s62, s78, 15\n"
"s_add_u32 s61, s61, s62\n"
"s_add_u32 m0, s79, 61440\n"
"s_add_u32 s65, s61, 0\n"
"buffer_load_dwordx4 v6, s[36:39], s65 offen  lds\n"
"buffer_load_dwordx4 v7, s[36:39], s65 offen offset:1024  lds\n"
"buffer_load_dwordx4 v8, s[36:39], s65 offen offset:2048  lds\n"
"buffer_load_dwordx4 v9, s[36:39], s65 offen offset:3072  lds\n"
"s_waitcnt vmcnt(9)\n"
"s_barrier\n"
"s_bfe_u32 s72, s21, 0x40004\n"
"global_load_dword v43, v34, s[52:53] sc1\n"
"s_mul_i32 s61, s72, 20480\n"
"s_mul_i32 s62, s78, 5120\n"
"s_add_u32 s61, s61, s62\n"
"s_add_u32 m0, s62, 20480\n"
"s_add_u32 s65, s61, 0\n"
"buffer_load_dwordx4 v18, s[40:43], s65 offen sc1 lds\n"
"buffer_load_dwordx4 v18, s[40:43], s65 offen offset:1024 sc1 lds\n"
"buffer_load_dwordx4 v18, s[40:43], s65 offen offset:2048 sc1 lds\n"
"buffer_load_dwordx4 v18, s[40:43], s65 offen offset:3072 sc1 lds\n"
"s_add_u32 m0, s62, 24576\n"
"s_add_u32 s65, s61, 4096\n"
"buffer_load_dwordx4 v18, s[40:43], s65 offen sc1 lds\n"
"s_lshl_b32 s61, s72, 17\n"
"s_lshl_b32 s62, s78, 15\n"
"s_add_u32 s61, s61, s62\n"
"s_add_u32 m0, s79, 77824\n"
"s_add_u32 s65, s61, 0\n"
"buffer_load_dwordx4 v6, s[36:39], s65 offen  lds\n"
"buffer_load_dwordx4 v7, s[36:39], s65 offen offset:1024  lds\n"
"buffer_load_dwordx4 v8, s[36:39], s65 offen offset:2048  lds\n"
"buffer_load_dwordx4 v9, s[36:39], s65 offen offset:3072  lds\n"
"s_waitcnt vmcnt(10)\n"
"s_barrier\n"
"s_bfe_u32 s72, s21, 0x40008\n"
"s_mul_i32 s61, s72, 20480\n"
"s_mul_i32 s62, s78, 5120\n"
"s_add_u32 s61, s61, s62\n"
"s_add_u32 m0, s62, 40960\n"
"s_add_u32 s65, s61, 0\n"
"buffer_load_dwordx4 v18, s[40:43], s65 offen sc1 lds\n"
"buffer_load_dwordx4 v18, s[40:43], s65 offen offset:1024 sc1 lds\n"
"buffer_load_dwordx4 v18, s[40:43], s65 offen offset:2048 sc1 lds\n"
"buffer_load_dwordx4 v18, s[40:43], s65 offen offset:3072 sc1 lds\n"
"s_add_u32 m0, s62, 45056\n"
"s_add_u32 s65, s61, 4096\n"
"buffer_load_dwordx4 v18, s[40:43], s65 offen sc1 lds\n"
"s_waitcnt vmcnt(14)\n"
"v_cmp_eq_u32 vcc, s70, v43\n"
"s_nop 0\n"
"s_lshr_b32 s61, vcc_lo, 16\n"
"s_and_b32 s68, s61, vcc_lo\n"
"s_lshl_b32 s74, 3, s64\n"
"s_xor_b32 s74, s74, 0xffff\n"
"s_mov_b32 s71, 0\n"
".Lk_p_47_%=:\n"
"s_andn2_b32 s61, s74, s68\n"
"s_cbranch_scc0 .Lk_g_48_%=\n"
"global_load_dword v41, v34, s[52:53] sc1\n"
"s_waitcnt vmcnt(0)\n"
"v_cmp_eq_u32 vcc, s70, v41\n"
"s_nop 0\n"
"s_lshr_b32 s61, vcc_lo, 16\n"
"s_and_b32 s68, s61, vcc_lo\n"
"s_add_u32 s71, s71, 1\n"
"s_cmp_lt_u32 s71, 400000\n"
"s_cbranch_scc1 .Lk_p_47_%=\n"
".Lk_g_48_%=:\n"
"v_mov_b32 v41, 16\n"
"ds_write_b32 v42, v41\n"
"s_lshl_b32 s61, s72, 17\n"
"s_lshl_b32 s62, s78, 15\n"
"s_add_u32 s61, s61, s62\n"
"s_add_u32 m0, s79, 94208\n"
"s_add_u32 s65, s61, 0\n"
"buffer_load_dwordx4 v6, s[36:39], s65 offen  lds\n"
"buffer_load_dwordx4 v7, s[36:39], s65 offen offset:1024  lds\n"
"buffer_load_dwordx4 v8, s[36:39], s65 offen offset:2048  lds\n"
"buffer_load_dwordx4 v9, s[36:39], s65 offen offset:3072  lds\n"
"s_waitcnt vmcnt(9)\n"
"s_waitcnt lgkmcnt(0)\n"
"s_barrier\n"
"s_bfe_u32 s72, s21, 0x4000c\n"
"s_mul_i32 s61, s72, 20480\n"
"s_mul_i32 s62, s78, 5120\n"
"s_add_u32 s61, s61, s62\n"
"s_add_u32 m0, s62, 126976\n"
"s_add_u32 s65, s61, 0\n"
"buffer_load_dwordx4 v18, s[40:43], s65 offen sc1 lds\n"
"buffer_load_dwordx4 v18, s[40:43], s65 offen offset:1024 sc1 lds\n"
"buffer_load_dwordx4 v18, s[40:43], s65 offen offset:2048 sc1 lds\n"
"buffer_load_dwordx4 v18, s[40:43], s65 offen offset:3072 sc1 lds\n"
"s_add_u32 m0, s62, 131072\n"
"s_add_u32 s65, s61, 4096\n"
"buffer_load_dwordx4 v18, s[40:43], s65 offen sc1 lds\n"
"s_lshl_b32 s61, s72, 17\n"
"s_lshl_b32 s62, s78, 15\n"
"s_add_u32 s61, s61, s62\n"
"s_add_u32 m0, s79, 110592\n"
"s_add_u32 s65, s61, 0\n"
"buffer_load_dwordx4 v6, s[36:39], s65 offen  lds\n"
"buffer_load_dwordx4 v7, s[36:39], s65 offen offset:1024  lds\n"
"buffer_load_dwordx4 v8, s[36:39], s65 offen offset:2048  lds\n"
"buffer_load_dwordx4 v9, s[36:39], s65 offen offset:3072  lds\n"
"s_waitcnt vmcnt(9)\n"
"s_barrier\n"
"s_bfe_u32 s72, s21, 0x40010\n"
"s_mul_i32 s61, s72, 20480\n"
"s_mul_i32 s62, s78, 5120\n"
"s_add_u32 s61, s61, s62\n"
"s_add_u32 m0, s62, 0\n"
"s_add_u32 s65, s61, 0\n"
"buffer_load_dwordx4 v18, s[40:43], s65 offen sc1 lds\n"
"buffer_load_dwordx4 v18, s[40:43], s65 offen offset:1024 sc1 lds\n"
"buffer_load_dwordx4 v18, s[40:43], s65 offen offset:2048 sc1 lds\n"
"buffer_load_dwordx4 v18, s[40:43], s65 offen offset:3072 sc1 lds\n"
"s_add_u32 m0, s62, 4096\n"
"s_add_u32 s65, s61, 4096\n"
"buffer_load_dwordx4 v18, s[40:43], s65 offen sc1 lds\n"
"s_lshl_b32 s61, s72, 17\n"
"s_lshl_b32 s62, s78, 15\n"
"s_add_u32 s61, s61, s62\n"
"s_add_u32 m0, s79, 61440\n"
"s_add_u32 s65, s61, 0\n"
"buffer_load_dwordx4 v6, s[36:39], s65 offen  lds\n"
"buffer_load_dwordx4 v7, s[36:39], s65 offen offset:1024  lds\n"
"buffer_load_dwordx4 v8, s[36:39], s65 offen offset:2048  lds\n"
"buffer_load_dwordx4 v9, s[36:39], s65 offen offset:3072  lds\n"
"s_waitcnt vmcnt(9)\n"
"s_barrier\n"
"s_bfe_u32 s72, s21, 0x40014\n"
"s_mul_i32 s61, s72, 20480\n"
"s_mul_i32 s62, s78, 5120\n"
"s_add_u32 s61, s61, s62\n"
"s_add_u32 m0, s62, 20480\n"
"s_add_u32 s65, s61, 0\n"
"buffer_load_dwordx4 v18, s[40:43], s65 offen sc1 lds\n"
"buffer_load_dwordx4 v18, s[40:43], s65 offen offset:1024 sc1 lds\n"
"buffer_load_dwordx4 v18, s[40:43], s65 offen offset:2048 sc1 lds\n"
"buffer_load_dwordx4 v18, s[40:43], s65 offen offset:3072 sc1 lds\n"
"s_add_u32 m0, s62, 24576\n"
"s_add_u32 s65, s61, 4096\n"
"buffer_load_dwordx4 v18, s[40:43], s65 offen sc1 lds\n"
"s_lshl_b32 s61, s72, 17\n"
"s_lshl_b32 s62, s78, 15\n"
"s_add_u32 s61, s61, s62\n"
"s_add_u32 m0, s79, 77824\n"
"s_add_u32 s65, s61, 0\n"
"buffer_load_dwordx4 v6, s[36:39], s65 offen  lds\n"
"buffer_load_dwordx4 v7, s[36:39], s65 offen offset:1024  lds\n"
"buffer_load_dwordx4 v8, s[36:39], s65 offen offset:2048  lds\n"
"buffer_load_dwordx4 v9, s[36:39], s65 offen offset:3072  lds\n"
"s_waitcnt vmcnt(9)\n"
"s_barrier\n"
"s_bfe_u32 s72, s21, 0x40018\n"
"s_mul_i32 s61, s72, 20480\n"
"s_mul_i32 s62, s78, 5120\n"
"s_add_u32 s61, s61, s62\n"
"s_add_u32 m0, s62, 40960\n"
"s_add_u32 s65, s61, 0\n"
"buffer_load_dwordx4 v18, s[40:43], s65 offen sc1 lds\n"
"buffer_load_dwordx4 v18, s[40:43], s65 offen offset:1024 sc1 lds\n"
"buffer_load_dwordx4 v18, s[40:43], s65 offen offset:2048 sc1 lds\n"
"buffer_load_dwordx4 v18, s[40:43], s65 offen offset:3072 sc1 lds\n"
"s_add_u32 m0, s62, 45056\n"
"s_add_u32 s65, s61, 4096\n"
"buffer_load_dwordx4 v18, s[40:43], s65 offen sc1 lds\n"
"s_lshl_b32 s61, s72, 17\n"
"s_lshl_b32 s62, s78, 15\n"
"s_add_u32 s61, s61, s62\n"
"s_add_u32 m0, s79, 94208\n"
"s_add_u32 s65, s61, 0\n"
"buffer_load_dwordx4 v6, s[36:39], s65 offen  lds\n"
"buffer_load_dwordx4 v7, s[36:39], s65 offen offset:1024  lds\n"
"buffer_load_dwordx4 v8, s[36:39], s65 offen offset:2048  lds\n"
"buffer_load_dwordx4 v9, s[36:39], s65 offen offset:3072  lds\n"
"s_waitcnt vmcnt(9)\n"
"s_barrier\n"
"s_bfe_u32 s72, s21, 0x4001c\n"
"s_mul_i32 s61, s72, 20480\n"
"s_mul_i32 s62, s78, 5120\n"
"s_add_u32 s61, s61, s62\n"
"s_add_u32 m0, s62, 126976\n"
"s_add_u32 s65, s61, 0\n"
"buffer_load_dwordx4 v18, s[40:43], s65 offen sc1 lds\n"
"buffer_load_dwordx4 v18, s[40:43], s65 offen offset:1024 sc1 lds\n"
"buffer_load_dwordx4 v18, s[40:43], s65 offen offset:2048 sc1 lds\n"
"buffer_load_dwordx4 v18, s[40:43], s65 offen offset:3072 sc1 lds\n"
"s_add_u32 m0, s62, 131072\n"
"s_add_u32 s65, s61, 4096\n"
"buffer_load_dwordx4 v18, s[40:43], s65 offen sc1 lds\n"
"s_lshl_b32 s61, s72, 17\n"
"s_lshl_b32 s62, s78, 15\n"
"s_add_u32 s61, s61, s62\n"
"s_add_u32 m0, s79, 110592\n"
"s_add_u32 s65, s61, 0\n"
"buffer_load_dwordx4 v6, s[36:39], s65 offen  lds\n"
"buffer_load_dwordx4 v7, s[36:39], s65 offen offset:1024  lds\n"
"buffer_load_dwordx4 v8, s[36:39], s65 offen offset:2048  lds\n"
"buffer_load_dwordx4 v9, s[36:39], s65 offen offset:3072  lds\n"
"s_waitcnt vmcnt(9)\n"
"s_barrier\n"
"s_waitcnt vmcnt(0)\n"
"s_barrier\n"
"s_branch .Lk_fm_42_%=\n"
".Lk_fo_41_%=:\n"
"s_bfe_u32 s73, s20, 0x40008\n"
"s_bfe_u32 s72, s20, 0x4000c\n"
".Lk_p_49_%=:\n"
"ds_read_b32 v41, v42\n"
"s_waitcnt lgkmcnt(0)\n"
"v_readfirstlane_b32 s61, v41\n"
"s_nop 0\n"
"s_cmp_lt_u32 s61, 4\n"
"s_cbranch_scc1 .Lk_p_49_%=\n"
".Lk_g_50_%=:\n"
"s_mul_i32 s61, s73, 20480\n"
"s_mul_i32 s62, s78, 5120\n"
"s_add_u32 s61, s61, s62\n"
"s_add_u32 m0, s62, 40960\n"
"s_add_u32 s65, s61, 0\n"
"buffer_load_dwordx4 v18, s[40:43], s65 offen sc1 lds\n"
"buffer_load_dwordx4 v18, s[40:43], s65 offen offset:1024 sc1 lds\n"
"buffer_load_dwordx4 v18, s[40:43], s65 offen offset:2048 sc1 lds\n"
"buffer_load_dwordx4 v18, s[40:43], s65 offen offset:3072 sc1 lds\n"
"s_add_u32 m0, s62, 45056\n"
"s_add_u32 s65, s61, 4096\n"
"buffer_load_dwordx4 v18, s[40:43], s65 offen sc1 lds\n"
"s_mul_i32 s61, s72, 20480\n"
"s_mul_i32 s62, s78, 5120\n"
"s_add_u32 s61, s61, s62\n"
"s_add_u32 m0, s62, 126976\n"
"s_add_u32 s65, s61, 0\n"
"buffer_load_dwordx4 v18, s[40:43], s65 offen sc1 lds\n"
"buffer_load_dwordx4 v18, s[40:43], s65 offen offset:1024 sc1 lds\n"
"buffer_load_dwordx4 v18, s[40:43], s65 offen offset:2048 sc1 lds\n"
"buffer_load_dwordx4 v18, s[40:43], s65 offen offset:3072 sc1 lds\n"
"s_add_u32 m0, s62, 131072\n"
"s_add_u32 s65, s61, 4096\n"
"buffer_load_dwordx4 v18, s[40:43], s65 offen sc1 lds\n"
"s_waitcnt vmcnt(5)\n"
"s_barrier\n"
"s_bfe_u32 s72, s20, 0x40010\n"
".Lk_p_51_%=:\n"
"ds_read_b32 v41, v42\n"
"s_waitcnt lgkmcnt(0)\n"
"v_readfirstlane_b32 s61, v41\n"
"s_nop 0\n"
"s_cmp_lt_u32 s61, 12\n"
"s_cbranch_scc1 .Lk_p_51_%=\n"
".Lk_g_52_%=:\n"
"s_mul_i32 s61, s72, 20480\n"
"s_mul_i32 s62, s78, 5120\n"
"s_add_u32 s61, s61, s62\n"
"s_add_u32 m0, s62, 0\n"
"s_add_u32 s65, s61, 0\n"
"buffer_load_dwordx4 v18, s[40:43], s65 offen sc1 lds\n"
"buffer_load_dwordx4 v18, s[40:43], s65 offen offset:1024 sc1 lds\n"
"buffer_load_dwordx4 v18, s[40:43], s65 offen offset:2048 sc1 lds\n"
"buffer_load_dwordx4 v18, s[40:43], s65 offen offset:3072 sc1 lds\n"
"s_add_u32 m0, s62, 4096\n"
"s_add_u32 s65, s61, 4096\n"
"buffer_load_dwordx4 v18, s[40:43], s65 offen sc1 lds\n"
"s_lshl_b32 s61, s72, 17\n"
"s_lshl_b32 s62, s78, 15\n"
"s_add_u32 s61, s61, s62\n"
"s_add_u32 m0, s79, 61440\n"
"s_add_u32 s65, s61, 0\n"
"buffer_load_dwordx4 v6, s[36:39], s65 offen  lds\n"
"buffer_load_dwordx4 v7, s[36:39], s65 offen offset:1024  lds\n"
"buffer_load_dwordx4 v8, s[36:39], s65 offen offset:2048  lds\n"
"buffer_load_dwordx4 v9, s[36:39], s65 offen offset:3072  lds\n"
"s_waitcnt vmcnt(9)\n"
"s_barrier\n"
"s_bfe_u32 s72, s20, 0x40014\n"
"s_mul_i32 s61, s72, 20480\n"
"s_mul_i32 s62, s78, 5120\n"
"s_add_u32 s61, s61, s62\n"
"s_add_u32 m0, s62, 20480\n"
"s_add_u32 s65, s61, 0\n"
"buffer_load_dwordx4 v18, s[40:43], s65 offen sc1 lds\n"
"buffer_load_dwordx4 v18, s[40:43], s65 offen offset:1024 sc1 lds\n"
"buffer_load_dwordx4 v18, s[40:43], s65 offen offset:2048 sc1 lds\n"
"buffer_load_dwordx4 v18, s[40:43], s65 offen offset:3072 sc1 lds\n"
"s_add_u32 m0, s62, 24576\n"
"s_add_u32 s65, s61, 4096\n"
"buffer_load_dwordx4 v18, s[40:43], s65 offen sc1 lds\n"
"s_lshl_b32 s61, s72, 17\n"
"s_lshl_b32 s62, s78, 15\n"
"s_add_u32 s61, s61, s62\n"
"s_add_u32 m0, s79, 77824\n"
"s_add_u32 s65, s61, 0\n"
"buffer_load_dwordx4 v6, s[36:39], s65 offen  lds\n"
"buffer_load_dwordx4 v7, s[36:39], s65 offen offset:1024  lds\n"
"buffer_load_dwordx4 v8, s[36:39], s65 offen offset:2048  lds\n"
"buffer_load_dwordx4 v9, s[36:39], s65 offen offset:3072  lds\n"
"s_waitcnt vmcnt(9)\n"
"s_barrier\n"
"s_bfe_u32 s72, s20, 0x40018\n"
"s_mul_i32 s61, s72, 20480\n"
"s_mul_i32 s62, s78, 5120\n"
"s_add_u32 s61, s61, s62\n"
"s_add_u32 m0, s62, 40960\n"
"s_add_u32 s65, s61, 0\n"
"buffer_load_dwordx4 v18, s[40:43], s65 offen sc1 lds\n"
"buffer_load_dwordx4 v18, s[40:43], s65 offen offset:1024 sc1 lds\n"
"buffer_load_dwordx4 v18, s[40:43], s65 offen offset:2048 sc1 lds\n"
"buffer_load_dwordx4 v18, s[40:43], s65 offen offset:3072 sc1 lds\n"
"s_add_u32 m0, s62, 45056\n"
"s_add_u32 s65, s61, 4096\n"
"buffer_load_dwordx4 v18, s[40:43], s65 offen sc1 lds\n"
"s_lshl_b32 s61, s72, 17\n"
"s_lshl_b32 s62, s78, 15\n"
"s_add_u32 s61, s61, s62\n"
"s_add_u32 m0, s79, 94208\n"
"s_add_u32 s65, s61, 0\n"
"buffer_load_dwordx4 v6, s[36:39], s65 offen  lds\n"
"buffer_load_dwordx4 v7, s[36:39], s65 offen offset:1024  lds\n"
"buffer_load_dwordx4 v8, s[36:39], s65 offen offset:2048  lds\n"
"buffer_load_dwordx4 v9, s[36:39], s65 offen offset:3072  lds\n"
"s_waitcnt vmcnt(9)\n"
"s_barrier\n"
"s_bfe_u32 s72, s20, 0x4001c\n"
"s_mul_i32 s61, s72, 20480\n"
"s_mul_i32 s62, s78, 5120\n"
"s_add_u32 s61, s61, s62\n"
"s_add_u32 m0, s62, 126976\n"
"s_add_u32 s65, s61, 0\n"
"buffer_load_dwordx4 v18, s[40:43], s65 offen sc1 lds\n"
"buffer_load_dwordx4 v18, s[40:43], s65 offen offset:1024 sc1 lds\n"
"buffer_load_dwordx4 v18, s[40:43], s65 offen offset:2048 sc1 lds\n"
"buffer_load_dwordx4 v18, s[40:43], s65 offen offset:3072 sc1 lds\n"
"s_add_u32 m0, s62, 131072\n"
"s_add_u32 s65, s61, 4096\n"
"buffer_load_dwordx4 v18, s[40:43], s65 offen sc1 lds\n"
"s_lshl_b32 s61, s72, 17\n"
"s_lshl_b32 s62, s78, 15\n"
"s_add_u32 s61, s61, s62\n"
"s_add_u32 m0, s79, 110592\n"
"s_add_u32 s65, s61, 0\n"
"buffer_load_dwordx4 v6, s[36:39], s65 offen  lds\n"
"buffer_load_dwordx4 v7, s[36:39], s65 offen offset:1024  lds\n"
"buffer_load_dwordx4 v8, s[36:39], s65 offen offset:2048  lds\n"
"buffer_load_dwordx4 v9, s[36:39], s65 offen offset:3072  lds\n"
"s_waitcnt vmcnt(9)\n"
"s_barrier\n"
"s_bfe_u32 s72, s21, 0x40000\n"
"s_mul_i32 s61, s72, 20480\n"
"s_mul_i32 s62, s78, 5120\n"
"s_add_u32 s61, s61, s62\n"
"s_add_u32 m0, s62, 0\n"
"s_add_u32 s65, s61, 0\n"
"buffer_load_dwordx4 v18, s[40:43], s65 offen sc1 lds\n"
"buffer_load_dwordx4 v18, s[40:43], s65 offen offset:1024 sc1 lds\n"
"buffer_load_dwordx4 v18, s[40:43], s65 offen offset:2048 sc1 lds\n"
"buffer_load_dwordx4 v18, s[40:43], s65 offen offset:3072 sc1 lds\n"
"s_add_u32 m0, s62, 4096\n"
"s_add_u32 s65, s61, 4096\n"
"buffer_load_dwordx4 v18, s[40:43], s65 offen sc1 lds\n"
"s_lshl_b32 s61, s72, 17\n"
"s_lshl_b32 s62, s78, 15\n"
"s_add_u32 s61, s61, s62\n"
"s_add_u32 m0, s79, 61440\n"
"s_add_u32 s65, s61, 0\n"
"buffer_load_dwordx4 v6, s[36:39], s65 offen  lds\n"
"buffer_load_dwordx4 v7, s[36:39], s65 offen offset:1024  lds\n"
"buffer_load_dwordx4 v8, s[36:39], s65 offen offset:2048  lds\n"
"buffer_load_dwordx4 v9, s[36:39], s65 offen offset:3072  lds\n"
"s_waitcnt vmcnt(9)\n"
"s_barrier\n"
"s_bfe_u32 s72, s21, 0x40004\n"
"s_mul_i32 s61, s72, 20480\n"
"s_mul_i32 s62, s78, 5120\n"
"s_add_u32 s61, s61, s62\n"
"s_add_u32 m0, s62, 20480\n"
"s_add_u32 s65, s61, 0\n"
"buffer_load_dwordx4 v18, s[40:43], s65 offen sc1 lds\n"
"buffer_load_dwordx4 v18, s[40:43], s65 offen offset:1024 sc1 lds\n"
"buffer_load_dwordx4 v18, s[40:43], s65 offen offset:2048 sc1 lds\n"
"buffer_load_dwordx4 v18, s[40:43], s65 offen offset:3072 sc1 lds\n"
"s_add_u32 m0, s62, 24576\n"
"s_add_u32 s65, s61, 4096\n"
"buffer_load_dwordx4 v18, s[40:43], s65 offen sc1 lds\n"
"s_lshl_b32 s61, s72, 17\n"
"s_lshl_b32 s62, s78, 15\n"
"s_add_u32 s61, s61, s62\n"
"s_add_u32 m0, s79, 77824\n"
"s_add_u32 s65, s61, 0\n"
"buffer_load_dwordx4 v6, s[36:39], s65 offen  lds\n"
"buffer_load_dwordx4 v7, s[36:39], s65 offen offset:1024  lds\n"
"buffer_load_dwordx4 v8, s[36:39], s65 offen offset:2048  lds\n"
"buffer_load_dwordx4 v9, s[36:39], s65 offen offset:3072  lds\n"
"s_waitcnt vmcnt(9)\n"
"s_barrier\n"
"s_bfe_u32 s72, s21, 0x40008\n"
"s_mul_i32 s61, s72, 20480\n"
"s_mul_i32 s62, s78, 5120\n"
"s_add_u32 s61, s61, s62\n"
"s_add_u32 m0, s62, 40960\n"
"s_add_u32 s65, s61, 0\n"
"buffer_load_dwordx4 v18, s[40:43], s65 offen sc1 lds\n"
"buffer_load_dwordx4 v18, s[40:43], s65 offen offset:1024 sc1 lds\n"
"buffer_load_dwordx4 v18, s[40:43], s65 offen offset:2048 sc1 lds\n"
"buffer_load_dwordx4 v18, s[40:43], s65 offen offset:3072 sc1 lds\n"
"s_add_u32 m0, s62, 45056\n"
"s_add_u32 s65, s61, 4096\n"
"buffer_load_dwordx4 v18, s[40:43], s65 offen sc1 lds\n"
"s_lshl_b32 s61, s72, 17\n"
"s_lshl_b32 s62, s78, 15\n"
"s_add_u32 s61, s61, s62\n"
"s_add_u32 m0, s79, 94208\n"
"s_add_u32 s65, s61, 0\n"
"buffer_load_dwordx4 v6, s[36:39], s65 offen  lds\n"
"buffer_load_dwordx4 v7, s[36:39], s65 offen offset:1024  lds\n"
"buffer_load_dwordx4 v8, s[36:39], s65 offen offset:2048  lds\n"
"buffer_load_dwordx4 v9, s[36:39], s65 offen offset:3072  lds\n"
"s_waitcnt vmcnt(9)\n"
"s_barrier\n"
"s_bfe_u32 s72, s21, 0x4000c\n"
"ds_read_b32 v41, v42\n"
"s_mul_i32 s61, s72, 20480\n"
"s_mul_i32 s62, s78, 5120\n"
"s_add_u32 s61, s61, s62\n"
"s_add_u32 m0, s62, 126976\n"
"s_add_u32 s65, s61, 0\n"
"buffer_load_dwordx4 v18, s[40:43], s65 offen sc1 lds\n"
"buffer_load_dwordx4 v18, s[40:43], s65 offen offset:1024 sc1 lds\n"
"buffer_load_dwordx4 v18, s[40:43], s65 offen offset:2048 sc1 lds\n"
"buffer_load_dwordx4 v18, s[40:43], s65 offen offset:3072 sc1 lds\n"
"s_add_u32 m0, s62, 131072\n"
"s_add_u32 s65, s61, 4096\n"
"buffer_load_dwordx4 v18, s[40:43], s65 offen sc1 lds\n"
"s_lshl_b32 s61, s72, 17\n"
"s_lshl_b32 s62, s78, 15\n"
"s_add_u32 s61, s61, s62\n"
"s_add_u32 m0, s79, 110592\n"
"s_add_u32 s65, s61, 0\n"
"buffer_load_dwordx4 v6, s[36:39], s65 offen  lds\n"
"buffer_load_dwordx4 v7, s[36:39], s65 offen offset:1024  lds\n"
"buffer_load_dwordx4 v8, s[36:39], s65 offen offset:2048  lds\n"
"buffer_load_dwordx4 v9, s[36:39], s65 offen offset:3072  lds\n"
"s_waitcnt lgkmcnt(0)\n"
"v_readfirstlane_b32 s61, v41\n"
"s_nop 0\n"
"s_cmp_ge_u32 s61, 16\n"
"s_cbranch_scc1 .Lk_g_54_%=\n"
".Lk_p_53_%=:\n"
"ds_read_b32 v41, v42\n"
"s_waitcnt lgkmcnt(0)\n"
"v_readfirstlane_b32 s61, v41\n"
"s_nop 0\n"
"s_cmp_lt_u32 s61, 16\n"
"s_cbranch_scc1 .Lk_p_53_%=\n"
".Lk_g_54_%=:\n"
"s_waitcnt vmcnt(9)\n"
"s_barrier\n"
"s_bfe_u32 s72, s21, 0x40010\n"
"s_mul_i32 s61, s72, 20480\n"
"s_mul_i32 s62, s78, 5120\n"
"s_add_u32 s61, s61, s62\n"
"s_add_u32 m0, s62, 0\n"
"s_add_u32 s65, s61, 0\n"
"buffer_load_dwordx4 v18, s[40:43], s65 offen sc1 lds\n"
"buffer_load_dwordx4 v18, s[40:43], s65 offen offset:1024 sc1 lds\n"
"buffer_load_dwordx4 v18, s[40:43], s65 offen offset:2048 sc1 lds\n"
"buffer_load_dwordx4 v18, s[40:43], s65 offen offset:3072 sc1 lds\n"
"s_add_u32 m0, s62, 4096\n"
"s_add_u32 s65, s61, 4096\n"
"buffer_load_dwordx4 v18, s[40:43], s65 offen sc1 lds\n"
"s_lshl_b32 s61, s72, 17\n"
"s_lshl_b32 s62, s78, 15\n"
"s_add_u32 s61, s61, s62\n"
"s_add_u32 m0, s79, 61440\n"
"s_add_u32 s65, s61, 0\n"
"buffer_load_dwordx4 v6, s[36:39], s65 offen  lds\n"
"buffer_load_dwordx4 v7, s[36:39], s65 offen offset:1024  lds\n"
"buffer_load_dwordx4 v8, s[36:39], s65 offen offset:2048  lds\n"
"buffer_load_dwordx4 v9, s[36:39], s65 offen offset:3072  lds\n"
"s_waitcnt vmcnt(9)\n"
"s_barrier\n"
"s_bfe_u32 s72, s21, 0x40014\n"
"s_mul_i32 s61, s72, 20480\n"
"s_mul_i32 s62, s78, 5120\n"
"s_add_u32 s61, s61, s62\n"
"s_add_u32 m0, s62, 20480\n"
"s_add_u32 s65, s61, 0\n"
"buffer_load_dwordx4 v18, s[40:43], s65 offen sc1 lds\n"
"buffer_load_dwordx4 v18, s[40:43], s65 offen offset:1024 sc1 lds\n"
"buffer_load_dwordx4 v18, s[40:43], s65 offen offset:2048 sc1 lds\n"
"buffer_load_dwordx4 v18, s[40:43], s65 offen offset:3072 sc1 lds\n"
"s_add_u32 m0, s62, 24576\n"
"s_add_u32 s65, s61, 4096\n"
"buffer_load_dwordx4 v18, s[40:43], s65 offen sc1 lds\n"
"s_lshl_b32 s61, s72, 17\n"
"s_lshl_b32 s62, s78, 15\n"
"s_add_u32 s61, s61, s62\n"
"s_add_u32 m0, s79, 77824\n"
"s_add_u32 s65, s61, 0\n"
"buffer_load_dwordx4 v6, s[36:39], s65 offen  lds\n"
"buffer_load_dwordx4 v7, s[36:39], s65 offen offset:1024  lds\n"
"buffer_load_dwordx4 v8, s[36:39], s65 offen offset:2048  lds\n"
"buffer_load_dwordx4 v9, s[36:39], s65 offen offset:3072  lds\n"
"s_waitcnt vmcnt(9)\n"
"s_barrier\n"
"s_bfe_u32 s72, s21, 0x40018\n"
"s_mul_i32 s61, s72, 20480\n"
"s_mul_i32 s62, s78, 5120\n"
"s_add_u32 s61, s61, s62\n"
"s_add_u32 m0, s62, 40960\n"
"s_add_u32 s65, s61, 0\n"
"buffer_load_dwordx4 v18, s[40:43], s65 offen sc1 lds\n"
"buffer_load_dwordx4 v18, s[40:43], s65 offen offset:1024 sc1 lds\n"
"buffer_load_dwordx4 v18, s[40:43], s65 offen offset:2048 sc1 lds\n"
"buffer_load_dwordx4 v18, s[40:43], s65 offen offset:3072 sc1 lds\n"
"s_add_u32 m0, s62, 45056\n"
"s_add_u32 s65, s61, 4096\n"
"buffer_load_dwordx4 v18, s[40:43], s65 offen sc1 lds\n"
"s_lshl_b32 s61, s72, 17\n"
"s_lshl_b32 s62, s78, 15\n"
"s_add_u32 s61, s61, s62\n"
"s_add_u32 m0, s79, 94208\n"
"s_add_u32 s65, s61, 0\n"
"buffer_load_dwordx4 v6, s[36:39], s65 offen  lds\n"
"buffer_load_dwordx4 v7, s[36:39], s65 offen offset:1024  lds\n"
"buffer_load_dwordx4 v8, s[36:39], s65 offen offset:2048  lds\n"
"buffer_load_dwordx4 v9, s[36:39], s65 offen offset:3072  lds\n"
"s_waitcnt vmcnt(9)\n"
"s_barrier\n"
"s_bfe_u32 s72, s21, 0x4001c\n"
"s_mul_i32 s61, s72, 20480\n"
"s_mul_i32 s62, s78, 5120\n"
"s_add_u32 s61, s61, s62\n"
"s_add_u32 m0, s62, 126976\n"
"s_add_u32 s65, s61, 0\n"
"buffer_load_dwordx4 v18, s[40:43], s65 offen sc1 lds\n"
"buffer_load_dwordx4 v18, s[40:43], s65 offen offset:1024 sc1 lds\n"
"buffer_load_dwordx4 v18, s[40:43], s65 offen offset:2048 sc1 lds\n"
"buffer_load_dwordx4 v18, s[40:43], s65 offen offset:3072 sc1 lds\n"
"s_add_u32 m0, s62, 131072\n"
"s_add_u32 s65, s61, 4096\n"
"buffer_load_dwordx4 v18, s[40:43], s65 offen sc1 lds\n"
"s_lshl_b32 s61, s72, 17\n"
"s_lshl_b32 s62, s78, 15\n"
"s_add_u32 s61, s61, s62\n"
"s_add_u32 m0, s79, 110592\n"
"s_add_u32 s65, s61, 0\n"
"buffer_load_dwordx4 v6, s[36:39], s65 offen  lds\n"
"buffer_load_dwordx4 v7, s[36:39], s65 offen offset:1024  lds\n"
"buffer_load_dwordx4 v8, s[36:39], s65 offen offset:2048  lds\n"
"buffer_load_dwordx4 v9, s[36:39], s65 offen offset:3072  lds\n"
"s_waitcnt vmcnt(9)\n"
"s_barrier\n"
"s_waitcnt vmcnt(0)\n"
"s_barrier\n"
".Lk_fm_42_%=:\n"
"s_mul_i32 s63, s57, 40960\n"
"s_lshl_b32 s61, s78, 10\n"
"s_add_u32 s63, s63, s61\n"
"s_lshl_b32 s61, s59, 9\n"
"v_and_b32 v40, 3, v0\n"
"v_lshl_add_u32 v34, v40, 2, s61\n"
"s_lshl_b32 s73, 1, s57\n"
"s_xor_b32 s73, s73, 15\n"
"s_mov_b32 s68, 0\n"
"s_mul_i32 s61, s66, 10240\n"
"s_lshl_b32 s62, s67, 10\n"
"s_add_u32 s61, s61, s62\n"
"s_add_u32 s61, s61, 20480\n"
"v_lshl_add_u32 v43, v0, 4, s61\n"
"v_and_b32 v40, 15, v0\n"
"v_lshrrev_b32 v41, 4, v0\n"
"v_and_b32 v44, 1, v41\n"
"v_lshrrev_b32 v41, 1, v41\n"
"v_lshlrev_b32 v44, 5, v44\n"
"v_lshl_add_u32 v44, v41, 4, v44\n"
"v_lshl_add_u32 v44, v40, 11, v44\n"
"s_lshr_b32 s61, s57, 1\n"
"s_mul_i32 s61, s61, 163840\n"
"s_lshl_b32 s62, s67, 15\n"
"s_add_u32 s61, s61, s62\n"
"s_and_b32 s62, s57, 1\n"
"s_lshl_b32 s62, s62, 7\n"
"s_add_u32 s61, s61, s62\n"
"s_lshl_b32 s62, s66, 6\n"
"s_add_u32 s61, s61, s62\n"
"v_add_u32 v44, s61, v44\n"
"v_add_u32 v45, 4096, v18\n"
"s_cmp_lt_u32 s78, 2\n"
"s_cselect_b32 s62, 8192, 0\n"
"v_add_u32 v46, s62, v18\n"
"s_add_u32 s24, s57, 1\n"
"s_and_b32 s24, s24, 3\n"
"s_mul_i32 s24, s24, 10240\n"
"s_add_u32 s24, s24, s63\n"
"s_add_u32 s25, s57, 2\n"
"s_and_b32 s25, s25, 3\n"
"s_mul_i32 s25, s25, 10240\n"
"s_add_u32 s25, s25, s63\n"
"s_add_u32 s26, s57, 3\n"
"s_and_b32 s26, s26, 3\n"
"s_mul_i32 s26, s26, 10240\n"
"s_add_u32 s26, s26, s63\n"
"s_barrier\n"
"ds_read_b128 v[136:139], v43 offset:0\n"
"ds_read_b128 v[140:143], v43 offset:5120\n"
"ds_read_b128 v[144:147], v43 offset:2048\n"
"ds_read_b128 v[148:151], v43 offset:7168\n"
"ds_read_b128 v[152:155], v43 offset:4096\n"
"ds_read_b128 v[156:159], v43 offset:9216\n"
"v_mov_b32 v42, 162432\n"
"s_cmp_lg_u32 s78, 0\n"
"s_cbranch_scc1 .Lk_l2f_55_%=\n"
"s_mov_b32 s71, 0\n"
".Lk_p_57_%=:\n"
"s_and_b32 s69, s68, s73\n"
"s_cmp_eq_u32 s69, s73\n"
"s_cbranch_scc1 .Lk_pd_58_%=\n"
"global_load_dword v41, v34, s[52:53] offset:128 sc1\n"
"s_waitcnt vmcnt(0)\n"
"v_cmp_eq_u32 vcc, s70, v41\n"
"s_add_u32 s71, s71, 1\n"
"s_mov_b32 s68, vcc_lo\n"
"s_cmp_lt_u32 s71, 400000\n"
"s_cbranch_scc1 .Lk_p_57_%=\n"
".Lk_pd_58_%=:\n"
"v_mov_b32 v41, 99\n"
"ds_write_b32 v42, v41\n"
"s_branch .Lk_l2d_56_%=\n"
".Lk_l2f_55_%=:\n"
"ds_read_b32 v41, v42\n"
"s_waitcnt lgkmcnt(0)\n"
"v_readfirstlane_b32 s61, v41\n"
"s_nop 0\n"
"s_cmp_lt_u32 s61, 99\n"
"s_cbranch_scc1 .Lk_l2f_55_%=\n"
".Lk_l2d_56_%=:\n"
"buffer_load_dwordx4 v[96:99], v18, s[44:47], s24 offen sc1\n"
"buffer_load_dwordx4 v[100:103], v18, s[44:47], s25 offen sc1\n"
"buffer_load_dwordx4 v[104:107], v18, s[44:47], s26 offen sc1\n"
"buffer_load_dwordx4 v[108:111], v45, s[44:47], s24 offen sc1\n"
"buffer_load_dwordx4 v[112:115], v45, s[44:47], s25 offen sc1\n"
"buffer_load_dwordx4 v[116:119], v45, s[44:47], s26 offen sc1\n"
"buffer_load_dwordx4 v[120:123], v46, s[44:47], s24 offen sc1\n"
"buffer_load_dwordx4 v[124:127], v46, s[44:47], s25 offen sc1\n"
"buffer_load_dwordx4 v[128:131], v46, s[44:47], s26 offen sc1\n"
"s_waitcnt vmcnt(8)\n"
"v_lshlrev_b32 v48, 16, v96\n"
"v_and_b32 v96, 0xffff0000, v96\n"
"v_lshlrev_b32 v49, 16, v97\n"
"v_and_b32 v97, 0xffff0000, v97\n"
"v_lshlrev_b32 v50, 16, v98\n"
"v_and_b32 v98, 0xffff0000, v98\n"
"v_lshlrev_b32 v51, 16, v99\n"
"v_and_b32 v99, 0xffff0000, v99\n"
"s_waitcnt lgkmcnt(6)\n"
"v_add_f32 v136, v48, v136\n"
"v_add_f32 v137, v96, v137\n"
"v_add_f32 v138, v49, v138\n"
"v_add_f32 v139, v97, v139\n"
"s_waitcnt lgkmcnt(5)\n"
"v_add_f32 v140, v50, v140\n"
"v_add_f32 v141, v98, v141\n"
"v_add_f32 v142, v51, v142\n"
"v_add_f32 v143, v99, v143\n"
"s_waitcnt vmcnt(7)\n"
"v_lshlrev_b32 v48, 16, v100\n"
"v_and_b32 v100, 0xffff0000, v100\n"
"v_lshlrev_b32 v49, 16, v101\n"
"v_and_b32 v101, 0xffff0000, v101\n"
"v_lshlrev_b32 v50, 16, v102\n"
"v_and_b32 v102, 0xffff0000, v102\n"
"v_lshlrev_b32 v51, 16, v103\n"
"v_and_b32 v103, 0xffff0000, v103\n"
"v_add_f32 v136, v48, v136\n"
"v_add_f32 v137, v100, v137\n"
"v_add_f32 v138, v49, v138\n"
"v_add_f32 v139, v101, v139\n"
"v_add_f32 v140, v50, v140\n"
"v_add_f32 v141, v102, v141\n"
"v_add_f32 v142, v51, v142\n"
"v_add_f32 v143, v103, v143\n"
"s_waitcnt vmcnt(6)\n"
"v_lshlrev_b32 v48, 16, v104\n"
"v_and_b32 v104, 0xffff0000, v104\n"
"v_lshlrev_b32 v49, 16, v105\n"
"v_and_b32 v105, 0xffff0000, v105\n"
"v_lshlrev_b32 v50, 16, v106\n"
"v_and_b32 v106, 0xffff0000, v106\n"
"v_lshlrev_b32 v51, 16, v107\n"
"v_and_b32 v107, 0xffff0000, v107\n"
"v_add_f32 v136, v48, v136\n"
"v_add_f32 v137, v104, v137\n"
"v_add_f32 v138, v49, v138\n"
"v_add_f32 v139, v105, v139\n"
"v_add_f32 v140, v50, v140\n"
"v_add_f32 v141, v106, v141\n"
"v_add_f32 v142, v51, v142\n"
"v_add_f32 v143, v107, v143\n"
"v_cvt_pk_bf16_f32 v136, v136, v137\n"
"v_cvt_pk_bf16_f32 v137, v138, v139\n"
"v_cvt_pk_bf16_f32 v138, v140, v141\n"
"v_cvt_pk_bf16_f32 v139, v142, v143\n"
"s_nop 0\n"
"v_permlane16_swap_b32 v136, v138\n"
"v_permlane16_swap_b32 v137, v139\n"
"s_mov_b32 s65, 0\n"
"buffer_store_dwordx4 v[136:139], v44, s[48:51], s65 offen\n"
"s_waitcnt vmcnt(6)\n"
"v_lshlrev_b32 v48, 16, v108\n"
"v_and_b32 v108, 0xffff0000, v108\n"
"v_lshlrev_b32 v49, 16, v109\n"
"v_and_b32 v109, 0xffff0000, v109\n"
"v_lshlrev_b32 v50, 16, v110\n"
"v_and_b32 v110, 0xffff0000, v110\n"
"v_lshlrev_b32 v51, 16, v111\n"
"v_and_b32 v111, 0xffff0000, v111\n"
"s_waitcnt lgkmcnt(4)\n"
"v_add_f32 v144, v48, v144\n"
"v_add_f32 v145, v108, v145\n"
"v_add_f32 v146, v49, v146\n"
"v_add_f32 v147, v109, v147\n"
"s_waitcnt lgkmcnt(3)\n"
"v_add_f32 v148, v50, v148\n"
"v_add_f32 v149, v110, v149\n"
"v_add_f32 v150, v51, v150\n"
"v_add_f32 v151, v111, v151\n"
"s_waitcnt vmcnt(5)\n"
"v_lshlrev_b32 v48, 16, v112\n"
"v_and_b32 v112, 0xffff0000, v112\n"
"v_lshlrev_b32 v49, 16, v113\n"
"v_and_b32 v113, 0xffff0000, v113\n"
"v_lshlrev_b32 v50, 16, v114\n"
"v_and_b32 v114, 0xffff0000, v114\n"
"v_lshlrev_b32 v51, 16, v115\n"
"v_and_b32 v115, 0xffff0000, v115\n"
"v_add_f32 v144, v48, v144\n"
"v_add_f32 v145, v112, v145\n"
"v_add_f32 v146, v49, v146\n"
"v_add_f32 v147, v113, v147\n"
"v_add_f32 v148, v50, v148\n"
"v_add_f32 v149, v114, v149\n"
"v_add_f32 v150, v51, v150\n"
"v_add_f32 v151, v115, v151\n"
"s_waitcnt vmcnt(4)\n"
"v_lshlrev_b32 v48, 16, v116\n"
"v_and_b32 v116, 0xffff0000, v116\n"
"v_lshlrev_b32 v49, 16, v117\n"
"v_and_b32 v117, 0xffff0000, v117\n"
"v_lshlrev_b32 v50, 16, v118\n"
"v_and_b32 v118, 0xffff0000, v118\n"
"v_lshlrev_b32 v51, 16, v119\n"
"v_and_b32 v119, 0xffff0000, v119\n"
"v_add_f32 v144, v48, v144\n"
"v_add_f32 v145, v116, v145\n"
"v_add_f32 v146, v49, v146\n"
"v_add_f32 v147, v117, v147\n"
"v_add_f32 v148, v50, v148\n"
"v_add_f32 v149, v118, v149\n"
"v_add_f32 v150, v51, v150\n"
"v_add_f32 v151, v119, v151\n"
"v_cvt_pk_bf16_f32 v144, v144, v145\n"
"v_cvt_pk_bf16_f32 v145, v146, v147\n"
"v_cvt_pk_bf16_f32 v146, v148, v149\n"
"v_cvt_pk_bf16_f32 v147, v150, v151\n"
"s_nop 0\n"
"v_permlane16_swap_b32 v144, v146\n"
"v_permlane16_swap_b32 v145, v147\n"
"s_mov_b32 s65, 65536\n"
"buffer_store_dwordx4 v[144:147], v44, s[48:51], s65 offen\n"
"s_cmp_ge_u32 s78, 2\n"
"s_cbranch_scc1 .Lk_x_59_%=\n"
"s_waitcnt vmcnt(4)\n"
"v_lshlrev_b32 v48, 16, v120\n"
"v_and_b32 v120, 0xffff0000, v120\n"
"v_lshlrev_b32 v49, 16, v121\n"
"v_and_b32 v121, 0xffff0000, v121\n"
"v_lshlrev_b32 v50, 16, v122\n"
"v_and_b32 v122, 0xffff0000, v122\n"
"v_lshlrev_b32 v51, 16, v123\n"
"v_and_b32 v123, 0xffff0000, v123\n"
"s_waitcnt lgkmcnt(2)\n"
"v_add_f32 v152, v48, v152\n"
"v_add_f32 v153, v120, v153\n"
"v_add_f32 v154, v49, v154\n"
"v_add_f32 v155, v121, v155\n"
"s_waitcnt lgkmcnt(1)\n"
"v_add_f32 v156, v50, v156\n"
"v_add_f32 v157, v122, v157\n"
"v_add_f32 v158, v51, v158\n"
"v_add_f32 v159, v123, v159\n"
"s_waitcnt vmcnt(3)\n"
"v_lshlrev_b32 v48, 16, v124\n"
"v_and_b32 v124, 0xffff0000, v124\n"
"v_lshlrev_b32 v49, 16, v125\n"
"v_and_b32 v125, 0xffff0000, v125\n"
"v_lshlrev_b32 v50, 16, v126\n"
"v_and_b32 v126, 0xffff0000, v126\n"
"v_lshlrev_b32 v51, 16, v127\n"
"v_and_b32 v127, 0xffff0000, v127\n"
"v_add_f32 v152, v48, v152\n"
"v_add_f32 v153, v124, v153\n"
"v_add_f32 v154, v49, v154\n"
"v_add_f32 v155, v125, v155\n"
"v_add_f32 v156, v50, v156\n"
"v_add_f32 v157, v126, v157\n"
"v_add_f32 v158, v51, v158\n"
"v_add_f32 v159, v127, v159\n"
"s_waitcnt vmcnt(2)\n"
"v_lshlrev_b32 v48, 16, v128\n"
"v_and_b32 v128, 0xffff0000, v128\n"
"v_lshlrev_b32 v49, 16, v129\n"
"v_and_b32 v129, 0xffff0000, v129\n"
"v_lshlrev_b32 v50, 16, v130\n"
"v_and_b32 v130, 0xffff0000, v130\n"
"v_lshlrev_b32 v51, 16, v131\n"
"v_and_b32 v131, 0xffff0000, v131\n"
"v_add_f32 v152, v48, v152\n"
"v_add_f32 v153, v128, v153\n"
"v_add_f32 v154, v49, v154\n"
"v_add_f32 v155, v129, v155\n"
"v_add_f32 v156, v50, v156\n"
"v_add_f32 v157, v130, v157\n"
"v_add_f32 v158, v51, v158\n"
"v_add_f32 v159, v131, v159\n"
"v_cvt_pk_bf16_f32 v152, v152, v153\n"
"v_cvt_pk_bf16_f32 v153, v154, v155\n"
"v_cvt_pk_bf16_f32 v154, v156, v157\n"
"v_cvt_pk_bf16_f32 v155, v158, v159\n"
"s_nop 0\n"
"v_permlane16_swap_b32 v152, v154\n"
"v_permlane16_swap_b32 v153, v155\n"
"s_mov_b32 s65, 131072\n"
"buffer_store_dwordx4 v[152:155], v44, s[48:51], s65 offen\n"
".Lk_x_59_%=:\n"
"s_waitcnt vmcnt(0)\n"
"s_branch .Lk_E_3_%=\n"
".Lk_B_2_%=:\n"
"s_and_b32 s66, s54, 1\n"
"s_lshr_b32 s67, s54, 1\n"
"s_and_b32 s67, s67, 1\n"
"v_and_b32 v40, 15, v0\n"
"v_lshrrev_b32 v41, 4, v0\n"
"v_lshrrev_b32 v42, 1, v40\n"
"v_xor_b32 v42, v41, v42\n"
"v_lshlrev_b32 v42, 4, v42\n"
"v_lshl_add_u32 v42, v40, 7, v42\n"
"s_mul_i32 s61, s67, 10240\n"
"v_add_u32 v2, s61, v42\n"
"v_xor_b32 v3, 64, v2\n"
"v_add_u32 v8, 126976, v2\n"
"v_add_u32 v9, 126976, v3\n"
"v_lshrrev_b32 v42, 2, v40\n"
"v_and_b32 v43, 3, v0\n"
"v_and_b32 v44, 1, v41\n"
"v_lshl_or_b32 v44, v44, 2, v42\n"
"v_lshl_add_u32 v45, v41, 3, v42\n"
"v_lshlrev_b32 v45, 8, v45\n"
"v_lshl_add_u32 v45, v43, 3, v45\n"
"v_add_u32 v45, 61440, v45\n"
"s_lshl_b32 s61, s66, 2\n"
"s_add_u32 s62, s61, 0\n"
"v_xor_b32 v46, s62, v44\n"
"v_lshl_add_u32 v4, v46, 5, v45\n"
"s_add_u32 s62, s61, 1\n"
"v_xor_b32 v46, s62, v44\n"
"v_lshl_add_u32 v5, v46, 5, v45\n"
"s_add_u32 s62, s61, 2\n"
"v_xor_b32 v46, s62, v44\n"
"v_lshl_add_u32 v6, v46, 5, v45\n"
"s_add_u32 s62, s61, 3\n"
"v_xor_b32 v46, s62, v44\n"
"v_lshl_add_u32 v7, v46, 5, v45\n"
"s_mov_b32 s68, 0x3e0293ee\n"
"s_mov_b32 s69, 0x3e0293ee\n"
"v_lshrrev_b32 v42, 1, v41\n"
"s_lshl_b32 s61, s66, 3\n"
"v_or_b32 v42, s61, v42\n"
"v_xor_b32 v42, v42, v40\n"
"v_lshlrev_b32 v42, 4, v42\n"
"v_lshl_add_u32 v42, v40, 8, v42\n"
"v_and_b32 v43, 1, v41\n"
"v_lshl_add_u32 v42, v43, 3, v42\n"
"s_mul_i32 s62, s67, 20480\n"
"s_add_u32 s62, s62, 20480\n"
"v_add_u32 v26, s62, v42\n"
"v_xor_b32 v42, v41, v40\n"
"v_lshlrev_b32 v42, 4, v42\n"
"v_lshl_add_u32 v42, v40, 8, v42\n"
"s_lshl_b32 s61, s66, 13\n"
"v_add_u32 v10, s61, v42\n"
"v_add_u32 v22, s62, v42\n"
"v_xor_b32 v11, 64, v10\n"
"v_xor_b32 v23, 64, v22\n"
"v_xor_b32 v27, 32, v26\n"
"v_xor_b32 v12, 128, v10\n"
"v_xor_b32 v24, 128, v22\n"
"v_xor_b32 v28, 64, v26\n"
"v_xor_b32 v13, 192, v10\n"
"v_xor_b32 v25, 192, v22\n"
"v_xor_b32 v29, 96, v26\n"
"v_add_u32 v14, 110592, v10\n"
"v_add_u32 v18, 61440, v10\n"
"v_add_u32 v15, 110592, v11\n"
"v_add_u32 v19, 61440, v11\n"
"v_add_u32 v16, 110592, v12\n"
"v_add_u32 v20, 61440, v12\n"
"v_add_u32 v17, 110592, v13\n"
"v_add_u32 v21, 61440, v13\n"
"s_mul_i32 s61, s54, 5120\n"
"v_lshl_add_u32 v30, v0, 4, s61\n"
"s_mul_i32 s61, s54, 640\n"
"s_add_u32 s61, s61, 159744\n"
"v_and_b32 v42, 31, v0\n"
"v_lshl_add_u32 v31, v42, 2, s61\n"
"s_lshl_b32 s61, s57, 21\n"
"s_lshl_b32 s62, s58, 8\n"
"s_add_u32 s61, s61, s62\n"
"s_mov_b64 s[36:37], s[12:13]\n"
"s_add_u32 s36, s36, s61\n"
"s_addc_u32 s37, s37, 0\n"
"s_and_b32 s37, s37, 0xffff\n"
"s_sub_u32 s38, 8388608, s61\n"
"s_mov_b32 s39, 0x20000\n"
"s_lshl_b32 s61, s55, 2\n"
"s_add_u32 s61, s61, s57\n"
"s_mul_i32 s61, s61, 327680\n"
"s_mov_b64 s[40:41], s[16:17]\n"
"s_add_u32 s61, s61, 1048576\n"
"s_add_u32 s40, s40, s61\n"
"s_addc_u32 s41, s41, 0\n"
"s_and_b32 s41, s41, 0xffff\n"
"s_mov_b32 s42, 327680\n"
"s_mov_b32 s43, 0x20000\n"
"s_lshl_b32 s61, s55, 3\n"
"s_add_u32 s61, s61, s58\n"
"s_mul_i32 s61, s61, 163840\n"
"s_mov_b64 s[44:45], s[16:17]\n"
"s_add_u32 s61, s61, 11534336\n"
"s_add_u32 s44, s44, s61\n"
"s_addc_u32 s45, s45, 0\n"
"s_and_b32 s45, s45, 0xffff\n"
"s_mov_b32 s46, 163840\n"
"s_mov_b32 s47, 0x20000\n"
"s_mul_i32 s61, s55, 327680\n"
"s_lshl_b32 s62, s58, 8\n"
"s_add_u32 s61, s61, s62\n"
"s_mov_b64 s[48:49], s[14:15]\n"
"s_add_u32 s48, s48, s61\n"
"s_addc_u32 s49, s49, 0\n"
"s_and_b32 s49, s49, 0xffff\n"
"s_sub_u32 s50, 327680, s62\n"
"s_mov_b32 s51, 0x20000\n"
"s_mov_b64 s[52:53], s[16:17]\n"
"s_lshl_b32 s61, s54, 16\n"
"s_lshl_b32 s62, s54, 12\n"
"s_add_u32 m0, s62, 20480\n"
"s_add_u32 s65, s61, 128\n"
"buffer_load_dwordx4 v60, s[20:23], s65 offen  lds\n"
"buffer_load_dwordx4 v61, s[20:23], s65 offen offset:1024  lds\n"
"buffer_load_dwordx4 v62, s[20:23], s65 offen offset:2048  lds\n"
"buffer_load_dwordx4 v63, s[20:23], s65 offen offset:3072  lds\n"
"s_lshl_b32 s61, s54, 14\n"
"s_lshl_b32 s62, s54, 10\n"
"s_add_u32 m0, s62, 36864\n"
"s_add_u32 s65, s61, 262272\n"
"buffer_load_dwordx4 v64, s[20:23], s65 offen  lds\n"
"v_mov_b32 v1, 162560\n"
"s_lshl_b32 s61, s54, 2\n"
"s_add_u32 s61, s61, 162576\n"
"v_mov_b32 v170, s61\n"
"v_mov_b32 v169, 1\n"
"s_waitcnt vmcnt(6)\n"
"s_barrier\n"
"ds_read_b128 v[96:99], v2 offset:0\n"
"ds_read_b64_tr_b16 v[136:137], v4 offset:0\n"
"ds_read_b64_tr_b16 v[138:139], v4 offset:1024\n"
"ds_read_b128 v[100:103], v2 offset:2048\n"
"ds_read_b128 v[104:107], v2 offset:4096\n"
"ds_read_b128 v[108:111], v2 offset:6144\n"
"ds_read_b128 v[112:115], v2 offset:8192\n"
"ds_read_b64_tr_b16 v[140:141], v5 offset:0\n"
"ds_read_b64_tr_b16 v[142:143], v5 offset:1024\n"
"ds_read_b64_tr_b16 v[144:145], v6 offset:0\n"
"ds_read_b64_tr_b16 v[146:147], v6 offset:1024\n"
"ds_read_b64_tr_b16 v[148:149], v7 offset:0\n"
"ds_read_b64_tr_b16 v[150:151], v7 offset:1024\n"
"s_waitcnt lgkmcnt(6)\n"
"v_mfma_f32_16x16x32_bf16 v[176:179], v[136:139], v[96:99], 0\n"
"ds_read_b128 v[116:119], v3 offset:0\n"
"v_mfma_f32_16x16x32_bf16 v[180:183], v[136:139], v[100:103], 0\n"
"ds_read_b64_tr_b16 v[152:153], v4 offset:8192\n"
"v_mfma_f32_16x16x32_bf16 v[184:187], v[136:139], v[104:107], 0\n"
"ds_read_b64_tr_b16 v[154:155], v4 offset:9216\n"
"v_mfma_f32_16x16x32_bf16 v[188:191], v[136:139], v[108:111], 0\n"
"ds_read_b128 v[120:123], v3 offset:2048\n"
"v_mfma_f32_16x16x32_bf16 v[192:195], v[136:139], v[112:115], 0\n"
"ds_read_b128 v[124:127], v3 offset:4096\n"
"s_waitcnt lgkmcnt(5)\n"
"v_mfma_f32_16x16x32_bf16 v[212:215], v[140:143], v[112:115], 0\n"
"ds_read_b128 v[128:131], v3 offset:6144\n"
"v_mfma_f32_16x16x32_bf16 v[208:211], v[140:143], v[108:111], 0\n"
"ds_read_b128 v[132:135], v3 offset:8192\n"
"v_mfma_f32_16x16x32_bf16 v[204:207], v[140:143], v[104:107], 0\n"
"ds_read_b64_tr_b16 v[156:157], v5 offset:8192\n"
"v_mfma_f32_16x16x32_bf16 v[200:203], v[140:143], v[100:103], 0\n"
"ds_read_b64_tr_b16 v[158:159], v5 offset:9216\n"
"v_mfma_f32_16x16x32_bf16 v[196:199], v[140:143], v[96:99], 0\n"
"ds_read_b64_tr_b16 v[160:161], v6 offset:8192\n"
"v_mfma_f32_16x16x32_bf16 v[216:219], v[144:147], v[96:99], 0\n"
"ds_read_b64_tr_b16 v[162:163], v6 offset:9216\n"
"v_mfma_f32_16x16x32_bf16 v[220:223], v[144:147], v[100:103], 0\n"
"ds_read_b64_tr_b16 v[164:165], v7 offset:8192\n"
"v_mfma_f32_16x16x32_bf16 v[224:227], v[144:147], v[104:107], 0\n"
"ds_read_b64_tr_b16 v[166:167], v7 offset:9216\n"
"v_mfma_f32_16x16x32_bf16 v[228:231], v[144:147], v[108:111], 0\n"
"v_mfma_f32_16x16x32_bf16 v[232:235], v[144:147], v[112:115], 0\n"
"v_mfma_f32_16x16x32_bf16 v[252:255], v[148:151], v[112:115], 0\n"
"v_mfma_f32_16x16x32_bf16 v[248:251], v[148:151], v[108:111], 0\n"
"v_mfma_f32_16x16x32_bf16 v[244:247], v[148:151], v[104:107], 0\n"
"v_mfma_f32_16x16x32_bf16 v[240:243], v[148:151], v[100:103], 0\n"
"v_mfma_f32_16x16x32_bf16 v[236:239], v[148:151], v[96:99], 0\n"
"s_waitcnt lgkmcnt(6)\n"
"v_mfma_f32_16x16x32_bf16 v[176:179], v[152:155], v[116:119], v[176:179]\n"
"v_mfma_f32_16x16x32_bf16 v[180:183], v[152:155], v[120:123], v[180:183]\n"
"v_mfma_f32_16x16x32_bf16 v[184:187], v[152:155], v[124:127], v[184:187]\n"
"s_waitcnt vmcnt(0)\n"
"s_waitcnt lgkmcnt(0)\n"
"s_barrier\n"
"v_mfma_f32_16x16x32_bf16 v[188:191], v[152:155], v[128:131], v[188:191]\n"
"ds_read_b128 v[96:99], v2 offset:20480\n"
"v_mfma_f32_16x16x32_bf16 v[192:195], v[152:155], v[132:135], v[192:195]\n"
"ds_read_b64_tr_b16 v[136:137], v4 offset:16384\n"
"v_mfma_f32_16x16x32_bf16 v[212:215], v[156:159], v[132:135], v[212:215]\n"
"ds_read_b64_tr_b16 v[138:139], v4 offset:17408\n"
"v_mfma_f32_16x16x32_bf16 v[208:211], v[156:159], v[128:131], v[208:211]\n"
"ds_read_b128 v[100:103], v2 offset:22528\n"
"v_mfma_f32_16x16x32_bf16 v[204:207], v[156:159], v[124:127], v[204:207]\n"
"ds_read_b128 v[104:107], v2 offset:24576\n"
"v_mfma_f32_16x16x32_bf16 v[200:203], v[156:159], v[120:123], v[200:203]\n"
"ds_read_b128 v[108:111], v2 offset:26624\n"
"v_mfma_f32_16x16x32_bf16 v[196:199], v[156:159], v[116:119], v[196:199]\n"
"ds_read_b128 v[112:115], v2 offset:28672\n"
"v_mfma_f32_16x16x32_bf16 v[216:219], v[160:163], v[116:119], v[216:219]\n"
"ds_read_b64_tr_b16 v[140:141], v5 offset:16384\n"
"v_mfma_f32_16x16x32_bf16 v[220:223], v[160:163], v[120:123], v[220:223]\n"
"ds_read_b64_tr_b16 v[142:143], v5 offset:17408\n"
"v_mfma_f32_16x16x32_bf16 v[224:227], v[160:163], v[124:127], v[224:227]\n"
"ds_read_b64_tr_b16 v[144:145], v6 offset:16384\n"
"v_mfma_f32_16x16x32_bf16 v[228:231], v[160:163], v[128:131], v[228:231]\n"
"ds_read_b64_tr_b16 v[146:147], v6 offset:17408\n"
"v_mfma_f32_16x16x32_bf16 v[232:235], v[160:163], v[132:135], v[232:235]\n"
"ds_read_b64_tr_b16 v[148:149], v7 offset:16384\n"
"v_mfma_f32_16x16x32_bf16 v[252:255], v[164:167], v[132:135], v[252:255]\n"
"ds_read_b64_tr_b16 v[150:151], v7 offset:17408\n"
"v_mfma_f32_16x16x32_bf16 v[248:251], v[164:167], v[128:131], v[248:251]\n"
"v_mfma_f32_16x16x32_bf16 v[244:247], v[164:167], v[124:127], v[244:247]\n"
"v_mfma_f32_16x16x32_bf16 v[240:243], v[164:167], v[120:123], v[240:243]\n"
"v_mfma_f32_16x16x32_bf16 v[236:239], v[164:167], v[116:119], v[236:239]\n"
"s_waitcnt lgkmcnt(6)\n"
"v_mfma_f32_16x16x32_bf16 v[176:179], v[136:139], v[96:99], v[176:179]\n"
"ds_read_b128 v[116:119], v3 offset:20480\n"
"v_mfma_f32_16x16x32_bf16 v[180:183], v[136:139], v[100:103], v[180:183]\n"
"ds_read_b64_tr_b16 v[152:153], v4 offset:24576\n"
"v_mfma_f32_16x16x32_bf16 v[184:187], v[136:139], v[104:107], v[184:187]\n"
"ds_read_b64_tr_b16 v[154:155], v4 offset:25600\n"
"v_mfma_f32_16x16x32_bf16 v[188:191], v[136:139], v[108:111], v[188:191]\n"
"ds_read_b128 v[120:123], v3 offset:22528\n"
"v_mfma_f32_16x16x32_bf16 v[192:195], v[136:139], v[112:115], v[192:195]\n"
"ds_read_b128 v[124:127], v3 offset:24576\n"
"s_waitcnt lgkmcnt(5)\n"
"v_mfma_f32_16x16x32_bf16 v[212:215], v[140:143], v[112:115], v[212:215]\n"
"ds_read_b128 v[128:131], v3 offset:26624\n"
"v_mfma_f32_16x16x32_bf16 v[208:211], v[140:143], v[108:111], v[208:211]\n"
"ds_read_b128 v[132:135], v3 offset:28672\n"
"v_mfma_f32_16x16x32_bf16 v[204:207], v[140:143], v[104:107], v[204:207]\n"
"ds_read_b64_tr_b16 v[156:157], v5 offset:24576\n"
"v_mfma_f32_16x16x32_bf16 v[200:203], v[140:143], v[100:103], v[200:203]\n"
"ds_read_b64_tr_b16 v[158:159], v5 offset:25600\n"
"v_mfma_f32_16x16x32_bf16 v[196:199], v[140:143], v[96:99], v[196:199]\n"
"ds_read_b64_tr_b16 v[160:161], v6 offset:24576\n"
"v_mfma_f32_16x16x32_bf16 v[216:219], v[144:147], v[96:99], v[216:219]\n"
"ds_read_b64_tr_b16 v[162:163], v6 offset:25600\n"
"v_mfma_f32_16x16x32_bf16 v[220:223], v[144:147], v[100:103], v[220:223]\n"
"ds_read_b64_tr_b16 v[164:165], v7 offset:24576\n"
"v_mfma_f32_16x16x32_bf16 v[224:227], v[144:147], v[104:107], v[224:227]\n"
"ds_read_b64_tr_b16 v[166:167], v7 offset:25600\n"
"v_mfma_f32_16x16x32_bf16 v[228:231], v[144:147], v[108:111], v[228:231]\n"
"v_mfma_f32_16x16x32_bf16 v[232:235], v[144:147], v[112:115], v[232:235]\n"
"v_mfma_f32_16x16x32_bf16 v[252:255], v[148:151], v[112:115], v[252:255]\n"
"v_mfma_f32_16x16x32_bf16 v[248:251], v[148:151], v[108:111], v[248:251]\n"
"v_mfma_f32_16x16x32_bf16 v[244:247], v[148:151], v[104:107], v[244:247]\n"
"v_mfma_f32_16x16x32_bf16 v[240:243], v[148:151], v[100:103], v[240:243]\n"
"v_mfma_f32_16x16x32_bf16 v[236:239], v[148:151], v[96:99], v[236:239]\n"
"s_waitcnt lgkmcnt(6)\n"
"v_mfma_f32_16x16x32_bf16 v[176:179], v[152:155], v[116:119], v[176:179]\n"
"v_mfma_f32_16x16x32_bf16 v[180:183], v[152:155], v[120:123], v[180:183]\n"
"v_mfma_f32_16x16x32_bf16 v[184:187], v[152:155], v[124:127], v[184:187]\n"
"s_waitcnt lgkmcnt(0)\n"
"s_barrier\n"
"v_mfma_f32_16x16x32_bf16 v[188:191], v[152:155], v[128:131], v[188:191]\n"
"ds_read_b128 v[96:99], v2 offset:40960\n"
"v_mfma_f32_16x16x32_bf16 v[192:195], v[152:155], v[132:135], v[192:195]\n"
"ds_read_b64_tr_b16 v[136:137], v4 offset:32768\n"
"v_mfma_f32_16x16x32_bf16 v[212:215], v[156:159], v[132:135], v[212:215]\n"
"ds_read_b64_tr_b16 v[138:139], v4 offset:33792\n"
"v_mfma_f32_16x16x32_bf16 v[208:211], v[156:159], v[128:131], v[208:211]\n"
"ds_read_b128 v[100:103], v2 offset:43008\n"
"v_mfma_f32_16x16x32_bf16 v[204:207], v[156:159], v[124:127], v[204:207]\n"
"ds_read_b128 v[104:107], v2 offset:45056\n"
"v_mfma_f32_16x16x32_bf16 v[200:203], v[156:159], v[120:123], v[200:203]\n"
"ds_read_b128 v[108:111], v2 offset:47104\n"
"v_mfma_f32_16x16x32_bf16 v[196:199], v[156:159], v[116:119], v[196:199]\n"
"ds_read_b128 v[112:115], v2 offset:49152\n"
"v_mfma_f32_16x16x32_bf16 v[216:219], v[160:163], v[116:119], v[216:219]\n"
"ds_read_b64_tr_b16 v[140:141], v5 offset:32768\n"
"v_mfma_f32_16x16x32_bf16 v[220:223], v[160:163], v[120:123], v[220:223]\n"
"ds_read_b64_tr_b16 v[142:143], v5 offset:33792\n"
"v_mfma_f32_16x16x32_bf16 v[224:227], v[160:163], v[124:127], v[224:227]\n"
"ds_read_b64_tr_b16 v[144:145], v6 offset:32768\n"
"v_mfma_f32_16x16x32_bf16 v[228:231], v[160:163], v[128:131], v[228:231]\n"
"ds_read_b64_tr_b16 v[146:147], v6 offset:33792\n"
"v_mfma_f32_16x16x32_bf16 v[232:235], v[160:163], v[132:135], v[232:235]\n"
"ds_read_b64_tr_b16 v[148:149], v7 offset:32768\n"
"v_mfma_f32_16x16x32_bf16 v[252:255], v[164:167], v[132:135], v[252:255]\n"
"ds_read_b64_tr_b16 v[150:151], v7 offset:33792\n"
"v_mfma_f32_16x16x32_bf16 v[248:251], v[164:167], v[128:131], v[248:251]\n"
"v_mfma_f32_16x16x32_bf16 v[244:247], v[164:167], v[124:127], v[244:247]\n"
"v_mfma_f32_16x16x32_bf16 v[240:243], v[164:167], v[120:123], v[240:243]\n"
"v_mfma_f32_16x16x32_bf16 v[236:239], v[164:167], v[116:119], v[236:239]\n"
"s_waitcnt lgkmcnt(6)\n"
"v_mfma_f32_16x16x32_bf16 v[176:179], v[136:139], v[96:99], v[176:179]\n"
"ds_read_b128 v[116:119], v3 offset:40960\n"
"v_mfma_f32_16x16x32_bf16 v[180:183], v[136:139], v[100:103], v[180:183]\n"
"ds_read_b64_tr_b16 v[152:153], v4 offset:40960\n"
"v_mfma_f32_16x16x32_bf16 v[184:187], v[136:139], v[104:107], v[184:187]\n"
"ds_read_b64_tr_b16 v[154:155], v4 offset:41984\n"
"v_mfma_f32_16x16x32_bf16 v[188:191], v[136:139], v[108:111], v[188:191]\n"
"ds_read_b128 v[120:123], v3 offset:43008\n"
"v_mfma_f32_16x16x32_bf16 v[192:195], v[136:139], v[112:115], v[192:195]\n"
"ds_read_b128 v[124:127], v3 offset:45056\n"
"s_waitcnt lgkmcnt(5)\n"
"v_mfma_f32_16x16x32_bf16 v[212:215], v[140:143], v[112:115], v[212:215]\n"
"ds_read_b128 v[128:131], v3 offset:47104\n"
"v_mfma_f32_16x16x32_bf16 v[208:211], v[140:143], v[108:111], v[208:211]\n"
"ds_read_b128 v[132:135], v3 offset:49152\n"
"v_mfma_f32_16x16x32_bf16 v[204:207], v[140:143], v[104:107], v[204:207]\n"
"ds_read_b64_tr_b16 v[156:157], v5 offset:40960\n"
"v_mfma_f32_16x16x32_bf16 v[200:203], v[140:143], v[100:103], v[200:203]\n"
"ds_read_b64_tr_b16 v[158:159], v5 offset:41984\n"
"v_mfma_f32_16x16x32_bf16 v[196:199], v[140:143], v[96:99], v[196:199]\n"
"ds_read_b64_tr_b16 v[160:161], v6 offset:40960\n"
"v_mfma_f32_16x16x32_bf16 v[216:219], v[144:147], v[96:99], v[216:219]\n"
"ds_read_b64_tr_b16 v[162:163], v6 offset:41984\n"
"v_mfma_f32_16x16x32_bf16 v[220:223], v[144:147], v[100:103], v[220:223]\n"
"ds_read_b64_tr_b16 v[164:165], v7 offset:40960\n"
"v_mfma_f32_16x16x32_bf16 v[224:227], v[144:147], v[104:107], v[224:227]\n"
"ds_read_b64_tr_b16 v[166:167], v7 offset:41984\n"
"v_mfma_f32_16x16x32_bf16 v[228:231], v[144:147], v[108:111], v[228:231]\n"
"v_mfma_f32_16x16x32_bf16 v[232:235], v[144:147], v[112:115], v[232:235]\n"
"v_mfma_f32_16x16x32_bf16 v[252:255], v[148:151], v[112:115], v[252:255]\n"
"v_mfma_f32_16x16x32_bf16 v[248:251], v[148:151], v[108:111], v[248:251]\n"
"v_mfma_f32_16x16x32_bf16 v[244:247], v[148:151], v[104:107], v[244:247]\n"
"v_mfma_f32_16x16x32_bf16 v[240:243], v[148:151], v[100:103], v[240:243]\n"
"v_mfma_f32_16x16x32_bf16 v[236:239], v[148:151], v[96:99], v[236:239]\n"
"s_waitcnt lgkmcnt(6)\n"
"v_mfma_f32_16x16x32_bf16 v[176:179], v[152:155], v[116:119], v[176:179]\n"
"v_mfma_f32_16x16x32_bf16 v[180:183], v[152:155], v[120:123], v[180:183]\n"
"v_mfma_f32_16x16x32_bf16 v[184:187], v[152:155], v[124:127], v[184:187]\n"
"s_waitcnt lgkmcnt(0)\n"
"s_barrier\n"
"v_mfma_f32_16x16x32_bf16 v[188:191], v[152:155], v[128:131], v[188:191]\n"
"ds_read_b128 v[96:99], v2 offset:0\n"
"v_mfma_f32_16x16x32_bf16 v[192:195], v[152:155], v[132:135], v[192:195]\n"
"ds_read_b64_tr_b16 v[136:137], v4 offset:0\n"
"v_mfma_f32_16x16x32_bf16 v[212:215], v[156:159], v[132:135], v[212:215]\n"
"ds_read_b64_tr_b16 v[138:139], v4 offset:1024\n"
"v_mfma_f32_16x16x32_bf16 v[208:211], v[156:159], v[128:131], v[208:211]\n"
"ds_read_b128 v[100:103], v2 offset:2048\n"
"v_mfma_f32_16x16x32_bf16 v[204:207], v[156:159], v[124:127], v[204:207]\n"
"ds_read_b128 v[104:107], v2 offset:4096\n"
"v_mfma_f32_16x16x32_bf16 v[200:203], v[156:159], v[120:123], v[200:203]\n"
"ds_read_b128 v[108:111], v2 offset:6144\n"
"v_mfma_f32_16x16x32_bf16 v[196:199], v[156:159], v[116:119], v[196:199]\n"
"ds_read_b128 v[112:115], v2 offset:8192\n"
"v_mfma_f32_16x16x32_bf16 v[216:219], v[160:163], v[116:119], v[216:219]\n"
"ds_read_b64_tr_b16 v[140:141], v5 offset:0\n"
"v_mfma_f32_16x16x32_bf16 v[220:223], v[160:163], v[120:123], v[220:223]\n"
"ds_read_b64_tr_b16 v[142:143], v5 offset:1024\n"
"v_mfma_f32_16x16x32_bf16 v[224:227], v[160:163], v[124:127], v[224:227]\n"
"ds_read_b64_tr_b16 v[144:145], v6 offset:0\n"
"v_mfma_f32_16x16x32_bf16 v[228:231], v[160:163], v[128:131], v[228:231]\n"
"ds_read_b64_tr_b16 v[146:147], v6 offset:1024\n"
"v_mfma_f32_16x16x32_bf16 v[232:235], v[160:163], v[132:135], v[232:235]\n"
"ds_read_b64_tr_b16 v[148:149], v7 offset:0\n"
"v_mfma_f32_16x16x32_bf16 v[252:255], v[164:167], v[132:135], v[252:255]\n"
"ds_read_b64_tr_b16 v[150:151], v7 offset:1024\n"
"v_mfma_f32_16x16x32_bf16 v[248:251], v[164:167], v[128:131], v[248:251]\n"
"v_mfma_f32_16x16x32_bf16 v[244:247], v[164:167], v[124:127], v[244:247]\n"
"v_mfma_f32_16x16x32_bf16 v[240:243], v[164:167], v[120:123], v[240:243]\n"
"v_mfma_f32_16x16x32_bf16 v[236:239], v[164:167], v[116:119], v[236:239]\n"
"s_waitcnt lgkmcnt(6)\n"
"v_mfma_f32_16x16x32_bf16 v[176:179], v[136:139], v[96:99], v[176:179]\n"
"ds_read_b128 v[116:119], v3 offset:0\n"
"v_mfma_f32_16x16x32_bf16 v[180:183], v[136:139], v[100:103], v[180:183]\n"
"ds_read_b64_tr_b16 v[152:153], v4 offset:8192\n"
"v_mfma_f32_16x16x32_bf16 v[184:187], v[136:139], v[104:107], v[184:187]\n"
"ds_read_b64_tr_b16 v[154:155], v4 offset:9216\n"
"v_mfma_f32_16x16x32_bf16 v[188:191], v[136:139], v[108:111], v[188:191]\n"
"ds_read_b128 v[120:123], v3 offset:2048\n"
"v_mfma_f32_16x16x32_bf16 v[192:195], v[136:139], v[112:115], v[192:195]\n"
"ds_read_b128 v[124:127], v3 offset:4096\n"
"s_waitcnt lgkmcnt(5)\n"
"v_mfma_f32_16x16x32_bf16 v[212:215], v[140:143], v[112:115], v[212:215]\n"
"ds_read_b128 v[128:131], v3 offset:6144\n"
"v_mfma_f32_16x16x32_bf16 v[208:211], v[140:143], v[108:111], v[208:211]\n"
"ds_read_b128 v[132:135], v3 offset:8192\n"
"v_mfma_f32_16x16x32_bf16 v[204:207], v[140:143], v[104:107], v[204:207]\n"
"ds_read_b64_tr_b16 v[156:157], v5 offset:8192\n"
"v_mfma_f32_16x16x32_bf16 v[200:203], v[140:143], v[100:103], v[200:203]\n"
"ds_read_b64_tr_b16 v[158:159], v5 offset:9216\n"
"v_mfma_f32_16x16x32_bf16 v[196:199], v[140:143], v[96:99], v[196:199]\n"
"ds_read_b64_tr_b16 v[160:161], v6 offset:8192\n"
"v_mfma_f32_16x16x32_bf16 v[216:219], v[144:147], v[96:99], v[216:219]\n"
"ds_read_b64_tr_b16 v[162:163], v6 offset:9216\n"
"v_mfma_f32_16x16x32_bf16 v[220:223], v[144:147], v[100:103], v[220:223]\n"
"ds_read_b64_tr_b16 v[164:165], v7 offset:8192\n"
"v_mfma_f32_16x16x32_bf16 v[224:227], v[144:147], v[104:107], v[224:227]\n"
"ds_read_b64_tr_b16 v[166:167], v7 offset:9216\n"
"v_mfma_f32_16x16x32_bf16 v[228:231], v[144:147], v[108:111], v[228:231]\n"
"v_mfma_f32_16x16x32_bf16 v[232:235], v[144:147], v[112:115], v[232:235]\n"
"v_mfma_f32_16x16x32_bf16 v[252:255], v[148:151], v[112:115], v[252:255]\n"
"v_mfma_f32_16x16x32_bf16 v[248:251], v[148:151], v[108:111], v[248:251]\n"
"v_mfma_f32_16x16x32_bf16 v[244:247], v[148:151], v[104:107], v[244:247]\n"
"v_mfma_f32_16x16x32_bf16 v[240:243], v[148:151], v[100:103], v[240:243]\n"
"v_mfma_f32_16x16x32_bf16 v[236:239], v[148:151], v[96:99], v[236:239]\n"
"s_waitcnt lgkmcnt(6)\n"
"v_mfma_f32_16x16x32_bf16 v[176:179], v[152:155], v[116:119], v[176:179]\n"
"v_mfma_f32_16x16x32_bf16 v[180:183], v[152:155], v[120:123], v[180:183]\n"
"v_mfma_f32_16x16x32_bf16 v[184:187], v[152:155], v[124:127], v[184:187]\n"
"s_waitcnt lgkmcnt(0)\n"
"s_barrier\n"
"v_mfma_f32_16x16x32_bf16 v[188:191], v[152:155], v[128:131], v[188:191]\n"
"ds_read_b128 v[96:99], v2 offset:20480\n"
"v_mfma_f32_16x16x32_bf16 v[192:195], v[152:155], v[132:135], v[192:195]\n"
"ds_read_b64_tr_b16 v[136:137], v4 offset:16384\n"
"v_mfma_f32_16x16x32_bf16 v[212:215], v[156:159], v[132:135], v[212:215]\n"
"ds_read_b64_tr_b16 v[138:139], v4 offset:17408\n"
"v_mfma_f32_16x16x32_bf16 v[208:211], v[156:159], v[128:131], v[208:211]\n"
"ds_read_b128 v[100:103], v2 offset:22528\n"
"v_mfma_f32_16x16x32_bf16 v[204:207], v[156:159], v[124:127], v[204:207]\n"
"ds_read_b128 v[104:107], v2 offset:24576\n"
"v_mfma_f32_16x16x32_bf16 v[200:203], v[156:159], v[120:123], v[200:203]\n"
"ds_read_b128 v[108:111], v2 offset:26624\n"
"v_mfma_f32_16x16x32_bf16 v[196:199], v[156:159], v[116:119], v[196:199]\n"
"ds_read_b128 v[112:115], v2 offset:28672\n"
"v_mfma_f32_16x16x32_bf16 v[216:219], v[160:163], v[116:119], v[216:219]\n"
"ds_read_b64_tr_b16 v[140:141], v5 offset:16384\n"
"v_mfma_f32_16x16x32_bf16 v[220:223], v[160:163], v[120:123], v[220:223]\n"
"ds_read_b64_tr_b16 v[142:143], v5 offset:17408\n"
"v_mfma_f32_16x16x32_bf16 v[224:227], v[160:163], v[124:127], v[224:227]\n"
"ds_read_b64_tr_b16 v[144:145], v6 offset:16384\n"
"v_mfma_f32_16x16x32_bf16 v[228:231], v[160:163], v[128:131], v[228:231]\n"
"ds_read_b64_tr_b16 v[146:147], v6 offset:17408\n"
"v_mfma_f32_16x16x32_bf16 v[232:235], v[160:163], v[132:135], v[232:235]\n"
"ds_read_b64_tr_b16 v[148:149], v7 offset:16384\n"
"v_mfma_f32_16x16x32_bf16 v[252:255], v[164:167], v[132:135], v[252:255]\n"
"ds_read_b64_tr_b16 v[150:151], v7 offset:17408\n"
"v_mfma_f32_16x16x32_bf16 v[248:251], v[164:167], v[128:131], v[248:251]\n"
"v_mfma_f32_16x16x32_bf16 v[244:247], v[164:167], v[124:127], v[244:247]\n"
"v_mfma_f32_16x16x32_bf16 v[240:243], v[164:167], v[120:123], v[240:243]\n"
"v_mfma_f32_16x16x32_bf16 v[236:239], v[164:167], v[116:119], v[236:239]\n"
"s_waitcnt lgkmcnt(6)\n"
"v_mfma_f32_16x16x32_bf16 v[176:179], v[136:139], v[96:99], v[176:179]\n"
"ds_read_b128 v[116:119], v3 offset:20480\n"
"v_mfma_f32_16x16x32_bf16 v[180:183], v[136:139], v[100:103], v[180:183]\n"
"ds_read_b64_tr_b16 v[152:153], v4 offset:24576\n"
"v_mfma_f32_16x16x32_bf16 v[184:187], v[136:139], v[104:107], v[184:187]\n"
"ds_read_b64_tr_b16 v[154:155], v4 offset:25600\n"
"v_mfma_f32_16x16x32_bf16 v[188:191], v[136:139], v[108:111], v[188:191]\n"
"ds_read_b128 v[120:123], v3 offset:22528\n"
"v_mfma_f32_16x16x32_bf16 v[192:195], v[136:139], v[112:115], v[192:195]\n"
"ds_read_b128 v[124:127], v3 offset:24576\n"
"s_waitcnt lgkmcnt(5)\n"
"v_mfma_f32_16x16x32_bf16 v[212:215], v[140:143], v[112:115], v[212:215]\n"
"ds_read_b128 v[128:131], v3 offset:26624\n"
"v_mfma_f32_16x16x32_bf16 v[208:211], v[140:143], v[108:111], v[208:211]\n"
"ds_read_b128 v[132:135], v3 offset:28672\n"
"v_mfma_f32_16x16x32_bf16 v[204:207], v[140:143], v[104:107], v[204:207]\n"
"ds_read_b64_tr_b16 v[156:157], v5 offset:24576\n"
"v_mfma_f32_16x16x32_bf16 v[200:203], v[140:143], v[100:103], v[200:203]\n"
"ds_read_b64_tr_b16 v[158:159], v5 offset:25600\n"
"v_mfma_f32_16x16x32_bf16 v[196:199], v[140:143], v[96:99], v[196:199]\n"
"ds_read_b64_tr_b16 v[160:161], v6 offset:24576\n"
"v_mfma_f32_16x16x32_bf16 v[216:219], v[144:147], v[96:99], v[216:219]\n"
"ds_read_b64_tr_b16 v[162:163], v6 offset:25600\n"
"v_mfma_f32_16x16x32_bf16 v[220:223], v[144:147], v[100:103], v[220:223]\n"
"ds_read_b64_tr_b16 v[164:165], v7 offset:24576\n"
"v_mfma_f32_16x16x32_bf16 v[224:227], v[144:147], v[104:107], v[224:227]\n"
"ds_read_b64_tr_b16 v[166:167], v7 offset:25600\n"
"v_mfma_f32_16x16x32_bf16 v[228:231], v[144:147], v[108:111], v[228:231]\n"
"v_mfma_f32_16x16x32_bf16 v[232:235], v[144:147], v[112:115], v[232:235]\n"
"v_mfma_f32_16x16x32_bf16 v[252:255], v[148:151], v[112:115], v[252:255]\n"
"v_mfma_f32_16x16x32_bf16 v[248:251], v[148:151], v[108:111], v[248:251]\n"
"v_mfma_f32_16x16x32_bf16 v[244:247], v[148:151], v[104:107], v[244:247]\n"
"v_mfma_f32_16x16x32_bf16 v[240:243], v[148:151], v[100:103], v[240:243]\n"
"v_mfma_f32_16x16x32_bf16 v[236:239], v[148:151], v[96:99], v[236:239]\n"
"s_waitcnt lgkmcnt(6)\n"
"v_mfma_f32_16x16x32_bf16 v[176:179], v[152:155], v[116:119], v[176:179]\n"
"v_mfma_f32_16x16x32_bf16 v[180:183], v[152:155], v[120:123], v[180:183]\n"
"v_mfma_f32_16x16x32_bf16 v[184:187], v[152:155], v[124:127], v[184:187]\n"
"s_waitcnt lgkmcnt(0)\n"
"s_barrier\n"
"v_mfma_f32_16x16x32_bf16 v[188:191], v[152:155], v[128:131], v[188:191]\n"
"ds_read_b128 v[96:99], v2 offset:40960\n"
"v_mfma_f32_16x16x32_bf16 v[192:195], v[152:155], v[132:135], v[192:195]\n"
"ds_read_b64_tr_b16 v[136:137], v4 offset:32768\n"
"v_mfma_f32_16x16x32_bf16 v[212:215], v[156:159], v[132:135], v[212:215]\n"
"ds_read_b64_tr_b16 v[138:139], v4 offset:33792\n"
"v_mfma_f32_16x16x32_bf16 v[208:211], v[156:159], v[128:131], v[208:211]\n"
"ds_read_b128 v[100:103], v2 offset:43008\n"
"v_mfma_f32_16x16x32_bf16 v[204:207], v[156:159], v[124:127], v[204:207]\n"
"ds_read_b128 v[104:107], v2 offset:45056\n"
"v_mfma_f32_16x16x32_bf16 v[200:203], v[156:159], v[120:123], v[200:203]\n"
"ds_read_b128 v[108:111], v2 offset:47104\n"
"v_mfma_f32_16x16x32_bf16 v[196:199], v[156:159], v[116:119], v[196:199]\n"
"ds_read_b128 v[112:115], v2 offset:49152\n"
"v_mfma_f32_16x16x32_bf16 v[216:219], v[160:163], v[116:119], v[216:219]\n"
"ds_read_b64_tr_b16 v[140:141], v5 offset:32768\n"
"v_mfma_f32_16x16x32_bf16 v[220:223], v[160:163], v[120:123], v[220:223]\n"
"ds_read_b64_tr_b16 v[142:143], v5 offset:33792\n"
"v_mfma_f32_16x16x32_bf16 v[224:227], v[160:163], v[124:127], v[224:227]\n"
"ds_read_b64_tr_b16 v[144:145], v6 offset:32768\n"
"v_mfma_f32_16x16x32_bf16 v[228:231], v[160:163], v[128:131], v[228:231]\n"
"ds_read_b64_tr_b16 v[146:147], v6 offset:33792\n"
"v_mfma_f32_16x16x32_bf16 v[232:235], v[160:163], v[132:135], v[232:235]\n"
"ds_read_b64_tr_b16 v[148:149], v7 offset:32768\n"
"v_mfma_f32_16x16x32_bf16 v[252:255], v[164:167], v[132:135], v[252:255]\n"
"ds_read_b64_tr_b16 v[150:151], v7 offset:33792\n"
"v_mfma_f32_16x16x32_bf16 v[248:251], v[164:167], v[128:131], v[248:251]\n"
"v_mfma_f32_16x16x32_bf16 v[244:247], v[164:167], v[124:127], v[244:247]\n"
"v_mfma_f32_16x16x32_bf16 v[240:243], v[164:167], v[120:123], v[240:243]\n"
"v_mfma_f32_16x16x32_bf16 v[236:239], v[164:167], v[116:119], v[236:239]\n"
"s_waitcnt lgkmcnt(6)\n"
"v_mfma_f32_16x16x32_bf16 v[176:179], v[136:139], v[96:99], v[176:179]\n"
"ds_read_b128 v[116:119], v3 offset:40960\n"
"v_mfma_f32_16x16x32_bf16 v[180:183], v[136:139], v[100:103], v[180:183]\n"
"ds_read_b64_tr_b16 v[152:153], v4 offset:40960\n"
"v_mfma_f32_16x16x32_bf16 v[184:187], v[136:139], v[104:107], v[184:187]\n"
"ds_read_b64_tr_b16 v[154:155], v4 offset:41984\n"
"v_mfma_f32_16x16x32_bf16 v[188:191], v[136:139], v[108:111], v[188:191]\n"
"ds_read_b128 v[120:123], v3 offset:43008\n"
"v_mfma_f32_16x16x32_bf16 v[192:195], v[136:139], v[112:115], v[192:195]\n"
"ds_read_b128 v[124:127], v3 offset:45056\n"
"s_waitcnt lgkmcnt(5)\n"
"v_mfma_f32_16x16x32_bf16 v[212:215], v[140:143], v[112:115], v[212:215]\n"
"ds_read_b128 v[128:131], v3 offset:47104\n"
"v_mfma_f32_16x16x32_bf16 v[208:211], v[140:143], v[108:111], v[208:211]\n"
"ds_read_b128 v[132:135], v3 offset:49152\n"
"v_mfma_f32_16x16x32_bf16 v[204:207], v[140:143], v[104:107], v[204:207]\n"
"ds_read_b64_tr_b16 v[156:157], v5 offset:40960\n"
"v_mfma_f32_16x16x32_bf16 v[200:203], v[140:143], v[100:103], v[200:203]\n"
"ds_read_b64_tr_b16 v[158:159], v5 offset:41984\n"
"v_mfma_f32_16x16x32_bf16 v[196:199], v[140:143], v[96:99], v[196:199]\n"
"ds_read_b64_tr_b16 v[160:161], v6 offset:40960\n"
"v_mfma_f32_16x16x32_bf16 v[216:219], v[144:147], v[96:99], v[216:219]\n"
"ds_read_b64_tr_b16 v[162:163], v6 offset:41984\n"
"v_mfma_f32_16x16x32_bf16 v[220:223], v[144:147], v[100:103], v[220:223]\n"
"ds_read_b64_tr_b16 v[164:165], v7 offset:40960\n"
"v_mfma_f32_16x16x32_bf16 v[224:227], v[144:147], v[104:107], v[224:227]\n"
"ds_read_b64_tr_b16 v[166:167], v7 offset:41984\n"
"v_mfma_f32_16x16x32_bf16 v[228:231], v[144:147], v[108:111], v[228:231]\n"
"v_mfma_f32_16x16x32_bf16 v[232:235], v[144:147], v[112:115], v[232:235]\n"
"v_mfma_f32_16x16x32_bf16 v[252:255], v[148:151], v[112:115], v[252:255]\n"
"v_mfma_f32_16x16x32_bf16 v[248:251], v[148:151], v[108:111], v[248:251]\n"
"v_mfma_f32_16x16x32_bf16 v[244:247], v[148:151], v[104:107], v[244:247]\n"
"v_mfma_f32_16x16x32_bf16 v[240:243], v[148:151], v[100:103], v[240:243]\n"
"v_mfma_f32_16x16x32_bf16 v[236:239], v[148:151], v[96:99], v[236:239]\n"
"s_waitcnt lgkmcnt(6)\n"
"v_mfma_f32_16x16x32_bf16 v[176:179], v[152:155], v[116:119], v[176:179]\n"
"v_mfma_f32_16x16x32_bf16 v[180:183], v[152:155], v[120:123], v[180:183]\n"
"v_mfma_f32_16x16x32_bf16 v[184:187], v[152:155], v[124:127], v[184:187]\n"
"s_waitcnt lgkmcnt(0)\n"
"s_barrier\n"
"v_mfma_f32_16x16x32_bf16 v[188:191], v[152:155], v[128:131], v[188:191]\n"
"ds_read_b128 v[96:99], v2 offset:0\n"
"v_mfma_f32_16x16x32_bf16 v[192:195], v[152:155], v[132:135], v[192:195]\n"
"ds_read_b64_tr_b16 v[136:137], v4 offset:0\n"
"v_mfma_f32_16x16x32_bf16 v[212:215], v[156:159], v[132:135], v[212:215]\n"
"ds_read_b64_tr_b16 v[138:139], v4 offset:1024\n"
"v_mfma_f32_16x16x32_bf16 v[208:211], v[156:159], v[128:131], v[208:211]\n"
"ds_read_b128 v[100:103], v2 offset:2048\n"
"v_mfma_f32_16x16x32_bf16 v[204:207], v[156:159], v[124:127], v[204:207]\n"
"ds_read_b128 v[104:107], v2 offset:4096\n"
"v_mfma_f32_16x16x32_bf16 v[200:203], v[156:159], v[120:123], v[200:203]\n"
"ds_read_b128 v[108:111], v2 offset:6144\n"
"v_mfma_f32_16x16x32_bf16 v[196:199], v[156:159], v[116:119], v[196:199]\n"
"ds_read_b128 v[112:115], v2 offset:8192\n"
"v_mfma_f32_16x16x32_bf16 v[216:219], v[160:163], v[116:119], v[216:219]\n"
"ds_read_b64_tr_b16 v[140:141], v5 offset:0\n"
"v_mfma_f32_16x16x32_bf16 v[220:223], v[160:163], v[120:123], v[220:223]\n"
"ds_read_b64_tr_b16 v[142:143], v5 offset:1024\n"
"v_mfma_f32_16x16x32_bf16 v[224:227], v[160:163], v[124:127], v[224:227]\n"
"ds_read_b64_tr_b16 v[144:145], v6 offset:0\n"
"v_mfma_f32_16x16x32_bf16 v[228:231], v[160:163], v[128:131], v[228:231]\n"
"ds_read_b64_tr_b16 v[146:147], v6 offset:1024\n"
"v_mfma_f32_16x16x32_bf16 v[232:235], v[160:163], v[132:135], v[232:235]\n"
"ds_read_b64_tr_b16 v[148:149], v7 offset:0\n"
"v_mfma_f32_16x16x32_bf16 v[252:255], v[164:167], v[132:135], v[252:255]\n"
"ds_read_b64_tr_b16 v[150:151], v7 offset:1024\n"
"v_mfma_f32_16x16x32_bf16 v[248:251], v[164:167], v[128:131], v[248:251]\n"
"v_mfma_f32_16x16x32_bf16 v[244:247], v[164:167], v[124:127], v[244:247]\n"
"v_mfma_f32_16x16x32_bf16 v[240:243], v[164:167], v[120:123], v[240:243]\n"
"v_mfma_f32_16x16x32_bf16 v[236:239], v[164:167], v[116:119], v[236:239]\n"
"s_waitcnt lgkmcnt(6)\n"
"v_mfma_f32_16x16x32_bf16 v[176:179], v[136:139], v[96:99], v[176:179]\n"
"ds_read_b128 v[116:119], v3 offset:0\n"
"v_mfma_f32_16x16x32_bf16 v[180:183], v[136:139], v[100:103], v[180:183]\n"
"ds_read_b64_tr_b16 v[152:153], v4 offset:8192\n"
"v_mfma_f32_16x16x32_bf16 v[184:187], v[136:139], v[104:107], v[184:187]\n"
"ds_read_b64_tr_b16 v[154:155], v4 offset:9216\n"
"v_mfma_f32_16x16x32_bf16 v[188:191], v[136:139], v[108:111], v[188:191]\n"
"ds_read_b128 v[120:123], v3 offset:2048\n"
"v_mfma_f32_16x16x32_bf16 v[192:195], v[136:139], v[112:115], v[192:195]\n"
"ds_read_b128 v[124:127], v3 offset:4096\n"
"s_waitcnt lgkmcnt(5)\n"
"v_mfma_f32_16x16x32_bf16 v[212:215], v[140:143], v[112:115], v[212:215]\n"
"ds_read_b128 v[128:131], v3 offset:6144\n"
"v_mfma_f32_16x16x32_bf16 v[208:211], v[140:143], v[108:111], v[208:211]\n"
"ds_read_b128 v[132:135], v3 offset:8192\n"
"v_mfma_f32_16x16x32_bf16 v[204:207], v[140:143], v[104:107], v[204:207]\n"
"ds_read_b64_tr_b16 v[156:157], v5 offset:8192\n"
"v_mfma_f32_16x16x32_bf16 v[200:203], v[140:143], v[100:103], v[200:203]\n"
"ds_read_b64_tr_b16 v[158:159], v5 offset:9216\n"
"v_mfma_f32_16x16x32_bf16 v[196:199], v[140:143], v[96:99], v[196:199]\n"
"ds_read_b64_tr_b16 v[160:161], v6 offset:8192\n"
"v_mfma_f32_16x16x32_bf16 v[216:219], v[144:147], v[96:99], v[216:219]\n"
"ds_read_b64_tr_b16 v[162:163], v6 offset:9216\n"
"v_mfma_f32_16x16x32_bf16 v[220:223], v[144:147], v[100:103], v[220:223]\n"
"ds_read_b64_tr_b16 v[164:165], v7 offset:8192\n"
"v_mfma_f32_16x16x32_bf16 v[224:227], v[144:147], v[104:107], v[224:227]\n"
"ds_read_b64_tr_b16 v[166:167], v7 offset:9216\n"
"v_mfma_f32_16x16x32_bf16 v[228:231], v[144:147], v[108:111], v[228:231]\n"
"v_mfma_f32_16x16x32_bf16 v[232:235], v[144:147], v[112:115], v[232:235]\n"
"v_mfma_f32_16x16x32_bf16 v[252:255], v[148:151], v[112:115], v[252:255]\n"
"v_mfma_f32_16x16x32_bf16 v[248:251], v[148:151], v[108:111], v[248:251]\n"
"v_mfma_f32_16x16x32_bf16 v[244:247], v[148:151], v[104:107], v[244:247]\n"
"v_mfma_f32_16x16x32_bf16 v[240:243], v[148:151], v[100:103], v[240:243]\n"
"v_mfma_f32_16x16x32_bf16 v[236:239], v[148:151], v[96:99], v[236:239]\n"
"s_waitcnt lgkmcnt(6)\n"
"v_mfma_f32_16x16x32_bf16 v[176:179], v[152:155], v[116:119], v[176:179]\n"
"v_mfma_f32_16x16x32_bf16 v[180:183], v[152:155], v[120:123], v[180:183]\n"
"v_mfma_f32_16x16x32_bf16 v[184:187], v[152:155], v[124:127], v[184:187]\n"
"s_waitcnt lgkmcnt(0)\n"
"s_barrier\n"
"v_mfma_f32_16x16x32_bf16 v[188:191], v[152:155], v[128:131], v[188:191]\n"
"ds_read_b128 v[96:99], v2 offset:20480\n"
"v_mfma_f32_16x16x32_bf16 v[192:195], v[152:155], v[132:135], v[192:195]\n"
"ds_read_b64_tr_b16 v[136:137], v4 offset:16384\n"
"v_mfma_f32_16x16x32_bf16 v[212:215], v[156:159], v[132:135], v[212:215]\n"
"ds_read_b64_tr_b16 v[138:139], v4 offset:17408\n"
"v_mfma_f32_16x16x32_bf16 v[208:211], v[156:159], v[128:131], v[208:211]\n"
"ds_read_b128 v[100:103], v2 offset:22528\n"
"v_mfma_f32_16x16x32_bf16 v[204:207], v[156:159], v[124:127], v[204:207]\n"
"ds_read_b128 v[104:107], v2 offset:24576\n"
"v_mfma_f32_16x16x32_bf16 v[200:203], v[156:159], v[120:123], v[200:203]\n"
"ds_read_b128 v[108:111], v2 offset:26624\n"
"v_mfma_f32_16x16x32_bf16 v[196:199], v[156:159], v[116:119], v[196:199]\n"
"ds_read_b128 v[112:115], v2 offset:28672\n"
"v_mfma_f32_16x16x32_bf16 v[216:219], v[160:163], v[116:119], v[216:219]\n"
"ds_read_b64_tr_b16 v[140:141], v5 offset:16384\n"
"v_mfma_f32_16x16x32_bf16 v[220:223], v[160:163], v[120:123], v[220:223]\n"
"ds_read_b64_tr_b16 v[142:143], v5 offset:17408\n"
"v_mfma_f32_16x16x32_bf16 v[224:227], v[160:163], v[124:127], v[224:227]\n"
"ds_read_b64_tr_b16 v[144:145], v6 offset:16384\n"
"v_mfma_f32_16x16x32_bf16 v[228:231], v[160:163], v[128:131], v[228:231]\n"
"ds_read_b64_tr_b16 v[146:147], v6 offset:17408\n"
"v_mfma_f32_16x16x32_bf16 v[232:235], v[160:163], v[132:135], v[232:235]\n"
"ds_read_b64_tr_b16 v[148:149], v7 offset:16384\n"
"v_mfma_f32_16x16x32_bf16 v[252:255], v[164:167], v[132:135], v[252:255]\n"
"ds_read_b64_tr_b16 v[150:151], v7 offset:17408\n"
"v_mfma_f32_16x16x32_bf16 v[248:251], v[164:167], v[128:131], v[248:251]\n"
"v_mfma_f32_16x16x32_bf16 v[244:247], v[164:167], v[124:127], v[244:247]\n"
"v_mfma_f32_16x16x32_bf16 v[240:243], v[164:167], v[120:123], v[240:243]\n"
"v_mfma_f32_16x16x32_bf16 v[236:239], v[164:167], v[116:119], v[236:239]\n"
"s_waitcnt lgkmcnt(6)\n"
"v_mfma_f32_16x16x32_bf16 v[176:179], v[136:139], v[96:99], v[176:179]\n"
"ds_read_b128 v[116:119], v3 offset:20480\n"
"v_mfma_f32_16x16x32_bf16 v[180:183], v[136:139], v[100:103], v[180:183]\n"
"ds_read_b64_tr_b16 v[152:153], v4 offset:24576\n"
"v_mfma_f32_16x16x32_bf16 v[184:187], v[136:139], v[104:107], v[184:187]\n"
"ds_read_b64_tr_b16 v[154:155], v4 offset:25600\n"
"v_mfma_f32_16x16x32_bf16 v[188:191], v[136:139], v[108:111], v[188:191]\n"
"ds_read_b128 v[120:123], v3 offset:22528\n"
"v_mfma_f32_16x16x32_bf16 v[192:195], v[136:139], v[112:115], v[192:195]\n"
"ds_read_b128 v[124:127], v3 offset:24576\n"
"s_waitcnt lgkmcnt(5)\n"
"v_mfma_f32_16x16x32_bf16 v[212:215], v[140:143], v[112:115], v[212:215]\n"
"ds_read_b128 v[128:131], v3 offset:26624\n"
"v_mfma_f32_16x16x32_bf16 v[208:211], v[140:143], v[108:111], v[208:211]\n"
"ds_read_b128 v[132:135], v3 offset:28672\n"
"v_mfma_f32_16x16x32_bf16 v[204:207], v[140:143], v[104:107], v[204:207]\n"
"ds_read_b64_tr_b16 v[156:157], v5 offset:24576\n"
"v_mfma_f32_16x16x32_bf16 v[200:203], v[140:143], v[100:103], v[200:203]\n"
"ds_read_b64_tr_b16 v[158:159], v5 offset:25600\n"
"v_mfma_f32_16x16x32_bf16 v[196:199], v[140:143], v[96:99], v[196:199]\n"
"ds_read_b64_tr_b16 v[160:161], v6 offset:24576\n"
"v_mfma_f32_16x16x32_bf16 v[216:219], v[144:147], v[96:99], v[216:219]\n"
"ds_read_b64_tr_b16 v[162:163], v6 offset:25600\n"
"v_mfma_f32_16x16x32_bf16 v[220:223], v[144:147], v[100:103], v[220:223]\n"
"ds_read_b64_tr_b16 v[164:165], v7 offset:24576\n"
"v_mfma_f32_16x16x32_bf16 v[224:227], v[144:147], v[104:107], v[224:227]\n"
"ds_read_b64_tr_b16 v[166:167], v7 offset:25600\n"
"v_mfma_f32_16x16x32_bf16 v[228:231], v[144:147], v[108:111], v[228:231]\n"
"v_mfma_f32_16x16x32_bf16 v[232:235], v[144:147], v[112:115], v[232:235]\n"
"v_mfma_f32_16x16x32_bf16 v[252:255], v[148:151], v[112:115], v[252:255]\n"
"v_mfma_f32_16x16x32_bf16 v[248:251], v[148:151], v[108:111], v[248:251]\n"
"v_mfma_f32_16x16x32_bf16 v[244:247], v[148:151], v[104:107], v[244:247]\n"
"v_mfma_f32_16x16x32_bf16 v[240:243], v[148:151], v[100:103], v[240:243]\n"
"v_mfma_f32_16x16x32_bf16 v[236:239], v[148:151], v[96:99], v[236:239]\n"
"s_waitcnt lgkmcnt(6)\n"
"v_mfma_f32_16x16x32_bf16 v[176:179], v[152:155], v[116:119], v[176:179]\n"
"v_mfma_f32_16x16x32_bf16 v[180:183], v[152:155], v[120:123], v[180:183]\n"
"v_mfma_f32_16x16x32_bf16 v[184:187], v[152:155], v[124:127], v[184:187]\n"
"s_waitcnt lgkmcnt(0)\n"
"s_barrier\n"
"v_mfma_f32_16x16x32_bf16 v[188:191], v[152:155], v[128:131], v[188:191]\n"
"ds_read_b128 v[96:99], v2 offset:40960\n"
"v_mfma_f32_16x16x32_bf16 v[192:195], v[152:155], v[132:135], v[192:195]\n"
"ds_read_b64_tr_b16 v[136:137], v4 offset:32768\n"
"v_mfma_f32_16x16x32_bf16 v[212:215], v[156:159], v[132:135], v[212:215]\n"
"ds_read_b64_tr_b16 v[138:139], v4 offset:33792\n"
"v_mfma_f32_16x16x32_bf16 v[208:211], v[156:159], v[128:131], v[208:211]\n"
"ds_read_b128 v[100:103], v2 offset:43008\n"
"v_mfma_f32_16x16x32_bf16 v[204:207], v[156:159], v[124:127], v[204:207]\n"
"ds_read_b128 v[104:107], v2 offset:45056\n"
"v_mfma_f32_16x16x32_bf16 v[200:203], v[156:159], v[120:123], v[200:203]\n"
"ds_read_b128 v[108:111], v2 offset:47104\n"
"v_mfma_f32_16x16x32_bf16 v[196:199], v[156:159], v[116:119], v[196:199]\n"
"ds_read_b128 v[112:115], v2 offset:49152\n"
"v_mfma_f32_16x16x32_bf16 v[216:219], v[160:163], v[116:119], v[216:219]\n"
"ds_read_b64_tr_b16 v[140:141], v5 offset:32768\n"
"v_mfma_f32_16x16x32_bf16 v[220:223], v[160:163], v[120:123], v[220:223]\n"
"ds_read_b64_tr_b16 v[142:143], v5 offset:33792\n"
"v_mfma_f32_16x16x32_bf16 v[224:227], v[160:163], v[124:127], v[224:227]\n"
"ds_read_b64_tr_b16 v[144:145], v6 offset:32768\n"
"v_mfma_f32_16x16x32_bf16 v[228:231], v[160:163], v[128:131], v[228:231]\n"
"ds_read_b64_tr_b16 v[146:147], v6 offset:33792\n"
"v_mfma_f32_16x16x32_bf16 v[232:235], v[160:163], v[132:135], v[232:235]\n"
"ds_read_b64_tr_b16 v[148:149], v7 offset:32768\n"
"v_mfma_f32_16x16x32_bf16 v[252:255], v[164:167], v[132:135], v[252:255]\n"
"ds_read_b64_tr_b16 v[150:151], v7 offset:33792\n"
"v_mfma_f32_16x16x32_bf16 v[248:251], v[164:167], v[128:131], v[248:251]\n"
"v_mfma_f32_16x16x32_bf16 v[244:247], v[164:167], v[124:127], v[244:247]\n"
"v_mfma_f32_16x16x32_bf16 v[240:243], v[164:167], v[120:123], v[240:243]\n"
"v_mfma_f32_16x16x32_bf16 v[236:239], v[164:167], v[116:119], v[236:239]\n"
"s_waitcnt lgkmcnt(6)\n"
"v_mfma_f32_16x16x32_bf16 v[176:179], v[136:139], v[96:99], v[176:179]\n"
"ds_read_b128 v[116:119], v3 offset:40960\n"
"v_mfma_f32_16x16x32_bf16 v[180:183], v[136:139], v[100:103], v[180:183]\n"
"ds_read_b64_tr_b16 v[152:153], v4 offset:40960\n"
"v_mfma_f32_16x16x32_bf16 v[184:187], v[136:139], v[104:107], v[184:187]\n"
"ds_read_b64_tr_b16 v[154:155], v4 offset:41984\n"
"v_mfma_f32_16x16x32_bf16 v[188:191], v[136:139], v[108:111], v[188:191]\n"
"ds_read_b128 v[120:123], v3 offset:43008\n"
"v_mfma_f32_16x16x32_bf16 v[192:195], v[136:139], v[112:115], v[192:195]\n"
"ds_read_b128 v[124:127], v3 offset:45056\n"
"s_waitcnt lgkmcnt(5)\n"
"v_mfma_f32_16x16x32_bf16 v[212:215], v[140:143], v[112:115], v[212:215]\n"
"ds_read_b128 v[128:131], v3 offset:47104\n"
"v_mfma_f32_16x16x32_bf16 v[208:211], v[140:143], v[108:111], v[208:211]\n"
"ds_read_b128 v[132:135], v3 offset:49152\n"
"v_mfma_f32_16x16x32_bf16 v[204:207], v[140:143], v[104:107], v[204:207]\n"
"ds_read_b64_tr_b16 v[156:157], v5 offset:40960\n"
"v_mfma_f32_16x16x32_bf16 v[200:203], v[140:143], v[100:103], v[200:203]\n"
"ds_read_b64_tr_b16 v[158:159], v5 offset:41984\n"
"v_mfma_f32_16x16x32_bf16 v[196:199], v[140:143], v[96:99], v[196:199]\n"
"ds_read_b64_tr_b16 v[160:161], v6 offset:40960\n"
"v_mfma_f32_16x16x32_bf16 v[216:219], v[144:147], v[96:99], v[216:219]\n"
"ds_read_b64_tr_b16 v[162:163], v6 offset:41984\n"
"v_mfma_f32_16x16x32_bf16 v[220:223], v[144:147], v[100:103], v[220:223]\n"
"ds_read_b64_tr_b16 v[164:165], v7 offset:40960\n"
"v_mfma_f32_16x16x32_bf16 v[224:227], v[144:147], v[104:107], v[224:227]\n"
"ds_read_b64_tr_b16 v[166:167], v7 offset:41984\n"
"v_mfma_f32_16x16x32_bf16 v[228:231], v[144:147], v[108:111], v[228:231]\n"
"v_mfma_f32_16x16x32_bf16 v[232:235], v[144:147], v[112:115], v[232:235]\n"
"v_mfma_f32_16x16x32_bf16 v[252:255], v[148:151], v[112:115], v[252:255]\n"
"v_mfma_f32_16x16x32_bf16 v[248:251], v[148:151], v[108:111], v[248:251]\n"
"v_mfma_f32_16x16x32_bf16 v[244:247], v[148:151], v[104:107], v[244:247]\n"
"v_mfma_f32_16x16x32_bf16 v[240:243], v[148:151], v[100:103], v[240:243]\n"
"v_mfma_f32_16x16x32_bf16 v[236:239], v[148:151], v[96:99], v[236:239]\n"
"s_waitcnt lgkmcnt(6)\n"
"v_mfma_f32_16x16x32_bf16 v[176:179], v[152:155], v[116:119], v[176:179]\n"
"v_mfma_f32_16x16x32_bf16 v[180:183], v[152:155], v[120:123], v[180:183]\n"
"v_mfma_f32_16x16x32_bf16 v[184:187], v[152:155], v[124:127], v[184:187]\n"
"s_waitcnt lgkmcnt(0)\n"
"s_barrier\n"
"v_mfma_f32_16x16x32_bf16 v[188:191], v[152:155], v[128:131], v[188:191]\n"
"ds_read_b128 v[96:99], v2 offset:0\n"
"v_mfma_f32_16x16x32_bf16 v[192:195], v[152:155], v[132:135], v[192:195]\n"
"ds_read_b64_tr_b16 v[136:137], v4 offset:0\n"
"v_mfma_f32_16x16x32_bf16 v[212:215], v[156:159], v[132:135], v[212:215]\n"
"ds_read_b64_tr_b16 v[138:139], v4 offset:1024\n"
"v_mfma_f32_16x16x32_bf16 v[208:211], v[156:159], v[128:131], v[208:211]\n"
"ds_read_b128 v[100:103], v2 offset:2048\n"
"v_mfma_f32_16x16x32_bf16 v[204:207], v[156:159], v[124:127], v[204:207]\n"
"ds_read_b128 v[104:107], v2 offset:4096\n"
"v_mfma_f32_16x16x32_bf16 v[200:203], v[156:159], v[120:123], v[200:203]\n"
"ds_read_b128 v[108:111], v2 offset:6144\n"
"v_mfma_f32_16x16x32_bf16 v[196:199], v[156:159], v[116:119], v[196:199]\n"
"ds_read_b128 v[112:115], v2 offset:8192\n"
"v_mfma_f32_16x16x32_bf16 v[216:219], v[160:163], v[116:119], v[216:219]\n"
"ds_read_b64_tr_b16 v[140:141], v5 offset:0\n"
"v_mfma_f32_16x16x32_bf16 v[220:223], v[160:163], v[120:123], v[220:223]\n"
"ds_read_b64_tr_b16 v[142:143], v5 offset:1024\n"
"v_mfma_f32_16x16x32_bf16 v[224:227], v[160:163], v[124:127], v[224:227]\n"
"ds_read_b64_tr_b16 v[144:145], v6 offset:0\n"
"v_mfma_f32_16x16x32_bf16 v[228:231], v[160:163], v[128:131], v[228:231]\n"
"ds_read_b64_tr_b16 v[146:147], v6 offset:1024\n"
"v_mfma_f32_16x16x32_bf16 v[232:235], v[160:163], v[132:135], v[232:235]\n"
"ds_read_b64_tr_b16 v[148:149], v7 offset:0\n"
"v_mfma_f32_16x16x32_bf16 v[252:255], v[164:167], v[132:135], v[252:255]\n"
"ds_read_b64_tr_b16 v[150:151], v7 offset:1024\n"
"v_mfma_f32_16x16x32_bf16 v[248:251], v[164:167], v[128:131], v[248:251]\n"
"v_mfma_f32_16x16x32_bf16 v[244:247], v[164:167], v[124:127], v[244:247]\n"
"v_mfma_f32_16x16x32_bf16 v[240:243], v[164:167], v[120:123], v[240:243]\n"
"v_mfma_f32_16x16x32_bf16 v[236:239], v[164:167], v[116:119], v[236:239]\n"
"s_waitcnt lgkmcnt(6)\n"
"v_mfma_f32_16x16x32_bf16 v[176:179], v[136:139], v[96:99], v[176:179]\n"
"ds_read_b128 v[116:119], v3 offset:0\n"
"v_mfma_f32_16x16x32_bf16 v[180:183], v[136:139], v[100:103], v[180:183]\n"
"ds_read_b64_tr_b16 v[152:153], v4 offset:8192\n"
"v_mfma_f32_16x16x32_bf16 v[184:187], v[136:139], v[104:107], v[184:187]\n"
"ds_read_b64_tr_b16 v[154:155], v4 offset:9216\n"
"v_mfma_f32_16x16x32_bf16 v[188:191], v[136:139], v[108:111], v[188:191]\n"
"ds_read_b128 v[120:123], v3 offset:2048\n"
"v_mfma_f32_16x16x32_bf16 v[192:195], v[136:139], v[112:115], v[192:195]\n"
"ds_read_b128 v[124:127], v3 offset:4096\n"
"s_waitcnt lgkmcnt(5)\n"
"v_mfma_f32_16x16x32_bf16 v[212:215], v[140:143], v[112:115], v[212:215]\n"
"ds_read_b128 v[128:131], v3 offset:6144\n"
"v_mfma_f32_16x16x32_bf16 v[208:211], v[140:143], v[108:111], v[208:211]\n"
"ds_read_b128 v[132:135], v3 offset:8192\n"
"v_mfma_f32_16x16x32_bf16 v[204:207], v[140:143], v[104:107], v[204:207]\n"
"ds_read_b64_tr_b16 v[156:157], v5 offset:8192\n"
"v_mfma_f32_16x16x32_bf16 v[200:203], v[140:143], v[100:103], v[200:203]\n"
"ds_read_b64_tr_b16 v[158:159], v5 offset:9216\n"
"v_mfma_f32_16x16x32_bf16 v[196:199], v[140:143], v[96:99], v[196:199]\n"
"ds_read_b64_tr_b16 v[160:161], v6 offset:8192\n"
"v_mfma_f32_16x16x32_bf16 v[216:219], v[144:147], v[96:99], v[216:219]\n"
"ds_read_b64_tr_b16 v[162:163], v6 offset:9216\n"
"v_mfma_f32_16x16x32_bf16 v[220:223], v[144:147], v[100:103], v[220:223]\n"
"ds_read_b64_tr_b16 v[164:165], v7 offset:8192\n"
"v_mfma_f32_16x16x32_bf16 v[224:227], v[144:147], v[104:107], v[224:227]\n"
"ds_read_b64_tr_b16 v[166:167], v7 offset:9216\n"
"v_mfma_f32_16x16x32_bf16 v[228:231], v[144:147], v[108:111], v[228:231]\n"
"v_mfma_f32_16x16x32_bf16 v[232:235], v[144:147], v[112:115], v[232:235]\n"
"v_mfma_f32_16x16x32_bf16 v[252:255], v[148:151], v[112:115], v[252:255]\n"
"v_mfma_f32_16x16x32_bf16 v[248:251], v[148:151], v[108:111], v[248:251]\n"
"v_mfma_f32_16x16x32_bf16 v[244:247], v[148:151], v[104:107], v[244:247]\n"
"v_mfma_f32_16x16x32_bf16 v[240:243], v[148:151], v[100:103], v[240:243]\n"
"v_mfma_f32_16x16x32_bf16 v[236:239], v[148:151], v[96:99], v[236:239]\n"
"s_waitcnt lgkmcnt(6)\n"
"v_mfma_f32_16x16x32_bf16 v[176:179], v[152:155], v[116:119], v[176:179]\n"
"v_mfma_f32_16x16x32_bf16 v[180:183], v[152:155], v[120:123], v[180:183]\n"
"v_mfma_f32_16x16x32_bf16 v[184:187], v[152:155], v[124:127], v[184:187]\n"
"s_waitcnt lgkmcnt(0)\n"
"s_barrier\n"
"v_mfma_f32_16x16x32_bf16 v[188:191], v[152:155], v[128:131], v[188:191]\n"
"ds_read_b128 v[96:99], v2 offset:20480\n"
"v_mfma_f32_16x16x32_bf16 v[192:195], v[152:155], v[132:135], v[192:195]\n"
"ds_read_b64_tr_b16 v[136:137], v4 offset:16384\n"
"v_mfma_f32_16x16x32_bf16 v[212:215], v[156:159], v[132:135], v[212:215]\n"
"ds_read_b64_tr_b16 v[138:139], v4 offset:17408\n"
"v_mfma_f32_16x16x32_bf16 v[208:211], v[156:159], v[128:131], v[208:211]\n"
"ds_read_b128 v[100:103], v2 offset:22528\n"
"v_mfma_f32_16x16x32_bf16 v[204:207], v[156:159], v[124:127], v[204:207]\n"
"ds_read_b128 v[104:107], v2 offset:24576\n"
"v_mfma_f32_16x16x32_bf16 v[200:203], v[156:159], v[120:123], v[200:203]\n"
"ds_read_b128 v[108:111], v2 offset:26624\n"
"v_mfma_f32_16x16x32_bf16 v[196:199], v[156:159], v[116:119], v[196:199]\n"
"ds_read_b128 v[112:115], v2 offset:28672\n"
"v_mfma_f32_16x16x32_bf16 v[216:219], v[160:163], v[116:119], v[216:219]\n"
"ds_read_b64_tr_b16 v[140:141], v5 offset:16384\n"
"v_mfma_f32_16x16x32_bf16 v[220:223], v[160:163], v[120:123], v[220:223]\n"
"ds_read_b64_tr_b16 v[142:143], v5 offset:17408\n"
"v_mfma_f32_16x16x32_bf16 v[224:227], v[160:163], v[124:127], v[224:227]\n"
"ds_read_b64_tr_b16 v[144:145], v6 offset:16384\n"
"v_mfma_f32_16x16x32_bf16 v[228:231], v[160:163], v[128:131], v[228:231]\n"
"ds_read_b64_tr_b16 v[146:147], v6 offset:17408\n"
"v_mfma_f32_16x16x32_bf16 v[232:235], v[160:163], v[132:135], v[232:235]\n"
"ds_read_b64_tr_b16 v[148:149], v7 offset:16384\n"
"v_mfma_f32_16x16x32_bf16 v[252:255], v[164:167], v[132:135], v[252:255]\n"
"ds_read_b64_tr_b16 v[150:151], v7 offset:17408\n"
"v_mfma_f32_16x16x32_bf16 v[248:251], v[164:167], v[128:131], v[248:251]\n"
"v_mfma_f32_16x16x32_bf16 v[244:247], v[164:167], v[124:127], v[244:247]\n"
"v_mfma_f32_16x16x32_bf16 v[240:243], v[164:167], v[120:123], v[240:243]\n"
"v_mfma_f32_16x16x32_bf16 v[236:239], v[164:167], v[116:119], v[236:239]\n"
"s_waitcnt lgkmcnt(6)\n"
"v_mfma_f32_16x16x32_bf16 v[176:179], v[136:139], v[96:99], v[176:179]\n"
"ds_read_b128 v[116:119], v3 offset:20480\n"
"v_mfma_f32_16x16x32_bf16 v[180:183], v[136:139], v[100:103], v[180:183]\n"
"ds_read_b64_tr_b16 v[152:153], v4 offset:24576\n"
"v_mfma_f32_16x16x32_bf16 v[184:187], v[136:139], v[104:107], v[184:187]\n"
"ds_read_b64_tr_b16 v[154:155], v4 offset:25600\n"
"v_mfma_f32_16x16x32_bf16 v[188:191], v[136:139], v[108:111], v[188:191]\n"
"ds_read_b128 v[120:123], v3 offset:22528\n"
"v_mfma_f32_16x16x32_bf16 v[192:195], v[136:139], v[112:115], v[192:195]\n"
"ds_read_b128 v[124:127], v3 offset:24576\n"
"s_waitcnt lgkmcnt(5)\n"
"v_mfma_f32_16x16x32_bf16 v[212:215], v[140:143], v[112:115], v[212:215]\n"
"ds_read_b128 v[128:131], v3 offset:26624\n"
"v_mfma_f32_16x16x32_bf16 v[208:211], v[140:143], v[108:111], v[208:211]\n"
"ds_read_b128 v[132:135], v3 offset:28672\n"
"v_mfma_f32_16x16x32_bf16 v[204:207], v[140:143], v[104:107], v[204:207]\n"
"ds_read_b64_tr_b16 v[156:157], v5 offset:24576\n"
"v_mfma_f32_16x16x32_bf16 v[200:203], v[140:143], v[100:103], v[200:203]\n"
"ds_read_b64_tr_b16 v[158:159], v5 offset:25600\n"
"v_mfma_f32_16x16x32_bf16 v[196:199], v[140:143], v[96:99], v[196:199]\n"
"ds_read_b64_tr_b16 v[160:161], v6 offset:24576\n"
"v_mfma_f32_16x16x32_bf16 v[216:219], v[144:147], v[96:99], v[216:219]\n"
"ds_read_b64_tr_b16 v[162:163], v6 offset:25600\n"
"v_mfma_f32_16x16x32_bf16 v[220:223], v[144:147], v[100:103], v[220:223]\n"
"ds_read_b64_tr_b16 v[164:165], v7 offset:24576\n"
"v_mfma_f32_16x16x32_bf16 v[224:227], v[144:147], v[104:107], v[224:227]\n"
"ds_read_b64_tr_b16 v[166:167], v7 offset:25600\n"
"v_mfma_f32_16x16x32_bf16 v[228:231], v[144:147], v[108:111], v[228:231]\n"
"v_mfma_f32_16x16x32_bf16 v[232:235], v[144:147], v[112:115], v[232:235]\n"
"v_mfma_f32_16x16x32_bf16 v[252:255], v[148:151], v[112:115], v[252:255]\n"
"v_mfma_f32_16x16x32_bf16 v[248:251], v[148:151], v[108:111], v[248:251]\n"
"v_mfma_f32_16x16x32_bf16 v[244:247], v[148:151], v[104:107], v[244:247]\n"
"v_mfma_f32_16x16x32_bf16 v[240:243], v[148:151], v[100:103], v[240:243]\n"
"v_mfma_f32_16x16x32_bf16 v[236:239], v[148:151], v[96:99], v[236:239]\n"
"s_waitcnt lgkmcnt(6)\n"
"v_mfma_f32_16x16x32_bf16 v[176:179], v[152:155], v[116:119], v[176:179]\n"
"v_mfma_f32_16x16x32_bf16 v[180:183], v[152:155], v[120:123], v[180:183]\n"
"v_mfma_f32_16x16x32_bf16 v[184:187], v[152:155], v[124:127], v[184:187]\n"
"s_waitcnt lgkmcnt(0)\n"
"s_barrier\n"
"v_mfma_f32_16x16x32_bf16 v[188:191], v[152:155], v[128:131], v[188:191]\n"
"ds_read_b128 v[96:99], v2 offset:40960\n"
"v_mfma_f32_16x16x32_bf16 v[192:195], v[152:155], v[132:135], v[192:195]\n"
"ds_read_b64_tr_b16 v[136:137], v4 offset:32768\n"
"v_mfma_f32_16x16x32_bf16 v[212:215], v[156:159], v[132:135], v[212:215]\n"
"ds_read_b64_tr_b16 v[138:139], v4 offset:33792\n"
"v_mfma_f32_16x16x32_bf16 v[208:211], v[156:159], v[128:131], v[208:211]\n"
"ds_read_b128 v[100:103], v2 offset:43008\n"
"v_mfma_f32_16x16x32_bf16 v[204:207], v[156:159], v[124:127], v[204:207]\n"
"ds_read_b128 v[104:107], v2 offset:45056\n"
"v_mfma_f32_16x16x32_bf16 v[200:203], v[156:159], v[120:123], v[200:203]\n"
"ds_read_b128 v[108:111], v2 offset:47104\n"
"v_mfma_f32_16x16x32_bf16 v[196:199], v[156:159], v[116:119], v[196:199]\n"
"ds_read_b128 v[112:115], v2 offset:49152\n"
"v_mfma_f32_16x16x32_bf16 v[216:219], v[160:163], v[116:119], v[216:219]\n"
"ds_read_b64_tr_b16 v[140:141], v5 offset:32768\n"
"v_mfma_f32_16x16x32_bf16 v[220:223], v[160:163], v[120:123], v[220:223]\n"
"ds_read_b64_tr_b16 v[142:143], v5 offset:33792\n"
"v_mfma_f32_16x16x32_bf16 v[224:227], v[160:163], v[124:127], v[224:227]\n"
"ds_read_b64_tr_b16 v[144:145], v6 offset:32768\n"
"v_mfma_f32_16x16x32_bf16 v[228:231], v[160:163], v[128:131], v[228:231]\n"
"ds_read_b64_tr_b16 v[146:147], v6 offset:33792\n"
"v_mfma_f32_16x16x32_bf16 v[232:235], v[160:163], v[132:135], v[232:235]\n"
"ds_read_b64_tr_b16 v[148:149], v7 offset:32768\n"
"v_mfma_f32_16x16x32_bf16 v[252:255], v[164:167], v[132:135], v[252:255]\n"
"ds_read_b64_tr_b16 v[150:151], v7 offset:33792\n"
"v_mfma_f32_16x16x32_bf16 v[248:251], v[164:167], v[128:131], v[248:251]\n"
"v_mfma_f32_16x16x32_bf16 v[244:247], v[164:167], v[124:127], v[244:247]\n"
"v_mfma_f32_16x16x32_bf16 v[240:243], v[164:167], v[120:123], v[240:243]\n"
"v_mfma_f32_16x16x32_bf16 v[236:239], v[164:167], v[116:119], v[236:239]\n"
"s_waitcnt lgkmcnt(6)\n"
"v_mfma_f32_16x16x32_bf16 v[176:179], v[136:139], v[96:99], v[176:179]\n"
"ds_read_b128 v[116:119], v3 offset:40960\n"
"v_mfma_f32_16x16x32_bf16 v[180:183], v[136:139], v[100:103], v[180:183]\n"
"ds_read_b64_tr_b16 v[152:153], v4 offset:40960\n"
"v_mfma_f32_16x16x32_bf16 v[184:187], v[136:139], v[104:107], v[184:187]\n"
"ds_read_b64_tr_b16 v[154:155], v4 offset:41984\n"
"v_mfma_f32_16x16x32_bf16 v[188:191], v[136:139], v[108:111], v[188:191]\n"
"ds_read_b128 v[120:123], v3 offset:43008\n"
"v_mfma_f32_16x16x32_bf16 v[192:195], v[136:139], v[112:115], v[192:195]\n"
"ds_read_b128 v[124:127], v3 offset:45056\n"
"s_waitcnt lgkmcnt(5)\n"
"v_mfma_f32_16x16x32_bf16 v[212:215], v[140:143], v[112:115], v[212:215]\n"
"ds_read_b128 v[128:131], v3 offset:47104\n"
"v_mfma_f32_16x16x32_bf16 v[208:211], v[140:143], v[108:111], v[208:211]\n"
"ds_read_b128 v[132:135], v3 offset:49152\n"
"v_mfma_f32_16x16x32_bf16 v[204:207], v[140:143], v[104:107], v[204:207]\n"
"ds_read_b64_tr_b16 v[156:157], v5 offset:40960\n"
"v_mfma_f32_16x16x32_bf16 v[200:203], v[140:143], v[100:103], v[200:203]\n"
"ds_read_b64_tr_b16 v[158:159], v5 offset:41984\n"
"v_mfma_f32_16x16x32_bf16 v[196:199], v[140:143], v[96:99], v[196:199]\n"
"ds_read_b64_tr_b16 v[160:161], v6 offset:40960\n"
"v_mfma_f32_16x16x32_bf16 v[216:219], v[144:147], v[96:99], v[216:219]\n"
"ds_read_b64_tr_b16 v[162:163], v6 offset:41984\n"
"v_mfma_f32_16x16x32_bf16 v[220:223], v[144:147], v[100:103], v[220:223]\n"
"ds_read_b64_tr_b16 v[164:165], v7 offset:40960\n"
"v_mfma_f32_16x16x32_bf16 v[224:227], v[144:147], v[104:107], v[224:227]\n"
"ds_read_b64_tr_b16 v[166:167], v7 offset:41984\n"
"v_mfma_f32_16x16x32_bf16 v[228:231], v[144:147], v[108:111], v[228:231]\n"
"v_mfma_f32_16x16x32_bf16 v[232:235], v[144:147], v[112:115], v[232:235]\n"
"v_mfma_f32_16x16x32_bf16 v[252:255], v[148:151], v[112:115], v[252:255]\n"
"v_mfma_f32_16x16x32_bf16 v[248:251], v[148:151], v[108:111], v[248:251]\n"
"v_mfma_f32_16x16x32_bf16 v[244:247], v[148:151], v[104:107], v[244:247]\n"
"v_mfma_f32_16x16x32_bf16 v[240:243], v[148:151], v[100:103], v[240:243]\n"
"v_mfma_f32_16x16x32_bf16 v[236:239], v[148:151], v[96:99], v[236:239]\n"
"s_waitcnt lgkmcnt(6)\n"
"v_mfma_f32_16x16x32_bf16 v[176:179], v[152:155], v[116:119], v[176:179]\n"
"v_mfma_f32_16x16x32_bf16 v[180:183], v[152:155], v[120:123], v[180:183]\n"
"v_mfma_f32_16x16x32_bf16 v[184:187], v[152:155], v[124:127], v[184:187]\n"
"s_waitcnt lgkmcnt(0)\n"
"s_barrier\n"
"v_mfma_f32_16x16x32_bf16 v[188:191], v[152:155], v[128:131], v[188:191]\n"
"ds_read_b128 v[96:99], v2 offset:0\n"
"v_mfma_f32_16x16x32_bf16 v[192:195], v[152:155], v[132:135], v[192:195]\n"
"ds_read_b64_tr_b16 v[136:137], v4 offset:0\n"
"v_mfma_f32_16x16x32_bf16 v[212:215], v[156:159], v[132:135], v[212:215]\n"
"ds_read_b64_tr_b16 v[138:139], v4 offset:1024\n"
"v_mfma_f32_16x16x32_bf16 v[208:211], v[156:159], v[128:131], v[208:211]\n"
"ds_read_b128 v[100:103], v2 offset:2048\n"
"v_mfma_f32_16x16x32_bf16 v[204:207], v[156:159], v[124:127], v[204:207]\n"
"ds_read_b128 v[104:107], v2 offset:4096\n"
"v_mfma_f32_16x16x32_bf16 v[200:203], v[156:159], v[120:123], v[200:203]\n"
"ds_read_b128 v[108:111], v2 offset:6144\n"
"v_mfma_f32_16x16x32_bf16 v[196:199], v[156:159], v[116:119], v[196:199]\n"
"ds_read_b128 v[112:115], v2 offset:8192\n"
"v_mfma_f32_16x16x32_bf16 v[216:219], v[160:163], v[116:119], v[216:219]\n"
"ds_read_b64_tr_b16 v[140:141], v5 offset:0\n"
"v_mfma_f32_16x16x32_bf16 v[220:223], v[160:163], v[120:123], v[220:223]\n"
"ds_read_b64_tr_b16 v[142:143], v5 offset:1024\n"
"v_mfma_f32_16x16x32_bf16 v[224:227], v[160:163], v[124:127], v[224:227]\n"
"ds_read_b64_tr_b16 v[144:145], v6 offset:0\n"
"v_mfma_f32_16x16x32_bf16 v[228:231], v[160:163], v[128:131], v[228:231]\n"
"ds_read_b64_tr_b16 v[146:147], v6 offset:1024\n"
"v_mfma_f32_16x16x32_bf16 v[232:235], v[160:163], v[132:135], v[232:235]\n"
"ds_read_b64_tr_b16 v[148:149], v7 offset:0\n"
"v_mfma_f32_16x16x32_bf16 v[252:255], v[164:167], v[132:135], v[252:255]\n"
"ds_read_b64_tr_b16 v[150:151], v7 offset:1024\n"
"v_mfma_f32_16x16x32_bf16 v[248:251], v[164:167], v[128:131], v[248:251]\n"
"v_mfma_f32_16x16x32_bf16 v[244:247], v[164:167], v[124:127], v[244:247]\n"
"v_mfma_f32_16x16x32_bf16 v[240:243], v[164:167], v[120:123], v[240:243]\n"
"v_mfma_f32_16x16x32_bf16 v[236:239], v[164:167], v[116:119], v[236:239]\n"
"s_waitcnt lgkmcnt(6)\n"
"v_mfma_f32_16x16x32_bf16 v[176:179], v[136:139], v[96:99], v[176:179]\n"
"ds_read_b128 v[116:119], v3 offset:0\n"
"v_mfma_f32_16x16x32_bf16 v[180:183], v[136:139], v[100:103], v[180:183]\n"
"ds_read_b64_tr_b16 v[152:153], v4 offset:8192\n"
"v_mfma_f32_16x16x32_bf16 v[184:187], v[136:139], v[104:107], v[184:187]\n"
"ds_read_b64_tr_b16 v[154:155], v4 offset:9216\n"
"v_mfma_f32_16x16x32_bf16 v[188:191], v[136:139], v[108:111], v[188:191]\n"
"ds_read_b128 v[120:123], v3 offset:2048\n"
"v_mfma_f32_16x16x32_bf16 v[192:195], v[136:139], v[112:115], v[192:195]\n"
"ds_read_b128 v[124:127], v3 offset:4096\n"
"s_waitcnt lgkmcnt(5)\n"
"v_mfma_f32_16x16x32_bf16 v[212:215], v[140:143], v[112:115], v[212:215]\n"
"ds_read_b128 v[128:131], v3 offset:6144\n"
"v_mfma_f32_16x16x32_bf16 v[208:211], v[140:143], v[108:111], v[208:211]\n"
"ds_read_b128 v[132:135], v3 offset:8192\n"
"v_mfma_f32_16x16x32_bf16 v[204:207], v[140:143], v[104:107], v[204:207]\n"
"ds_read_b64_tr_b16 v[156:157], v5 offset:8192\n"
"v_mfma_f32_16x16x32_bf16 v[200:203], v[140:143], v[100:103], v[200:203]\n"
"ds_read_b64_tr_b16 v[158:159], v5 offset:9216\n"
"v_mfma_f32_16x16x32_bf16 v[196:199], v[140:143], v[96:99], v[196:199]\n"
"ds_read_b64_tr_b16 v[160:161], v6 offset:8192\n"
"v_mfma_f32_16x16x32_bf16 v[216:219], v[144:147], v[96:99], v[216:219]\n"
"ds_read_b64_tr_b16 v[162:163], v6 offset:9216\n"
"v_mfma_f32_16x16x32_bf16 v[220:223], v[144:147], v[100:103], v[220:223]\n"
"ds_read_b64_tr_b16 v[164:165], v7 offset:8192\n"
"v_mfma_f32_16x16x32_bf16 v[224:227], v[144:147], v[104:107], v[224:227]\n"
"ds_read_b64_tr_b16 v[166:167], v7 offset:9216\n"
"v_mfma_f32_16x16x32_bf16 v[228:231], v[144:147], v[108:111], v[228:231]\n"
"v_mfma_f32_16x16x32_bf16 v[232:235], v[144:147], v[112:115], v[232:235]\n"
"v_mfma_f32_16x16x32_bf16 v[252:255], v[148:151], v[112:115], v[252:255]\n"
"v_mfma_f32_16x16x32_bf16 v[248:251], v[148:151], v[108:111], v[248:251]\n"
"v_mfma_f32_16x16x32_bf16 v[244:247], v[148:151], v[104:107], v[244:247]\n"
"v_mfma_f32_16x16x32_bf16 v[240:243], v[148:151], v[100:103], v[240:243]\n"
"v_mfma_f32_16x16x32_bf16 v[236:239], v[148:151], v[96:99], v[236:239]\n"
"s_waitcnt lgkmcnt(6)\n"
"v_mfma_f32_16x16x32_bf16 v[176:179], v[152:155], v[116:119], v[176:179]\n"
"v_mfma_f32_16x16x32_bf16 v[180:183], v[152:155], v[120:123], v[180:183]\n"
"v_mfma_f32_16x16x32_bf16 v[184:187], v[152:155], v[124:127], v[184:187]\n"
"s_waitcnt lgkmcnt(0)\n"
"s_barrier\n"
"v_mfma_f32_16x16x32_bf16 v[188:191], v[152:155], v[128:131], v[188:191]\n"
"ds_read_b128 v[96:99], v2 offset:20480\n"
"v_mfma_f32_16x16x32_bf16 v[192:195], v[152:155], v[132:135], v[192:195]\n"
"ds_read_b64_tr_b16 v[136:137], v4 offset:16384\n"
"v_mfma_f32_16x16x32_bf16 v[212:215], v[156:159], v[132:135], v[212:215]\n"
"ds_read_b64_tr_b16 v[138:139], v4 offset:17408\n"
"v_mfma_f32_16x16x32_bf16 v[208:211], v[156:159], v[128:131], v[208:211]\n"
"ds_read_b128 v[100:103], v2 offset:22528\n"
"v_mfma_f32_16x16x32_bf16 v[204:207], v[156:159], v[124:127], v[204:207]\n"
"ds_read_b128 v[104:107], v2 offset:24576\n"
"v_mfma_f32_16x16x32_bf16 v[200:203], v[156:159], v[120:123], v[200:203]\n"
"ds_read_b128 v[108:111], v2 offset:26624\n"
"v_mfma_f32_16x16x32_bf16 v[196:199], v[156:159], v[116:119], v[196:199]\n"
"ds_read_b128 v[112:115], v2 offset:28672\n"
"v_mfma_f32_16x16x32_bf16 v[216:219], v[160:163], v[116:119], v[216:219]\n"
"ds_read_b64_tr_b16 v[140:141], v5 offset:16384\n"
"v_mfma_f32_16x16x32_bf16 v[220:223], v[160:163], v[120:123], v[220:223]\n"
"ds_read_b64_tr_b16 v[142:143], v5 offset:17408\n"
"v_mfma_f32_16x16x32_bf16 v[224:227], v[160:163], v[124:127], v[224:227]\n"
"ds_read_b64_tr_b16 v[144:145], v6 offset:16384\n"
"v_mfma_f32_16x16x32_bf16 v[228:231], v[160:163], v[128:131], v[228:231]\n"
"ds_read_b64_tr_b16 v[146:147], v6 offset:17408\n"
"v_mfma_f32_16x16x32_bf16 v[232:235], v[160:163], v[132:135], v[232:235]\n"
"ds_read_b64_tr_b16 v[148:149], v7 offset:16384\n"
"v_mfma_f32_16x16x32_bf16 v[252:255], v[164:167], v[132:135], v[252:255]\n"
"ds_read_b64_tr_b16 v[150:151], v7 offset:17408\n"
"v_mfma_f32_16x16x32_bf16 v[248:251], v[164:167], v[128:131], v[248:251]\n"
"v_mfma_f32_16x16x32_bf16 v[244:247], v[164:167], v[124:127], v[244:247]\n"
"v_mfma_f32_16x16x32_bf16 v[240:243], v[164:167], v[120:123], v[240:243]\n"
"v_mfma_f32_16x16x32_bf16 v[236:239], v[164:167], v[116:119], v[236:239]\n"
"s_waitcnt lgkmcnt(6)\n"
"v_mfma_f32_16x16x32_bf16 v[176:179], v[136:139], v[96:99], v[176:179]\n"
"ds_read_b128 v[116:119], v3 offset:20480\n"
"v_mfma_f32_16x16x32_bf16 v[180:183], v[136:139], v[100:103], v[180:183]\n"
"ds_read_b64_tr_b16 v[152:153], v4 offset:24576\n"
"v_mfma_f32_16x16x32_bf16 v[184:187], v[136:139], v[104:107], v[184:187]\n"
"ds_read_b64_tr_b16 v[154:155], v4 offset:25600\n"
"v_mfma_f32_16x16x32_bf16 v[188:191], v[136:139], v[108:111], v[188:191]\n"
"ds_read_b128 v[120:123], v3 offset:22528\n"
"v_mfma_f32_16x16x32_bf16 v[192:195], v[136:139], v[112:115], v[192:195]\n"
"ds_read_b128 v[124:127], v3 offset:24576\n"
"s_waitcnt lgkmcnt(5)\n"
"v_mfma_f32_16x16x32_bf16 v[212:215], v[140:143], v[112:115], v[212:215]\n"
"ds_read_b128 v[128:131], v3 offset:26624\n"
"v_mfma_f32_16x16x32_bf16 v[208:211], v[140:143], v[108:111], v[208:211]\n"
"ds_read_b128 v[132:135], v3 offset:28672\n"
"v_mfma_f32_16x16x32_bf16 v[204:207], v[140:143], v[104:107], v[204:207]\n"
"ds_read_b64_tr_b16 v[156:157], v5 offset:24576\n"
"v_mfma_f32_16x16x32_bf16 v[200:203], v[140:143], v[100:103], v[200:203]\n"
"ds_read_b64_tr_b16 v[158:159], v5 offset:25600\n"
"v_mfma_f32_16x16x32_bf16 v[196:199], v[140:143], v[96:99], v[196:199]\n"
"ds_read_b64_tr_b16 v[160:161], v6 offset:24576\n"
"v_mfma_f32_16x16x32_bf16 v[216:219], v[144:147], v[96:99], v[216:219]\n"
"ds_read_b64_tr_b16 v[162:163], v6 offset:25600\n"
"v_mfma_f32_16x16x32_bf16 v[220:223], v[144:147], v[100:103], v[220:223]\n"
"ds_read_b64_tr_b16 v[164:165], v7 offset:24576\n"
"v_mfma_f32_16x16x32_bf16 v[224:227], v[144:147], v[104:107], v[224:227]\n"
"ds_read_b64_tr_b16 v[166:167], v7 offset:25600\n"
"v_mfma_f32_16x16x32_bf16 v[228:231], v[144:147], v[108:111], v[228:231]\n"
"v_mfma_f32_16x16x32_bf16 v[232:235], v[144:147], v[112:115], v[232:235]\n"
"v_mfma_f32_16x16x32_bf16 v[252:255], v[148:151], v[112:115], v[252:255]\n"
"v_mfma_f32_16x16x32_bf16 v[248:251], v[148:151], v[108:111], v[248:251]\n"
"v_mfma_f32_16x16x32_bf16 v[244:247], v[148:151], v[104:107], v[244:247]\n"
"v_mfma_f32_16x16x32_bf16 v[240:243], v[148:151], v[100:103], v[240:243]\n"
"v_mfma_f32_16x16x32_bf16 v[236:239], v[148:151], v[96:99], v[236:239]\n"
"s_waitcnt lgkmcnt(6)\n"
"v_mfma_f32_16x16x32_bf16 v[176:179], v[152:155], v[116:119], v[176:179]\n"
"v_mfma_f32_16x16x32_bf16 v[180:183], v[152:155], v[120:123], v[180:183]\n"
"v_mfma_f32_16x16x32_bf16 v[184:187], v[152:155], v[124:127], v[184:187]\n"
"s_waitcnt lgkmcnt(0)\n"
"s_barrier\n"
"v_mfma_f32_16x16x32_bf16 v[188:191], v[152:155], v[128:131], v[188:191]\n"
"ds_read_b128 v[96:99], v2 offset:40960\n"
"v_mfma_f32_16x16x32_bf16 v[192:195], v[152:155], v[132:135], v[192:195]\n"
"ds_read_b64_tr_b16 v[136:137], v4 offset:32768\n"
"v_mfma_f32_16x16x32_bf16 v[212:215], v[156:159], v[132:135], v[212:215]\n"
"ds_read_b64_tr_b16 v[138:139], v4 offset:33792\n"
"v_mfma_f32_16x16x32_bf16 v[208:211], v[156:159], v[128:131], v[208:211]\n"
"ds_read_b128 v[100:103], v2 offset:43008\n"
"v_mfma_f32_16x16x32_bf16 v[204:207], v[156:159], v[124:127], v[204:207]\n"
"ds_read_b128 v[104:107], v2 offset:45056\n"
"v_mfma_f32_16x16x32_bf16 v[200:203], v[156:159], v[120:123], v[200:203]\n"
"ds_read_b128 v[108:111], v2 offset:47104\n"
"v_mfma_f32_16x16x32_bf16 v[196:199], v[156:159], v[116:119], v[196:199]\n"
"ds_read_b128 v[112:115], v2 offset:49152\n"
"v_mfma_f32_16x16x32_bf16 v[216:219], v[160:163], v[116:119], v[216:219]\n"
"ds_read_b64_tr_b16 v[140:141], v5 offset:32768\n"
"v_mfma_f32_16x16x32_bf16 v[220:223], v[160:163], v[120:123], v[220:223]\n"
"ds_read_b64_tr_b16 v[142:143], v5 offset:33792\n"
"v_mfma_f32_16x16x32_bf16 v[224:227], v[160:163], v[124:127], v[224:227]\n"
"ds_read_b64_tr_b16 v[144:145], v6 offset:32768\n"
"v_mfma_f32_16x16x32_bf16 v[228:231], v[160:163], v[128:131], v[228:231]\n"
"ds_read_b64_tr_b16 v[146:147], v6 offset:33792\n"
"v_mfma_f32_16x16x32_bf16 v[232:235], v[160:163], v[132:135], v[232:235]\n"
"ds_read_b64_tr_b16 v[148:149], v7 offset:32768\n"
"v_mfma_f32_16x16x32_bf16 v[252:255], v[164:167], v[132:135], v[252:255]\n"
"ds_read_b64_tr_b16 v[150:151], v7 offset:33792\n"
"v_mfma_f32_16x16x32_bf16 v[248:251], v[164:167], v[128:131], v[248:251]\n"
"v_mfma_f32_16x16x32_bf16 v[244:247], v[164:167], v[124:127], v[244:247]\n"
"v_mfma_f32_16x16x32_bf16 v[240:243], v[164:167], v[120:123], v[240:243]\n"
"v_mfma_f32_16x16x32_bf16 v[236:239], v[164:167], v[116:119], v[236:239]\n"
"s_waitcnt lgkmcnt(6)\n"
"v_mfma_f32_16x16x32_bf16 v[176:179], v[136:139], v[96:99], v[176:179]\n"
"ds_read_b128 v[116:119], v3 offset:40960\n"
"v_mfma_f32_16x16x32_bf16 v[180:183], v[136:139], v[100:103], v[180:183]\n"
"ds_read_b64_tr_b16 v[152:153], v4 offset:40960\n"
"v_mfma_f32_16x16x32_bf16 v[184:187], v[136:139], v[104:107], v[184:187]\n"
"ds_read_b64_tr_b16 v[154:155], v4 offset:41984\n"
"v_mfma_f32_16x16x32_bf16 v[188:191], v[136:139], v[108:111], v[188:191]\n"
"ds_read_b128 v[120:123], v3 offset:43008\n"
"v_mfma_f32_16x16x32_bf16 v[192:195], v[136:139], v[112:115], v[192:195]\n"
"ds_read_b128 v[124:127], v3 offset:45056\n"
"s_waitcnt lgkmcnt(5)\n"
"v_mfma_f32_16x16x32_bf16 v[212:215], v[140:143], v[112:115], v[212:215]\n"
"ds_read_b128 v[128:131], v3 offset:47104\n"
"v_mfma_f32_16x16x32_bf16 v[208:211], v[140:143], v[108:111], v[208:211]\n"
"ds_read_b128 v[132:135], v3 offset:49152\n"
"v_mfma_f32_16x16x32_bf16 v[204:207], v[140:143], v[104:107], v[204:207]\n"
"ds_read_b64_tr_b16 v[156:157], v5 offset:40960\n"
"v_mfma_f32_16x16x32_bf16 v[200:203], v[140:143], v[100:103], v[200:203]\n"
"ds_read_b64_tr_b16 v[158:159], v5 offset:41984\n"
"v_mfma_f32_16x16x32_bf16 v[196:199], v[140:143], v[96:99], v[196:199]\n"
"ds_read_b64_tr_b16 v[160:161], v6 offset:40960\n"
"v_mfma_f32_16x16x32_bf16 v[216:219], v[144:147], v[96:99], v[216:219]\n"
"ds_read_b64_tr_b16 v[162:163], v6 offset:41984\n"
"v_mfma_f32_16x16x32_bf16 v[220:223], v[144:147], v[100:103], v[220:223]\n"
"ds_read_b64_tr_b16 v[164:165], v7 offset:40960\n"
"v_mfma_f32_16x16x32_bf16 v[224:227], v[144:147], v[104:107], v[224:227]\n"
"ds_read_b64_tr_b16 v[166:167], v7 offset:41984\n"
"v_mfma_f32_16x16x32_bf16 v[228:231], v[144:147], v[108:111], v[228:231]\n"
"v_mfma_f32_16x16x32_bf16 v[232:235], v[144:147], v[112:115], v[232:235]\n"
"v_mfma_f32_16x16x32_bf16 v[252:255], v[148:151], v[112:115], v[252:255]\n"
"v_mfma_f32_16x16x32_bf16 v[248:251], v[148:151], v[108:111], v[248:251]\n"
"v_mfma_f32_16x16x32_bf16 v[244:247], v[148:151], v[104:107], v[244:247]\n"
"v_mfma_f32_16x16x32_bf16 v[240:243], v[148:151], v[100:103], v[240:243]\n"
"v_mfma_f32_16x16x32_bf16 v[236:239], v[148:151], v[96:99], v[236:239]\n"
"s_waitcnt lgkmcnt(6)\n"
"v_mfma_f32_16x16x32_bf16 v[176:179], v[152:155], v[116:119], v[176:179]\n"
"v_mfma_f32_16x16x32_bf16 v[180:183], v[152:155], v[120:123], v[180:183]\n"
"v_mfma_f32_16x16x32_bf16 v[184:187], v[152:155], v[124:127], v[184:187]\n"
"s_waitcnt lgkmcnt(0)\n"
"s_barrier\n"
"v_mfma_f32_16x16x32_bf16 v[188:191], v[152:155], v[128:131], v[188:191]\n"
"ds_read_b128 v[96:99], v2 offset:0\n"
"v_mfma_f32_16x16x32_bf16 v[192:195], v[152:155], v[132:135], v[192:195]\n"
"ds_read_b64_tr_b16 v[136:137], v4 offset:0\n"
"v_mfma_f32_16x16x32_bf16 v[212:215], v[156:159], v[132:135], v[212:215]\n"
"ds_read_b64_tr_b16 v[138:139], v4 offset:1024\n"
"v_mfma_f32_16x16x32_bf16 v[208:211], v[156:159], v[128:131], v[208:211]\n"
"ds_read_b128 v[100:103], v2 offset:2048\n"
"v_mfma_f32_16x16x32_bf16 v[204:207], v[156:159], v[124:127], v[204:207]\n"
"ds_read_b128 v[104:107], v2 offset:4096\n"
"v_mfma_f32_16x16x32_bf16 v[200:203], v[156:159], v[120:123], v[200:203]\n"
"ds_read_b128 v[108:111], v2 offset:6144\n"
"v_mfma_f32_16x16x32_bf16 v[196:199], v[156:159], v[116:119], v[196:199]\n"
"ds_read_b128 v[112:115], v2 offset:8192\n"
"v_mfma_f32_16x16x32_bf16 v[216:219], v[160:163], v[116:119], v[216:219]\n"
"ds_read_b64_tr_b16 v[140:141], v5 offset:0\n"
"v_mfma_f32_16x16x32_bf16 v[220:223], v[160:163], v[120:123], v[220:223]\n"
"ds_read_b64_tr_b16 v[142:143], v5 offset:1024\n"
"v_mfma_f32_16x16x32_bf16 v[224:227], v[160:163], v[124:127], v[224:227]\n"
"ds_read_b64_tr_b16 v[144:145], v6 offset:0\n"
"v_mfma_f32_16x16x32_bf16 v[228:231], v[160:163], v[128:131], v[228:231]\n"
"ds_read_b64_tr_b16 v[146:147], v6 offset:1024\n"
"v_mfma_f32_16x16x32_bf16 v[232:235], v[160:163], v[132:135], v[232:235]\n"
"ds_read_b64_tr_b16 v[148:149], v7 offset:0\n"
"v_mfma_f32_16x16x32_bf16 v[252:255], v[164:167], v[132:135], v[252:255]\n"
"ds_read_b64_tr_b16 v[150:151], v7 offset:1024\n"
"v_mfma_f32_16x16x32_bf16 v[248:251], v[164:167], v[128:131], v[248:251]\n"
"v_mfma_f32_16x16x32_bf16 v[244:247], v[164:167], v[124:127], v[244:247]\n"
"v_mfma_f32_16x16x32_bf16 v[240:243], v[164:167], v[120:123], v[240:243]\n"
"v_mfma_f32_16x16x32_bf16 v[236:239], v[164:167], v[116:119], v[236:239]\n"
"s_waitcnt lgkmcnt(6)\n"
"v_mfma_f32_16x16x32_bf16 v[176:179], v[136:139], v[96:99], v[176:179]\n"
"ds_read_b128 v[116:119], v3 offset:0\n"
"v_mfma_f32_16x16x32_bf16 v[180:183], v[136:139], v[100:103], v[180:183]\n"
"ds_read_b64_tr_b16 v[152:153], v4 offset:8192\n"
"v_mfma_f32_16x16x32_bf16 v[184:187], v[136:139], v[104:107], v[184:187]\n"
"ds_read_b64_tr_b16 v[154:155], v4 offset:9216\n"
"v_mfma_f32_16x16x32_bf16 v[188:191], v[136:139], v[108:111], v[188:191]\n"
"ds_read_b128 v[120:123], v3 offset:2048\n"
"v_mfma_f32_16x16x32_bf16 v[192:195], v[136:139], v[112:115], v[192:195]\n"
"ds_read_b128 v[124:127], v3 offset:4096\n"
"s_waitcnt lgkmcnt(5)\n"
"v_mfma_f32_16x16x32_bf16 v[212:215], v[140:143], v[112:115], v[212:215]\n"
"ds_read_b128 v[128:131], v3 offset:6144\n"
"v_mfma_f32_16x16x32_bf16 v[208:211], v[140:143], v[108:111], v[208:211]\n"
"ds_read_b128 v[132:135], v3 offset:8192\n"
"v_mfma_f32_16x16x32_bf16 v[204:207], v[140:143], v[104:107], v[204:207]\n"
"ds_read_b64_tr_b16 v[156:157], v5 offset:8192\n"
"v_mfma_f32_16x16x32_bf16 v[200:203], v[140:143], v[100:103], v[200:203]\n"
"ds_read_b64_tr_b16 v[158:159], v5 offset:9216\n"
"v_mfma_f32_16x16x32_bf16 v[196:199], v[140:143], v[96:99], v[196:199]\n"
"ds_read_b64_tr_b16 v[160:161], v6 offset:8192\n"
"v_mfma_f32_16x16x32_bf16 v[216:219], v[144:147], v[96:99], v[216:219]\n"
"ds_read_b64_tr_b16 v[162:163], v6 offset:9216\n"
"v_mfma_f32_16x16x32_bf16 v[220:223], v[144:147], v[100:103], v[220:223]\n"
"ds_read_b64_tr_b16 v[164:165], v7 offset:8192\n"
"v_mfma_f32_16x16x32_bf16 v[224:227], v[144:147], v[104:107], v[224:227]\n"
"ds_read_b64_tr_b16 v[166:167], v7 offset:9216\n"
"v_mfma_f32_16x16x32_bf16 v[228:231], v[144:147], v[108:111], v[228:231]\n"
"v_mfma_f32_16x16x32_bf16 v[232:235], v[144:147], v[112:115], v[232:235]\n"
"v_mfma_f32_16x16x32_bf16 v[252:255], v[148:151], v[112:115], v[252:255]\n"
"v_mfma_f32_16x16x32_bf16 v[248:251], v[148:151], v[108:111], v[248:251]\n"
"v_mfma_f32_16x16x32_bf16 v[244:247], v[148:151], v[104:107], v[244:247]\n"
"v_mfma_f32_16x16x32_bf16 v[240:243], v[148:151], v[100:103], v[240:243]\n"
"v_mfma_f32_16x16x32_bf16 v[236:239], v[148:151], v[96:99], v[236:239]\n"
"s_waitcnt lgkmcnt(6)\n"
"v_mfma_f32_16x16x32_bf16 v[176:179], v[152:155], v[116:119], v[176:179]\n"
"v_mfma_f32_16x16x32_bf16 v[180:183], v[152:155], v[120:123], v[180:183]\n"
"v_mfma_f32_16x16x32_bf16 v[184:187], v[152:155], v[124:127], v[184:187]\n"
"s_waitcnt lgkmcnt(0)\n"
"s_barrier\n"
"v_mfma_f32_16x16x32_bf16 v[188:191], v[152:155], v[128:131], v[188:191]\n"
"ds_read_b128 v[32:35], v14 offset:0\n"
"v_mfma_f32_16x16x32_bf16 v[192:195], v[152:155], v[132:135], v[192:195]\n"
"ds_read_b128 v[36:39], v14 offset:4096\n"
"v_mfma_f32_16x16x32_bf16 v[212:215], v[156:159], v[132:135], v[212:215]\n"
"ds_read_b128 v[40:43], v15 offset:0\n"
"v_mfma_f32_16x16x32_bf16 v[208:211], v[156:159], v[128:131], v[208:211]\n"
"ds_read_b128 v[44:47], v15 offset:4096\n"
"v_mfma_f32_16x16x32_bf16 v[204:207], v[156:159], v[124:127], v[204:207]\n"
"ds_read_b128 v[48:51], v16 offset:0\n"
"v_mfma_f32_16x16x32_bf16 v[200:203], v[156:159], v[120:123], v[200:203]\n"
"ds_read_b128 v[52:55], v16 offset:4096\n"
"v_mfma_f32_16x16x32_bf16 v[196:199], v[156:159], v[116:119], v[196:199]\n"
"ds_read_b128 v[56:59], v17 offset:0\n"
"v_mfma_f32_16x16x32_bf16 v[216:219], v[160:163], v[116:119], v[216:219]\n"
"ds_read_b128 v[60:63], v17 offset:4096\n"
"v_mfma_f32_16x16x32_bf16 v[220:223], v[160:163], v[120:123], v[220:223]\n"
"v_mfma_f32_16x16x32_bf16 v[224:227], v[160:163], v[124:127], v[224:227]\n"
"v_mul_f32 v176, s68, v176\n"
"v_mul_f32 v177, s68, v177\n"
"v_mul_f32 v178, s68, v178\n"
"v_mfma_f32_16x16x32_bf16 v[228:231], v[160:163], v[128:131], v[228:231]\n"
"v_mul_f32 v179, s68, v179\n"
"v_mul_f32 v180, s68, v180\n"
"v_mul_f32 v181, s68, v181\n"
"v_mfma_f32_16x16x32_bf16 v[232:235], v[160:163], v[132:135], v[232:235]\n"
"v_mul_f32 v182, s68, v182\n"
"v_mul_f32 v183, s68, v183\n"
"v_mul_f32 v184, s68, v184\n"
"v_mfma_f32_16x16x32_bf16 v[252:255], v[164:167], v[132:135], v[252:255]\n"
"v_mul_f32 v185, s68, v185\n"
"v_mul_f32 v186, s68, v186\n"
"v_mul_f32 v187, s68, v187\n"
"v_mfma_f32_16x16x32_bf16 v[248:251], v[164:167], v[128:131], v[248:251]\n"
"v_mul_f32 v188, s68, v188\n"
"v_mul_f32 v189, s68, v189\n"
"v_mul_f32 v190, s68, v190\n"
"v_mfma_f32_16x16x32_bf16 v[244:247], v[164:167], v[124:127], v[244:247]\n"
"v_mul_f32 v191, s68, v191\n"
"v_mul_f32 v192, s68, v192\n"
"v_mul_f32 v193, s68, v193\n"
"v_mfma_f32_16x16x32_bf16 v[240:243], v[164:167], v[120:123], v[240:243]\n"
"v_mul_f32 v194, s68, v194\n"
"v_mul_f32 v195, s68, v195\n"
"v_mul_f32 v196, s68, v196\n"
"v_mfma_f32_16x16x32_bf16 v[236:239], v[164:167], v[116:119], v[236:239]\n"
"v_mul_f32 v197, s68, v197\n"
"v_mul_f32 v198, s68, v198\n"
"v_mul_f32 v199, s68, v199\n"
"v_cvt_pk_bf16_f32 v176, v176, v177\n"
"v_pk_mul_f32 v[200:201], v[200:201], s[68:69]\n"
"v_cvt_pk_bf16_f32 v177, v178, v179\n"
"v_pk_mul_f32 v[202:203], v[202:203], s[68:69]\n"
"v_cvt_pk_bf16_f32 v180, v180, v181\n"
"v_pk_mul_f32 v[204:205], v[204:205], s[68:69]\n"
"v_cvt_pk_bf16_f32 v181, v182, v183\n"
"v_pk_mul_f32 v[206:207], v[206:207], s[68:69]\n"
"v_cvt_pk_bf16_f32 v184, v184, v185\n"
"v_pk_mul_f32 v[208:209], v[208:209], s[68:69]\n"
"v_cvt_pk_bf16_f32 v185, v186, v187\n"
"v_pk_mul_f32 v[210:211], v[210:211], s[68:69]\n"
"v_cvt_pk_bf16_f32 v188, v188, v189\n"
"v_pk_mul_f32 v[212:213], v[212:213], s[68:69]\n"
"v_cvt_pk_bf16_f32 v189, v190, v191\n"
"v_pk_mul_f32 v[214:215], v[214:215], s[68:69]\n"
"v_cvt_pk_bf16_f32 v192, v192, v193\n"
"v_cvt_pk_bf16_f32 v193, v194, v195\n"
"v_cvt_pk_bf16_f32 v196, v196, v197\n"
"v_pk_mul_f32 v[216:217], v[216:217], s[68:69]\n"
"ds_write_b64 v26, v[176:177] offset:0\n"
"v_cvt_pk_bf16_f32 v197, v198, v199\n"
"v_pk_mul_f32 v[218:219], v[218:219], s[68:69]\n"
"v_cvt_pk_bf16_f32 v200, v200, v201\n"
"v_pk_mul_f32 v[220:221], v[220:221], s[68:69]\n"
"ds_write_b64 v26, v[180:181] offset:4096\n"
"v_cvt_pk_bf16_f32 v201, v202, v203\n"
"v_pk_mul_f32 v[222:223], v[222:223], s[68:69]\n"
"v_cvt_pk_bf16_f32 v204, v204, v205\n"
"v_pk_mul_f32 v[224:225], v[224:225], s[68:69]\n"
"ds_write_b64 v26, v[184:185] offset:8192\n"
"v_cvt_pk_bf16_f32 v205, v206, v207\n"
"v_pk_mul_f32 v[226:227], v[226:227], s[68:69]\n"
"v_cvt_pk_bf16_f32 v208, v208, v209\n"
"v_pk_mul_f32 v[228:229], v[228:229], s[68:69]\n"
"ds_write_b64 v26, v[188:189] offset:12288\n"
"v_cvt_pk_bf16_f32 v209, v210, v211\n"
"v_pk_mul_f32 v[230:231], v[230:231], s[68:69]\n"
"v_cvt_pk_bf16_f32 v212, v212, v213\n"
"v_pk_mul_f32 v[232:233], v[232:233], s[68:69]\n"
"ds_write_b64 v26, v[192:193] offset:16384\n"
"v_cvt_pk_bf16_f32 v213, v214, v215\n"
"v_pk_mul_f32 v[234:235], v[234:235], s[68:69]\n"
"v_cvt_pk_bf16_f32 v216, v216, v217\n"
"v_pk_mul_f32 v[236:237], v[236:237], s[68:69]\n"
"ds_write_b64 v27, v[196:197] offset:0\n"
"v_cvt_pk_bf16_f32 v217, v218, v219\n"
"v_pk_mul_f32 v[238:239], v[238:239], s[68:69]\n"
"v_cvt_pk_bf16_f32 v220, v220, v221\n"
"v_pk_mul_f32 v[240:241], v[240:241], s[68:69]\n"
"ds_write_b64 v27, v[200:201] offset:4096\n"
"v_cvt_pk_bf16_f32 v221, v222, v223\n"
"v_pk_mul_f32 v[242:243], v[242:243], s[68:69]\n"
"v_cvt_pk_bf16_f32 v224, v224, v225\n"
"v_pk_mul_f32 v[244:245], v[244:245], s[68:69]\n"
"ds_write_b64 v27, v[204:205] offset:8192\n"
"v_cvt_pk_bf16_f32 v225, v226, v227\n"
"v_pk_mul_f32 v[246:247], v[246:247], s[68:69]\n"
"v_cvt_pk_bf16_f32 v228, v228, v229\n"
"v_pk_mul_f32 v[248:249], v[248:249], s[68:69]\n"
"ds_write_b64 v27, v[208:209] offset:12288\n"
"v_cvt_pk_bf16_f32 v229, v230, v231\n"
"v_pk_mul_f32 v[250:251], v[250:251], s[68:69]\n"
"v_cvt_pk_bf16_f32 v232, v232, v233\n"
"v_pk_mul_f32 v[252:253], v[252:253], s[68:69]\n"
"ds_write_b64 v27, v[212:213] offset:16384\n"
"v_cvt_pk_bf16_f32 v233, v234, v235\n"
"v_pk_mul_f32 v[254:255], v[254:255], s[68:69]\n"
"v_cvt_pk_bf16_f32 v236, v236, v237\n"
"ds_write_b64 v28, v[216:217] offset:0\n"
"v_cvt_pk_bf16_f32 v237, v238, v239\n"
"v_cvt_pk_bf16_f32 v240, v240, v241\n"
"ds_write_b64 v28, v[220:221] offset:4096\n"
"v_cvt_pk_bf16_f32 v241, v242, v243\n"
"v_cvt_pk_bf16_f32 v244, v244, v245\n"
"ds_write_b64 v28, v[224:225] offset:8192\n"
"v_cvt_pk_bf16_f32 v245, v246, v247\n"
"v_cvt_pk_bf16_f32 v248, v248, v249\n"
"ds_write_b64 v28, v[228:229] offset:12288\n"
"v_cvt_pk_bf16_f32 v249, v250, v251\n"
"v_cvt_pk_bf16_f32 v252, v252, v253\n"
"ds_write_b64 v28, v[232:233] offset:16384\n"
"v_cvt_pk_bf16_f32 v253, v254, v255\n"
"ds_write_b64 v29, v[236:237] offset:0\n"
"ds_write_b64 v29, v[240:241] offset:4096\n"
"ds_write_b64 v29, v[244:245] offset:8192\n"
"ds_write_b64 v29, v[248:249] offset:12288\n"
"ds_write_b64 v29, v[252:253] offset:16384\n"
"v_mov_b32 v208, 0\n"
"v_mov_b32 v209, 0\n"
"v_mov_b32 v210, 0\n"
"v_mov_b32 v211, 0\n"
"v_mov_b32 v212, 0\n"
"s_waitcnt lgkmcnt(0)\n"
"s_setprio 1\n"
"s_barrier\n"
"ds_read_b128 v[96:99], v22 offset:0\n"
"ds_read_b128 v[100:103], v23 offset:0\n"
"ds_read_b128 v[104:107], v24 offset:0\n"
"ds_read_b128 v[108:111], v25 offset:0\n"
"ds_read_b128 v[112:115], v22 offset:4096\n"
"ds_read_b128 v[116:119], v23 offset:4096\n"
"ds_read_b128 v[120:123], v24 offset:4096\n"
"ds_read_b128 v[124:127], v25 offset:4096\n"
"s_waitcnt lgkmcnt(7)\n"
"v_mfma_f32_16x16x32_bf16 v[176:179], v[32:35], v[96:99], 0\n"
"ds_read_b128 v[128:131], v22 offset:8192\n"
"v_mfma_f32_16x16x32_bf16 v[180:183], v[36:39], v[96:99], 0\n"
"ds_read_b128 v[132:135], v23 offset:8192\n"
"v_mov_b32 v248, 162368\n"
"ds_read_b32 v248, v248\n"
"s_waitcnt lgkmcnt(9)\n"
"v_mfma_f32_16x16x32_bf16 v[176:179], v[40:43], v[100:103], v[176:179]\n"
"ds_read_b128 v[136:139], v24 offset:8192\n"
"s_lshl_b32 s61, s59, 9\n"
"v_min_u32 v252, 11, v0\n"
"v_mfma_f32_16x16x32_bf16 v[180:183], v[44:47], v[100:103], v[180:183]\n"
"ds_read_b128 v[140:143], v25 offset:8192\n"
"s_add_u32 s62, s61, 144\n"
"v_lshl_add_u32 v249, v252, 2, s62\n"
"s_waitcnt lgkmcnt(10)\n"
"v_mfma_f32_16x16x32_bf16 v[176:179], v[48:51], v[104:107], v[176:179]\n"
"ds_read_b128 v[144:147], v22 offset:12288\n"
"s_and_b32 s61, s59, 31\n"
"v_lshl_or_b32 v253, v252, 5, s61\n"
"v_mfma_f32_16x16x32_bf16 v[180:183], v[52:55], v[104:107], v[180:183]\n"
"ds_read_b128 v[148:151], v23 offset:12288\n"
"s_and_b32 s62, s59, 0xf9\n"
"v_and_b32 v254, 3, v252\n"
"s_waitcnt lgkmcnt(11)\n"
"v_mfma_f32_16x16x32_bf16 v[176:179], v[56:59], v[108:111], v[176:179]\n"
"ds_read_b128 v[152:155], v24 offset:12288\n"
"v_lshl_or_b32 v254, v254, 1, s62\n"
"v_cmp_gt_u32 vcc, 8, v252\n"
"v_mfma_f32_16x16x32_bf16 v[180:183], v[60:63], v[108:111], v[180:183]\n"
"ds_read_b128 v[156:159], v25 offset:12288\n"
"s_add_u32 s61, s57, 8\n"
"v_mov_b32 v252, s58\n"
"s_waitcnt lgkmcnt(12)\n"
"v_mfma_f32_16x16x32_bf16 v[184:187], v[32:35], v[112:115], 0\n"
"ds_read_b128 v[160:163], v22 offset:16384\n"
"v_cndmask_b32 v253, v254, v253, vcc\n"
"v_mov_b32 v254, s61\n"
"v_mfma_f32_16x16x32_bf16 v[188:191], v[36:39], v[112:115], 0\n"
"ds_read_b128 v[164:167], v23 offset:16384\n"
"v_lshlrev_b32 v250, 9, v253\n"
"v_cndmask_b32 v254, v254, v252, vcc\n"
"s_waitcnt lgkmcnt(13)\n"
"v_mfma_f32_16x16x32_bf16 v[184:187], v[40:43], v[116:119], v[184:187]\n"
"v_exp_f32 v176, v176\n"
"ds_read_b128 v[168:171], v24 offset:16384\n"
"v_lshl_add_u32 v251, v254, 2, v250\n"
"s_waitcnt lgkmcnt(9)\n"
"v_readfirstlane_b32 s70, v248\n"
"v_mfma_f32_16x16x32_bf16 v[188:191], v[44:47], v[116:119], v[188:191]\n"
"v_exp_f32 v177, v177\n"
"ds_read_b128 v[172:175], v25 offset:16384\n"
"v_add_f32 v208, v176, v208\n"
"v_mfma_f32_16x16x32_bf16 v[184:187], v[48:51], v[120:123], v[184:187]\n"
"v_exp_f32 v178, v178\n"
"v_add_f32 v208, v177, v208\n"
"v_cvt_pk_bf16_f32 v200, v176, v177\n"
"v_mfma_f32_16x16x32_bf16 v[188:191], v[52:55], v[120:123], v[188:191]\n"
"v_exp_f32 v179, v179\n"
"v_add_f32 v208, v178, v208\n"
"v_mfma_f32_16x16x32_bf16 v[184:187], v[56:59], v[124:127], v[184:187]\n"
"v_exp_f32 v180, v180\n"
"ds_read_b128 v[64:67], v14 offset:16384\n"
"v_add_f32 v208, v179, v208\n"
"v_cvt_pk_bf16_f32 v201, v178, v179\n"
"v_mfma_f32_16x16x32_bf16 v[188:191], v[60:63], v[124:127], v[188:191]\n"
"v_exp_f32 v181, v181\n"
"v_add_f32 v208, v180, v208\n"
"v_mfma_f32_16x16x32_bf16 v[192:195], v[32:35], v[128:131], 0\n"
"v_exp_f32 v182, v182\n"
"ds_read_b128 v[68:71], v14 offset:20480\n"
"v_add_f32 v208, v181, v208\n"
"v_cvt_pk_bf16_f32 v202, v180, v181\n"
"v_mfma_f32_16x16x32_bf16 v[196:199], v[36:39], v[128:131], 0\n"
"v_exp_f32 v183, v183\n"
"v_add_f32 v208, v182, v208\n"
"v_mfma_f32_16x16x32_bf16 v[192:195], v[40:43], v[132:135], v[192:195]\n"
"v_exp_f32 v184, v184\n"
"ds_read_b128 v[72:75], v15 offset:16384\n"
"v_add_f32 v208, v183, v208\n"
"v_cvt_pk_bf16_f32 v203, v182, v183\n"
"v_mfma_f32_16x16x32_bf16 v[196:199], v[44:47], v[132:135], v[196:199]\n"
"v_exp_f32 v185, v185\n"
"ds_write_b128 v30, v[200:203] offset:0\n"
"v_add_f32 v209, v184, v209\n"
"s_waitcnt lgkmcnt(13)\n"
"v_mfma_f32_16x16x32_bf16 v[192:195], v[48:51], v[136:139], v[192:195]\n"
"v_exp_f32 v186, v186\n"
"ds_read_b128 v[76:79], v15 offset:20480\n"
"v_add_f32 v209, v185, v209\n"
"v_cvt_pk_bf16_f32 v204, v184, v185\n"
"v_mfma_f32_16x16x32_bf16 v[196:199], v[52:55], v[136:139], v[196:199]\n"
"v_exp_f32 v187, v187\n"
"v_add_f32 v209, v186, v209\n"
"s_waitcnt lgkmcnt(13)\n"
"v_mfma_f32_16x16x32_bf16 v[192:195], v[56:59], v[140:143], v[192:195]\n"
"v_exp_f32 v188, v188\n"
"ds_read_b128 v[80:83], v16 offset:16384\n"
"v_add_f32 v209, v187, v209\n"
"v_cvt_pk_bf16_f32 v205, v186, v187\n"
"v_mfma_f32_16x16x32_bf16 v[196:199], v[60:63], v[140:143], v[196:199]\n"
"v_exp_f32 v189, v189\n"
"v_add_f32 v209, v188, v209\n"
"s_waitcnt lgkmcnt(13)\n"
"v_mfma_f32_16x16x32_bf16 v[176:179], v[32:35], v[144:147], 0\n"
"v_exp_f32 v190, v190\n"
"ds_read_b128 v[84:87], v16 offset:20480\n"
"v_add_f32 v209, v189, v209\n"
"v_cvt_pk_bf16_f32 v206, v188, v189\n"
"v_mfma_f32_16x16x32_bf16 v[180:183], v[36:39], v[144:147], 0\n"
"v_exp_f32 v191, v191\n"
"v_add_f32 v209, v190, v209\n"
"s_waitcnt lgkmcnt(13)\n"
"v_mfma_f32_16x16x32_bf16 v[176:179], v[40:43], v[148:151], v[176:179]\n"
"v_exp_f32 v192, v192\n"
"ds_read_b128 v[88:91], v17 offset:16384\n"
"v_add_f32 v209, v191, v209\n"
"v_cvt_pk_bf16_f32 v207, v190, v191\n"
"v_mfma_f32_16x16x32_bf16 v[180:183], v[44:47], v[148:151], v[180:183]\n"
"v_exp_f32 v193, v193\n"
"ds_write_b128 v30, v[204:207] offset:1024\n"
"v_add_f32 v210, v192, v210\n"
"s_waitcnt lgkmcnt(14)\n"
"v_mfma_f32_16x16x32_bf16 v[176:179], v[48:51], v[152:155], v[176:179]\n"
"v_exp_f32 v194, v194\n"
"ds_read_b128 v[92:95], v17 offset:20480\n"
"v_add_f32 v210, v193, v210\n"
"v_cvt_pk_bf16_f32 v200, v192, v193\n"
"v_mfma_f32_16x16x32_bf16 v[180:183], v[52:55], v[152:155], v[180:183]\n"
"v_exp_f32 v195, v195\n"
"v_add_f32 v210, v194, v210\n"
"s_waitcnt lgkmcnt(14)\n"
"v_mfma_f32_16x16x32_bf16 v[176:179], v[56:59], v[156:159], v[176:179]\n"
"v_exp_f32 v196, v196\n"
"v_add_f32 v210, v195, v210\n"
"v_cvt_pk_bf16_f32 v201, v194, v195\n"
"v_mfma_f32_16x16x32_bf16 v[180:183], v[60:63], v[156:159], v[180:183]\n"
"v_exp_f32 v197, v197\n"
"v_add_f32 v210, v196, v210\n"
"s_waitcnt lgkmcnt(13)\n"
"v_mfma_f32_16x16x32_bf16 v[184:187], v[32:35], v[160:163], 0\n"
"v_exp_f32 v198, v198\n"
"v_add_f32 v210, v197, v210\n"
"v_cvt_pk_bf16_f32 v202, v196, v197\n"
"v_mfma_f32_16x16x32_bf16 v[188:191], v[36:39], v[160:163], 0\n"
"v_exp_f32 v199, v199\n"
"v_add_f32 v210, v198, v210\n"
"s_waitcnt lgkmcnt(12)\n"
"v_mfma_f32_16x16x32_bf16 v[184:187], v[40:43], v[164:167], v[184:187]\n"
"v_exp_f32 v176, v176\n"
"v_add_f32 v210, v199, v210\n"
"v_cvt_pk_bf16_f32 v203, v198, v199\n"
"v_mfma_f32_16x16x32_bf16 v[188:191], v[44:47], v[164:167], v[188:191]\n"
"v_exp_f32 v177, v177\n"
"ds_write_b128 v30, v[200:203] offset:2048\n"
"v_add_f32 v211, v176, v211\n"
"s_waitcnt lgkmcnt(12)\n"
"v_mfma_f32_16x16x32_bf16 v[184:187], v[48:51], v[168:171], v[184:187]\n"
"v_exp_f32 v178, v178\n"
"v_add_f32 v211, v177, v211\n"
"v_cvt_pk_bf16_f32 v204, v176, v177\n"
"v_mfma_f32_16x16x32_bf16 v[188:191], v[52:55], v[168:171], v[188:191]\n"
"v_exp_f32 v179, v179\n"
"v_add_f32 v211, v178, v211\n"
"s_waitcnt lgkmcnt(11)\n"
"v_mfma_f32_16x16x32_bf16 v[184:187], v[56:59], v[172:175], v[184:187]\n"
"v_exp_f32 v180, v180\n"
"v_add_f32 v211, v179, v211\n"
"v_cvt_pk_bf16_f32 v205, v178, v179\n"
"v_mfma_f32_16x16x32_bf16 v[188:191], v[60:63], v[172:175], v[188:191]\n"
"v_exp_f32 v181, v181\n"
"v_add_f32 v211, v180, v211\n"
"s_waitcnt lgkmcnt(10)\n"
"v_mfma_f32_16x16x32_bf16 v[192:195], v[64:67], v[96:99], 0\n"
"v_exp_f32 v182, v182\n"
"v_add_f32 v211, v181, v211\n"
"v_cvt_pk_bf16_f32 v206, v180, v181\n"
"s_waitcnt lgkmcnt(9)\n"
"v_mfma_f32_16x16x32_bf16 v[196:199], v[68:71], v[96:99], 0\n"
"v_exp_f32 v183, v183\n"
"v_add_f32 v211, v182, v211\n"
"s_waitcnt lgkmcnt(8)\n"
"v_mfma_f32_16x16x32_bf16 v[192:195], v[72:75], v[100:103], v[192:195]\n"
"v_exp_f32 v184, v184\n"
"v_add_f32 v211, v183, v211\n"
"v_cvt_pk_bf16_f32 v207, v182, v183\n"
"s_waitcnt lgkmcnt(6)\n"
"v_mfma_f32_16x16x32_bf16 v[196:199], v[76:79], v[100:103], v[196:199]\n"
"v_exp_f32 v185, v185\n"
"ds_write_b128 v30, v[204:207] offset:3072\n"
"v_add_f32 v212, v184, v212\n"
"s_waitcnt lgkmcnt(6)\n"
"v_mfma_f32_16x16x32_bf16 v[192:195], v[80:83], v[104:107], v[192:195]\n"
"v_exp_f32 v186, v186\n"
"v_add_f32 v212, v185, v212\n"
"v_cvt_pk_bf16_f32 v200, v184, v185\n"
"s_waitcnt lgkmcnt(5)\n"
"v_mfma_f32_16x16x32_bf16 v[196:199], v[84:87], v[104:107], v[196:199]\n"
"v_exp_f32 v187, v187\n"
"v_add_f32 v212, v186, v212\n"
"s_waitcnt lgkmcnt(4)\n"
"v_mfma_f32_16x16x32_bf16 v[192:195], v[88:91], v[108:111], v[192:195]\n"
"v_exp_f32 v188, v188\n"
"v_add_f32 v212, v187, v212\n"
"v_cvt_pk_bf16_f32 v201, v186, v187\n"
"s_waitcnt lgkmcnt(2)\n"
"v_mfma_f32_16x16x32_bf16 v[196:199], v[92:95], v[108:111], v[196:199]\n"
"v_exp_f32 v189, v189\n"
"v_add_f32 v212, v188, v212\n"
"v_mfma_f32_16x16x32_bf16 v[176:179], v[64:67], v[112:115], 0\n"
"v_exp_f32 v190, v190\n"
"v_add_f32 v212, v189, v212\n"
"v_cvt_pk_bf16_f32 v202, v188, v189\n"
"v_mfma_f32_16x16x32_bf16 v[180:183], v[68:71], v[112:115], 0\n"
"v_exp_f32 v191, v191\n"
"v_add_f32 v212, v190, v212\n"
"v_mfma_f32_16x16x32_bf16 v[176:179], v[72:75], v[116:119], v[176:179]\n"
"v_exp_f32 v192, v192\n"
"v_add_f32 v212, v191, v212\n"
"v_cvt_pk_bf16_f32 v203, v190, v191\n"
"v_mfma_f32_16x16x32_bf16 v[180:183], v[76:79], v[116:119], v[180:183]\n"
"v_exp_f32 v193, v193\n"
"ds_write_b128 v30, v[200:203] offset:4096\n"
"v_add_f32 v208, v192, v208\n"
"v_mfma_f32_16x16x32_bf16 v[176:179], v[80:83], v[120:123], v[176:179]\n"
"v_exp_f32 v194, v194\n"
"v_add_f32 v208, v193, v208\n"
"v_cvt_pk_bf16_f32 v204, v192, v193\n"
"v_mfma_f32_16x16x32_bf16 v[180:183], v[84:87], v[120:123], v[180:183]\n"
"v_exp_f32 v195, v195\n"
"s_waitcnt lgkmcnt(0)\n"
"s_barrier\n"
"v_add_f32 v208, v194, v208\n"
"v_mfma_f32_16x16x32_bf16 v[176:179], v[88:91], v[124:127], v[176:179]\n"
"v_exp_f32 v196, v196\n"
"ds_read_b128 v[32:35], v14 offset:32768\n"
"v_add_f32 v208, v195, v208\n"
"v_cvt_pk_bf16_f32 v205, v194, v195\n"
"v_mfma_f32_16x16x32_bf16 v[180:183], v[92:95], v[124:127], v[180:183]\n"
"v_exp_f32 v197, v197\n"
"v_add_f32 v208, v196, v208\n"
"v_mfma_f32_16x16x32_bf16 v[184:187], v[64:67], v[128:131], 0\n"
"v_exp_f32 v198, v198\n"
"ds_read_b128 v[36:39], v14 offset:36864\n"
"v_add_f32 v208, v197, v208\n"
"v_cvt_pk_bf16_f32 v206, v196, v197\n"
"v_mfma_f32_16x16x32_bf16 v[188:191], v[68:71], v[128:131], 0\n"
"v_exp_f32 v199, v199\n"
"v_add_f32 v208, v198, v208\n"
"v_mfma_f32_16x16x32_bf16 v[184:187], v[72:75], v[132:135], v[184:187]\n"
"v_exp_f32 v176, v176\n"
"ds_read_b128 v[40:43], v15 offset:32768\n"
"v_add_f32 v208, v199, v208\n"
"v_cvt_pk_bf16_f32 v207, v198, v199\n"
"v_mfma_f32_16x16x32_bf16 v[188:191], v[76:79], v[132:135], v[188:191]\n"
"v_exp_f32 v177, v177\n"
"ds_write_b128 v30, v[204:207] offset:40960\n"
"v_add_f32 v209, v176, v209\n"
"v_mfma_f32_16x16x32_bf16 v[184:187], v[80:83], v[136:139], v[184:187]\n"
"v_exp_f32 v178, v178\n"
"ds_read_b128 v[44:47], v15 offset:36864\n"
"v_add_f32 v209, v177, v209\n"
"v_cvt_pk_bf16_f32 v200, v176, v177\n"
"global_load_dword v248, v249, s[52:53] sc1\n"
"v_mfma_f32_16x16x32_bf16 v[188:191], v[84:87], v[136:139], v[188:191]\n"
"v_exp_f32 v179, v179\n"
"v_add_f32 v209, v178, v209\n"
"v_mfma_f32_16x16x32_bf16 v[184:187], v[88:91], v[140:143], v[184:187]\n"
"v_exp_f32 v180, v180\n"
"ds_read_b128 v[48:51], v16 offset:32768\n"
"v_add_f32 v209, v179, v209\n"
"v_cvt_pk_bf16_f32 v201, v178, v179\n"
"v_mfma_f32_16x16x32_bf16 v[188:191], v[92:95], v[140:143], v[188:191]\n"
"v_exp_f32 v181, v181\n"
"v_add_f32 v209, v180, v209\n"
"v_mfma_f32_16x16x32_bf16 v[192:195], v[64:67], v[144:147], 0\n"
"v_exp_f32 v182, v182\n"
"ds_read_b128 v[52:55], v16 offset:36864\n"
"v_add_f32 v209, v181, v209\n"
"v_cvt_pk_bf16_f32 v202, v180, v181\n"
"v_mfma_f32_16x16x32_bf16 v[196:199], v[68:71], v[144:147], 0\n"
"v_exp_f32 v183, v183\n"
"v_add_f32 v209, v182, v209\n"
"v_mfma_f32_16x16x32_bf16 v[192:195], v[72:75], v[148:151], v[192:195]\n"
"v_exp_f32 v184, v184\n"
"ds_read_b128 v[56:59], v17 offset:32768\n"
"v_add_f32 v209, v183, v209\n"
"v_cvt_pk_bf16_f32 v203, v182, v183\n"
"v_mfma_f32_16x16x32_bf16 v[196:199], v[76:79], v[148:151], v[196:199]\n"
"v_exp_f32 v185, v185\n"
"ds_write_b128 v30, v[200:203] offset:41984\n"
"v_add_f32 v210, v184, v210\n"
"v_mfma_f32_16x16x32_bf16 v[192:195], v[80:83], v[152:155], v[192:195]\n"
"v_exp_f32 v186, v186\n"
"ds_read_b128 v[60:63], v17 offset:36864\n"
"v_add_f32 v210, v185, v210\n"
"v_cvt_pk_bf16_f32 v204, v184, v185\n"
"v_mfma_f32_16x16x32_bf16 v[196:199], v[84:87], v[152:155], v[196:199]\n"
"v_exp_f32 v187, v187\n"
"v_add_f32 v210, v186, v210\n"
"v_mfma_f32_16x16x32_bf16 v[192:195], v[88:91], v[156:159], v[192:195]\n"
"v_exp_f32 v188, v188\n"
"v_add_f32 v210, v187, v210\n"
"v_cvt_pk_bf16_f32 v205, v186, v187\n"
"v_mfma_f32_16x16x32_bf16 v[196:199], v[92:95], v[156:159], v[196:199]\n"
"v_exp_f32 v189, v189\n"
"v_add_f32 v210, v188, v210\n"
"v_mfma_f32_16x16x32_bf16 v[176:179], v[64:67], v[160:163], 0\n"
"v_exp_f32 v190, v190\n"
"v_add_f32 v210, v189, v210\n"
"v_cvt_pk_bf16_f32 v206, v188, v189\n"
"v_mfma_f32_16x16x32_bf16 v[180:183], v[68:71], v[160:163], 0\n"
"v_exp_f32 v191, v191\n"
"v_add_f32 v210, v190, v210\n"
"v_mfma_f32_16x16x32_bf16 v[176:179], v[72:75], v[164:167], v[176:179]\n"
"v_exp_f32 v192, v192\n"
"v_add_f32 v210, v191, v210\n"
"v_cvt_pk_bf16_f32 v207, v190, v191\n"
"v_mfma_f32_16x16x32_bf16 v[180:183], v[76:79], v[164:167], v[180:183]\n"
"v_exp_f32 v193, v193\n"
"ds_write_b128 v30, v[204:207] offset:43008\n"
"v_add_f32 v211, v192, v211\n"
"v_mfma_f32_16x16x32_bf16 v[176:179], v[80:83], v[168:171], v[176:179]\n"
"v_exp_f32 v194, v194\n"
"v_add_f32 v211, v193, v211\n"
"v_cvt_pk_bf16_f32 v200, v192, v193\n"
"v_mfma_f32_16x16x32_bf16 v[180:183], v[84:87], v[168:171], v[180:183]\n"
"v_exp_f32 v195, v195\n"
"v_add_f32 v211, v194, v211\n"
"v_mfma_f32_16x16x32_bf16 v[176:179], v[88:91], v[172:175], v[176:179]\n"
"v_exp_f32 v196, v196\n"
"v_add_f32 v211, v195, v211\n"
"v_cvt_pk_bf16_f32 v201, v194, v195\n"
"v_mfma_f32_16x16x32_bf16 v[180:183], v[92:95], v[172:175], v[180:183]\n"
"v_exp_f32 v197, v197\n"
"v_add_f32 v211, v196, v211\n"
"s_waitcnt lgkmcnt(10)\n"
"v_mfma_f32_16x16x32_bf16 v[184:187], v[32:35], v[96:99], 0\n"
"v_exp_f32 v198, v198\n"
"v_add_f32 v211, v197, v211\n"
"v_cvt_pk_bf16_f32 v202, v196, v197\n"
"s_waitcnt lgkmcnt(9)\n"
"v_mfma_f32_16x16x32_bf16 v[188:191], v[36:39], v[96:99], 0\n"
"v_exp_f32 v199, v199\n"
"v_add_f32 v211, v198, v211\n"
"s_waitcnt lgkmcnt(8)\n"
"v_mfma_f32_16x16x32_bf16 v[184:187], v[40:43], v[100:103], v[184:187]\n"
"v_exp_f32 v176, v176\n"
"v_add_f32 v211, v199, v211\n"
"v_cvt_pk_bf16_f32 v203, v198, v199\n"
"s_waitcnt lgkmcnt(6)\n"
"v_mfma_f32_16x16x32_bf16 v[188:191], v[44:47], v[100:103], v[188:191]\n"
"v_exp_f32 v177, v177\n"
"ds_write_b128 v30, v[200:203] offset:44032\n"
"v_add_f32 v212, v176, v212\n"
"s_waitcnt lgkmcnt(6)\n"
"v_mfma_f32_16x16x32_bf16 v[184:187], v[48:51], v[104:107], v[184:187]\n"
"v_exp_f32 v178, v178\n"
"v_add_f32 v212, v177, v212\n"
"v_cvt_pk_bf16_f32 v204, v176, v177\n"
"s_waitcnt lgkmcnt(5)\n"
"v_mfma_f32_16x16x32_bf16 v[188:191], v[52:55], v[104:107], v[188:191]\n"
"v_exp_f32 v179, v179\n"
"v_add_f32 v212, v178, v212\n"
"s_waitcnt lgkmcnt(4)\n"
"v_mfma_f32_16x16x32_bf16 v[184:187], v[56:59], v[108:111], v[184:187]\n"
"v_exp_f32 v180, v180\n"
"v_add_f32 v212, v179, v212\n"
"v_cvt_pk_bf16_f32 v205, v178, v179\n"
"s_waitcnt lgkmcnt(2)\n"
"v_mfma_f32_16x16x32_bf16 v[188:191], v[60:63], v[108:111], v[188:191]\n"
"v_exp_f32 v181, v181\n"
"v_add_f32 v212, v180, v212\n"
"v_mfma_f32_16x16x32_bf16 v[192:195], v[32:35], v[112:115], 0\n"
"v_exp_f32 v182, v182\n"
"v_add_f32 v212, v181, v212\n"
"v_cvt_pk_bf16_f32 v206, v180, v181\n"
"v_mfma_f32_16x16x32_bf16 v[196:199], v[36:39], v[112:115], 0\n"
"v_exp_f32 v183, v183\n"
"v_add_f32 v212, v182, v212\n"
"v_mfma_f32_16x16x32_bf16 v[192:195], v[40:43], v[116:119], v[192:195]\n"
"v_exp_f32 v184, v184\n"
"v_add_f32 v212, v183, v212\n"
"v_cvt_pk_bf16_f32 v207, v182, v183\n"
"s_mul_i32 s61, s58, 40960\n"
"s_mul_i32 s62, s54, 10240\n"
"s_add_u32 s61, s61, s62\n"
"s_mul_i32 s62, s54, 40960\n"
"s_mul_i32 s63, s57, 10240\n"
"s_add_u32 s62, s62, s63\n"
"s_lshr_b32 s63, s57, 1\n"
"s_mul_i32 s63, s63, 163840\n"
"s_and_b32 s65, s57, 1\n"
"s_lshl_b32 s65, s65, 7\n"
"s_add_u32 s63, s63, s65\n"
"s_mul_i32 s65, s54, 0\n"
"s_add_u32 s63, s63, s65\n"
"v_mov_b32 v252, 0\n"
"s_mov_b64 exec, 1\n"
"buffer_load_dword v254, v252, s[40:43], s61 offen\n"
"buffer_load_dword v254, v252, s[44:47], s62 offen\n"
"buffer_load_dword v254, v252, s[48:51], s63 offen\n"
"s_mov_b64 exec, -1\n"
"v_mfma_f32_16x16x32_bf16 v[196:199], v[44:47], v[116:119], v[196:199]\n"
"v_exp_f32 v185, v185\n"
"ds_write_b128 v30, v[204:207] offset:45056\n"
"v_add_f32 v208, v184, v208\n"
"v_mfma_f32_16x16x32_bf16 v[192:195], v[48:51], v[120:123], v[192:195]\n"
"v_exp_f32 v186, v186\n"
"v_add_f32 v208, v185, v208\n"
"v_cvt_pk_bf16_f32 v200, v184, v185\n"
"v_mfma_f32_16x16x32_bf16 v[196:199], v[52:55], v[120:123], v[196:199]\n"
"v_exp_f32 v187, v187\n"
"s_waitcnt lgkmcnt(0)\n"
"s_barrier\n"
"v_add_f32 v208, v186, v208\n"
"v_mfma_f32_16x16x32_bf16 v[192:195], v[56:59], v[124:127], v[192:195]\n"
"v_exp_f32 v188, v188\n"
"ds_read_b128 v[64:67], v18 offset:32768\n"
"v_add_f32 v208, v187, v208\n"
"v_cvt_pk_bf16_f32 v201, v186, v187\n"
"v_mfma_f32_16x16x32_bf16 v[196:199], v[60:63], v[124:127], v[196:199]\n"
"v_exp_f32 v189, v189\n"
"v_add_f32 v208, v188, v208\n"
"v_mfma_f32_16x16x32_bf16 v[176:179], v[32:35], v[128:131], 0\n"
"v_exp_f32 v190, v190\n"
"ds_read_b128 v[68:71], v18 offset:36864\n"
"v_add_f32 v208, v189, v208\n"
"v_cvt_pk_bf16_f32 v202, v188, v189\n"
"v_mfma_f32_16x16x32_bf16 v[180:183], v[36:39], v[128:131], 0\n"
"v_exp_f32 v191, v191\n"
"v_add_f32 v208, v190, v208\n"
"v_mfma_f32_16x16x32_bf16 v[176:179], v[40:43], v[132:135], v[176:179]\n"
"v_exp_f32 v192, v192\n"
"ds_read_b128 v[72:75], v19 offset:32768\n"
"v_add_f32 v208, v191, v208\n"
"v_cvt_pk_bf16_f32 v203, v190, v191\n"
"v_mfma_f32_16x16x32_bf16 v[180:183], v[44:47], v[132:135], v[180:183]\n"
"v_exp_f32 v193, v193\n"
"ds_write_b128 v30, v[200:203] offset:0\n"
"v_add_f32 v209, v192, v209\n"
"v_mfma_f32_16x16x32_bf16 v[176:179], v[48:51], v[136:139], v[176:179]\n"
"v_exp_f32 v194, v194\n"
"ds_read_b128 v[76:79], v19 offset:36864\n"
"v_add_f32 v209, v193, v209\n"
"v_cvt_pk_bf16_f32 v204, v192, v193\n"
"v_mfma_f32_16x16x32_bf16 v[180:183], v[52:55], v[136:139], v[180:183]\n"
"v_exp_f32 v195, v195\n"
"v_add_f32 v209, v194, v209\n"
"v_mfma_f32_16x16x32_bf16 v[176:179], v[56:59], v[140:143], v[176:179]\n"
"v_exp_f32 v196, v196\n"
"ds_read_b128 v[80:83], v20 offset:32768\n"
"v_add_f32 v209, v195, v209\n"
"v_cvt_pk_bf16_f32 v205, v194, v195\n"
"v_mfma_f32_16x16x32_bf16 v[180:183], v[60:63], v[140:143], v[180:183]\n"
"v_exp_f32 v197, v197\n"
"v_add_f32 v209, v196, v209\n"
"v_mfma_f32_16x16x32_bf16 v[184:187], v[32:35], v[144:147], 0\n"
"v_exp_f32 v198, v198\n"
"ds_read_b128 v[84:87], v20 offset:36864\n"
"v_add_f32 v209, v197, v209\n"
"v_cvt_pk_bf16_f32 v206, v196, v197\n"
"v_mfma_f32_16x16x32_bf16 v[188:191], v[36:39], v[144:147], 0\n"
"v_exp_f32 v199, v199\n"
"v_add_f32 v209, v198, v209\n"
"v_mfma_f32_16x16x32_bf16 v[184:187], v[40:43], v[148:151], v[184:187]\n"
"v_exp_f32 v176, v176\n"
"ds_read_b128 v[88:91], v21 offset:32768\n"
"v_add_f32 v209, v199, v209\n"
"v_cvt_pk_bf16_f32 v207, v198, v199\n"
"v_mfma_f32_16x16x32_bf16 v[188:191], v[44:47], v[148:151], v[188:191]\n"
"v_exp_f32 v177, v177\n"
"ds_write_b128 v30, v[204:207] offset:1024\n"
"v_add_f32 v210, v176, v210\n"
"v_mfma_f32_16x16x32_bf16 v[184:187], v[48:51], v[152:155], v[184:187]\n"
"v_exp_f32 v178, v178\n"
"ds_read_b128 v[92:95], v21 offset:36864\n"
"v_add_f32 v210, v177, v210\n"
"v_cvt_pk_bf16_f32 v200, v176, v177\n"
"v_mfma_f32_16x16x32_bf16 v[188:191], v[52:55], v[152:155], v[188:191]\n"
"v_exp_f32 v179, v179\n"
"v_add_f32 v210, v178, v210\n"
"v_mfma_f32_16x16x32_bf16 v[184:187], v[56:59], v[156:159], v[184:187]\n"
"v_exp_f32 v180, v180\n"
"v_add_f32 v210, v179, v210\n"
"v_cvt_pk_bf16_f32 v201, v178, v179\n"
"v_mfma_f32_16x16x32_bf16 v[188:191], v[60:63], v[156:159], v[188:191]\n"
"v_exp_f32 v181, v181\n"
"v_add_f32 v210, v180, v210\n"
"v_mfma_f32_16x16x32_bf16 v[192:195], v[32:35], v[160:163], 0\n"
"v_exp_f32 v182, v182\n"
"v_add_f32 v210, v181, v210\n"
"v_cvt_pk_bf16_f32 v202, v180, v181\n"
"v_mfma_f32_16x16x32_bf16 v[196:199], v[36:39], v[160:163], 0\n"
"v_exp_f32 v183, v183\n"
"v_add_f32 v210, v182, v210\n"
"v_mfma_f32_16x16x32_bf16 v[192:195], v[40:43], v[164:167], v[192:195]\n"
"v_exp_f32 v184, v184\n"
"v_add_f32 v210, v183, v210\n"
"v_cvt_pk_bf16_f32 v203, v182, v183\n"
"v_mfma_f32_16x16x32_bf16 v[196:199], v[44:47], v[164:167], v[196:199]\n"
"v_exp_f32 v185, v185\n"
"ds_write_b128 v30, v[200:203] offset:2048\n"
"v_add_f32 v211, v184, v211\n"
"v_mfma_f32_16x16x32_bf16 v[192:195], v[48:51], v[168:171], v[192:195]\n"
"v_exp_f32 v186, v186\n"
"v_add_f32 v211, v185, v211\n"
"v_cvt_pk_bf16_f32 v204, v184, v185\n"
"v_mfma_f32_16x16x32_bf16 v[196:199], v[52:55], v[168:171], v[196:199]\n"
"v_exp_f32 v187, v187\n"
"v_add_f32 v211, v186, v211\n"
"v_mfma_f32_16x16x32_bf16 v[192:195], v[56:59], v[172:175], v[192:195]\n"
"v_exp_f32 v188, v188\n"
"v_add_f32 v211, v187, v211\n"
"v_cvt_pk_bf16_f32 v205, v186, v187\n"
"v_mfma_f32_16x16x32_bf16 v[196:199], v[60:63], v[172:175], v[196:199]\n"
"v_exp_f32 v189, v189\n"
"v_add_f32 v211, v188, v211\n"
"s_waitcnt lgkmcnt(10)\n"
"v_mfma_f32_16x16x32_bf16 v[176:179], v[64:67], v[96:99], 0\n"
"v_exp_f32 v190, v190\n"
"v_add_f32 v211, v189, v211\n"
"v_cvt_pk_bf16_f32 v206, v188, v189\n"
"s_waitcnt lgkmcnt(9)\n"
"v_mfma_f32_16x16x32_bf16 v[180:183], v[68:71], v[96:99], 0\n"
"v_exp_f32 v191, v191\n"
"v_add_f32 v211, v190, v211\n"
"s_waitcnt lgkmcnt(8)\n"
"v_mfma_f32_16x16x32_bf16 v[176:179], v[72:75], v[100:103], v[176:179]\n"
"v_exp_f32 v192, v192\n"
"v_add_f32 v211, v191, v211\n"
"v_cvt_pk_bf16_f32 v207, v190, v191\n"
"s_waitcnt lgkmcnt(6)\n"
"v_mfma_f32_16x16x32_bf16 v[180:183], v[76:79], v[100:103], v[180:183]\n"
"v_exp_f32 v193, v193\n"
"ds_write_b128 v30, v[204:207] offset:3072\n"
"v_add_f32 v212, v192, v212\n"
"s_waitcnt lgkmcnt(6)\n"
"v_mfma_f32_16x16x32_bf16 v[176:179], v[80:83], v[104:107], v[176:179]\n"
"v_exp_f32 v194, v194\n"
"v_add_f32 v212, v193, v212\n"
"v_cvt_pk_bf16_f32 v200, v192, v193\n"
"s_waitcnt lgkmcnt(5)\n"
"v_mfma_f32_16x16x32_bf16 v[180:183], v[84:87], v[104:107], v[180:183]\n"
"v_exp_f32 v195, v195\n"
"v_add_f32 v212, v194, v212\n"
"s_waitcnt lgkmcnt(4)\n"
"v_mfma_f32_16x16x32_bf16 v[176:179], v[88:91], v[108:111], v[176:179]\n"
"v_exp_f32 v196, v196\n"
"v_add_f32 v212, v195, v212\n"
"v_cvt_pk_bf16_f32 v201, v194, v195\n"
"s_waitcnt lgkmcnt(2)\n"
"v_mfma_f32_16x16x32_bf16 v[180:183], v[92:95], v[108:111], v[180:183]\n"
"v_exp_f32 v197, v197\n"
"v_add_f32 v212, v196, v212\n"
"v_mfma_f32_16x16x32_bf16 v[184:187], v[64:67], v[112:115], 0\n"
"v_exp_f32 v198, v198\n"
"v_add_f32 v212, v197, v212\n"
"v_cvt_pk_bf16_f32 v202, v196, v197\n"
"v_mfma_f32_16x16x32_bf16 v[188:191], v[68:71], v[112:115], 0\n"
"v_exp_f32 v199, v199\n"
"v_add_f32 v212, v198, v212\n"
"v_mfma_f32_16x16x32_bf16 v[184:187], v[72:75], v[116:119], v[184:187]\n"
"v_exp_f32 v176, v176\n"
"v_add_f32 v212, v199, v212\n"
"v_cvt_pk_bf16_f32 v203, v198, v199\n"
"s_waitcnt vmcnt(3)\n"
"v_cmp_eq_u32 vcc, s70, v248\n"
"s_mov_b32 s73, 0xfff\n"
"s_mov_b32 s68, vcc_lo\n"
"s_mov_b32 s71, 0\n"
".Lk_p_60_%=:\n"
"s_and_b32 s69, s68, s73\n"
"s_cmp_eq_u32 s69, s73\n"
"s_cbranch_scc1 .Lk_pd_61_%=\n"
"global_load_dword v248, v249, s[52:53] offset:0 sc1\n"
"s_waitcnt vmcnt(0)\n"
"v_cmp_eq_u32 vcc, s70, v248\n"
"s_add_u32 s71, s71, 1\n"
"s_mov_b32 s68, vcc_lo\n"
"s_cmp_lt_u32 s71, 400000\n"
"s_cbranch_scc1 .Lk_p_60_%=\n"
".Lk_pd_61_%=:\n"
"global_load_dword v248, v250, s[52:53] offset:256 sc1\n"
"v_mfma_f32_16x16x32_bf16 v[188:191], v[76:79], v[116:119], v[188:191]\n"
"v_exp_f32 v177, v177\n"
"ds_write_b128 v30, v[200:203] offset:4096\n"
"v_add_f32 v208, v176, v208\n"
"v_mfma_f32_16x16x32_bf16 v[184:187], v[80:83], v[120:123], v[184:187]\n"
"v_exp_f32 v178, v178\n"
"v_add_f32 v208, v177, v208\n"
"v_cvt_pk_bf16_f32 v204, v176, v177\n"
"v_mfma_f32_16x16x32_bf16 v[188:191], v[84:87], v[120:123], v[188:191]\n"
"v_exp_f32 v179, v179\n"
"s_waitcnt lgkmcnt(0)\n"
"s_barrier\n"
"v_add_f32 v208, v178, v208\n"
"v_mfma_f32_16x16x32_bf16 v[184:187], v[88:91], v[124:127], v[184:187]\n"
"v_exp_f32 v180, v180\n"
"ds_read_b128 v[32:35], v14 offset:0\n"
"v_add_f32 v208, v179, v208\n"
"v_cvt_pk_bf16_f32 v205, v178, v179\n"
"v_mfma_f32_16x16x32_bf16 v[188:191], v[92:95], v[124:127], v[188:191]\n"
"v_exp_f32 v181, v181\n"
"v_add_f32 v208, v180, v208\n"
"v_mfma_f32_16x16x32_bf16 v[192:195], v[64:67], v[128:131], 0\n"
"v_exp_f32 v182, v182\n"
"ds_read_b128 v[36:39], v14 offset:4096\n"
"v_add_f32 v208, v181, v208\n"
"v_cvt_pk_bf16_f32 v206, v180, v181\n"
"v_mfma_f32_16x16x32_bf16 v[196:199], v[68:71], v[128:131], 0\n"
"v_exp_f32 v183, v183\n"
"v_add_f32 v208, v182, v208\n"
"v_mfma_f32_16x16x32_bf16 v[192:195], v[72:75], v[132:135], v[192:195]\n"
"v_exp_f32 v184, v184\n"
"ds_read_b128 v[40:43], v15 offset:0\n"
"v_add_f32 v208, v183, v208\n"
"v_cvt_pk_bf16_f32 v207, v182, v183\n"
"v_mfma_f32_16x16x32_bf16 v[196:199], v[76:79], v[132:135], v[196:199]\n"
"v_exp_f32 v185, v185\n"
"ds_write_b128 v30, v[204:207] offset:40960\n"
"v_add_f32 v209, v184, v209\n"
"v_mfma_f32_16x16x32_bf16 v[192:195], v[80:83], v[136:139], v[192:195]\n"
"v_exp_f32 v186, v186\n"
"ds_read_b128 v[44:47], v15 offset:4096\n"
"v_add_f32 v209, v185, v209\n"
"v_cvt_pk_bf16_f32 v200, v184, v185\n"
"v_mfma_f32_16x16x32_bf16 v[196:199], v[84:87], v[136:139], v[196:199]\n"
"v_exp_f32 v187, v187\n"
"v_add_f32 v209, v186, v209\n"
"v_mfma_f32_16x16x32_bf16 v[192:195], v[88:91], v[140:143], v[192:195]\n"
"v_exp_f32 v188, v188\n"
"ds_read_b128 v[48:51], v16 offset:0\n"
"v_add_f32 v209, v187, v209\n"
"v_cvt_pk_bf16_f32 v201, v186, v187\n"
"v_mfma_f32_16x16x32_bf16 v[196:199], v[92:95], v[140:143], v[196:199]\n"
"v_exp_f32 v189, v189\n"
"v_add_f32 v209, v188, v209\n"
"v_mfma_f32_16x16x32_bf16 v[176:179], v[64:67], v[144:147], 0\n"
"v_exp_f32 v190, v190\n"
"ds_read_b128 v[52:55], v16 offset:4096\n"
"v_add_f32 v209, v189, v209\n"
"v_cvt_pk_bf16_f32 v202, v188, v189\n"
"v_mfma_f32_16x16x32_bf16 v[180:183], v[68:71], v[144:147], 0\n"
"v_exp_f32 v191, v191\n"
"v_add_f32 v209, v190, v209\n"
"v_mfma_f32_16x16x32_bf16 v[176:179], v[72:75], v[148:151], v[176:179]\n"
"v_exp_f32 v192, v192\n"
"ds_read_b128 v[56:59], v17 offset:0\n"
"v_add_f32 v209, v191, v209\n"
"v_cvt_pk_bf16_f32 v203, v190, v191\n"
"v_mfma_f32_16x16x32_bf16 v[180:183], v[76:79], v[148:151], v[180:183]\n"
"v_exp_f32 v193, v193\n"
"ds_write_b128 v30, v[200:203] offset:41984\n"
"v_add_f32 v210, v192, v210\n"
"v_mfma_f32_16x16x32_bf16 v[176:179], v[80:83], v[152:155], v[176:179]\n"
"v_exp_f32 v194, v194\n"
"ds_read_b128 v[60:63], v17 offset:4096\n"
"v_add_f32 v210, v193, v210\n"
"v_cvt_pk_bf16_f32 v204, v192, v193\n"
"v_mfma_f32_16x16x32_bf16 v[180:183], v[84:87], v[152:155], v[180:183]\n"
"v_exp_f32 v195, v195\n"
"v_add_f32 v210, v194, v210\n"
"v_mfma_f32_16x16x32_bf16 v[176:179], v[88:91], v[156:159], v[176:179]\n"
"v_exp_f32 v196, v196\n"
"v_add_f32 v210, v195, v210\n"
"v_cvt_pk_bf16_f32 v205, v194, v195\n"
"v_mfma_f32_16x16x32_bf16 v[180:183], v[92:95], v[156:159], v[180:183]\n"
"v_exp_f32 v197, v197\n"
"v_add_f32 v210, v196, v210\n"
"v_mfma_f32_16x16x32_bf16 v[184:187], v[64:67], v[160:163], 0\n"
"v_exp_f32 v198, v198\n"
"v_add_f32 v210, v197, v210\n"
"v_cvt_pk_bf16_f32 v206, v196, v197\n"
"v_mfma_f32_16x16x32_bf16 v[188:191], v[68:71], v[160:163], 0\n"
"v_exp_f32 v199, v199\n"
"v_add_f32 v210, v198, v210\n"
"v_mfma_f32_16x16x32_bf16 v[184:187], v[72:75], v[164:167], v[184:187]\n"
"v_exp_f32 v176, v176\n"
"v_add_f32 v210, v199, v210\n"
"v_cvt_pk_bf16_f32 v207, v198, v199\n"
"v_mfma_f32_16x16x32_bf16 v[188:191], v[76:79], v[164:167], v[188:191]\n"
"v_exp_f32 v177, v177\n"
"ds_write_b128 v30, v[204:207] offset:43008\n"
"v_add_f32 v211, v176, v211\n"
"v_mfma_f32_16x16x32_bf16 v[184:187], v[80:83], v[168:171], v[184:187]\n"
"v_exp_f32 v178, v178\n"
"v_add_f32 v211, v177, v211\n"
"v_cvt_pk_bf16_f32 v200, v176, v177\n"
"v_mfma_f32_16x16x32_bf16 v[188:191], v[84:87], v[168:171], v[188:191]\n"
"v_exp_f32 v179, v179\n"
"v_add_f32 v211, v178, v211\n"
"v_mfma_f32_16x16x32_bf16 v[184:187], v[88:91], v[172:175], v[184:187]\n"
"v_exp_f32 v180, v180\n"
"v_add_f32 v211, v179, v211\n"
"v_cvt_pk_bf16_f32 v201, v178, v179\n"
"v_mfma_f32_16x16x32_bf16 v[188:191], v[92:95], v[172:175], v[188:191]\n"
"v_exp_f32 v181, v181\n"
"v_add_f32 v211, v180, v211\n"
"s_waitcnt lgkmcnt(10)\n"
"v_mfma_f32_16x16x32_bf16 v[192:195], v[32:35], v[96:99], 0\n"
"v_exp_f32 v182, v182\n"
"v_add_f32 v211, v181, v211\n"
"v_cvt_pk_bf16_f32 v202, v180, v181\n"
"s_waitcnt lgkmcnt(9)\n"
"v_mfma_f32_16x16x32_bf16 v[196:199], v[36:39], v[96:99], 0\n"
"v_exp_f32 v183, v183\n"
"v_add_f32 v211, v182, v211\n"
"s_waitcnt lgkmcnt(8)\n"
"v_mfma_f32_16x16x32_bf16 v[192:195], v[40:43], v[100:103], v[192:195]\n"
"v_exp_f32 v184, v184\n"
"v_add_f32 v211, v183, v211\n"
"v_cvt_pk_bf16_f32 v203, v182, v183\n"
"s_waitcnt lgkmcnt(6)\n"
"v_mfma_f32_16x16x32_bf16 v[196:199], v[44:47], v[100:103], v[196:199]\n"
"v_exp_f32 v185, v185\n"
"ds_write_b128 v30, v[200:203] offset:44032\n"
"v_add_f32 v212, v184, v212\n"
"s_waitcnt lgkmcnt(6)\n"
"v_mfma_f32_16x16x32_bf16 v[192:195], v[48:51], v[104:107], v[192:195]\n"
"v_exp_f32 v186, v186\n"
"v_add_f32 v212, v185, v212\n"
"v_cvt_pk_bf16_f32 v204, v184, v185\n"
"s_waitcnt lgkmcnt(5)\n"
"v_mfma_f32_16x16x32_bf16 v[196:199], v[52:55], v[104:107], v[196:199]\n"
"v_exp_f32 v187, v187\n"
"v_add_f32 v212, v186, v212\n"
"s_waitcnt lgkmcnt(4)\n"
"v_mfma_f32_16x16x32_bf16 v[192:195], v[56:59], v[108:111], v[192:195]\n"
"v_exp_f32 v188, v188\n"
"v_add_f32 v212, v187, v212\n"
"v_cvt_pk_bf16_f32 v205, v186, v187\n"
"s_waitcnt lgkmcnt(2)\n"
"v_mfma_f32_16x16x32_bf16 v[196:199], v[60:63], v[108:111], v[196:199]\n"
"v_exp_f32 v189, v189\n"
"v_add_f32 v212, v188, v212\n"
"v_mfma_f32_16x16x32_bf16 v[176:179], v[32:35], v[112:115], 0\n"
"v_exp_f32 v190, v190\n"
"v_add_f32 v212, v189, v212\n"
"v_cvt_pk_bf16_f32 v206, v188, v189\n"
"v_mfma_f32_16x16x32_bf16 v[180:183], v[36:39], v[112:115], 0\n"
"v_exp_f32 v191, v191\n"
"v_add_f32 v212, v190, v212\n"
"v_mfma_f32_16x16x32_bf16 v[176:179], v[40:43], v[116:119], v[176:179]\n"
"v_exp_f32 v192, v192\n"
"v_add_f32 v212, v191, v212\n"
"v_cvt_pk_bf16_f32 v207, v190, v191\n"
"v_mfma_f32_16x16x32_bf16 v[180:183], v[44:47], v[116:119], v[180:183]\n"
"v_exp_f32 v193, v193\n"
"ds_write_b128 v30, v[204:207] offset:45056\n"
"v_add_f32 v208, v192, v208\n"
"v_mfma_f32_16x16x32_bf16 v[176:179], v[48:51], v[120:123], v[176:179]\n"
"v_exp_f32 v194, v194\n"
"v_add_f32 v208, v193, v208\n"
"v_cvt_pk_bf16_f32 v200, v192, v193\n"
"v_mfma_f32_16x16x32_bf16 v[180:183], v[52:55], v[120:123], v[180:183]\n"
"v_exp_f32 v195, v195\n"
"s_waitcnt lgkmcnt(0)\n"
"s_barrier\n"
"v_add_f32 v208, v194, v208\n"
"v_mfma_f32_16x16x32_bf16 v[176:179], v[56:59], v[124:127], v[176:179]\n"
"v_exp_f32 v196, v196\n"
"ds_read_b128 v[64:67], v10 offset:20480\n"
"v_add_f32 v208, v195, v208\n"
"v_cvt_pk_bf16_f32 v201, v194, v195\n"
"v_mfma_f32_16x16x32_bf16 v[180:183], v[60:63], v[124:127], v[180:183]\n"
"v_exp_f32 v197, v197\n"
"v_add_f32 v208, v196, v208\n"
"v_mfma_f32_16x16x32_bf16 v[184:187], v[32:35], v[128:131], 0\n"
"v_exp_f32 v198, v198\n"
"ds_read_b128 v[68:71], v10 offset:24576\n"
"v_add_f32 v208, v197, v208\n"
"v_cvt_pk_bf16_f32 v202, v196, v197\n"
"v_mfma_f32_16x16x32_bf16 v[188:191], v[36:39], v[128:131], 0\n"
"v_exp_f32 v199, v199\n"
"v_add_f32 v208, v198, v208\n"
"v_mfma_f32_16x16x32_bf16 v[184:187], v[40:43], v[132:135], v[184:187]\n"
"v_exp_f32 v176, v176\n"
"ds_read_b128 v[72:75], v11 offset:20480\n"
"v_add_f32 v208, v199, v208\n"
"v_cvt_pk_bf16_f32 v203, v198, v199\n"
"v_mfma_f32_16x16x32_bf16 v[188:191], v[44:47], v[132:135], v[188:191]\n"
"v_exp_f32 v177, v177\n"
"ds_write_b128 v30, v[200:203] offset:0\n"
"v_add_f32 v209, v176, v209\n"
"v_mfma_f32_16x16x32_bf16 v[184:187], v[48:51], v[136:139], v[184:187]\n"
"v_exp_f32 v178, v178\n"
"ds_read_b128 v[76:79], v11 offset:24576\n"
"v_add_f32 v209, v177, v209\n"
"v_cvt_pk_bf16_f32 v204, v176, v177\n"
"v_mfma_f32_16x16x32_bf16 v[188:191], v[52:55], v[136:139], v[188:191]\n"
"v_exp_f32 v179, v179\n"
"v_add_f32 v209, v178, v209\n"
"v_mfma_f32_16x16x32_bf16 v[184:187], v[56:59], v[140:143], v[184:187]\n"
"v_exp_f32 v180, v180\n"
"ds_read_b128 v[80:83], v12 offset:20480\n"
"v_add_f32 v209, v179, v209\n"
"v_cvt_pk_bf16_f32 v205, v178, v179\n"
"v_mfma_f32_16x16x32_bf16 v[188:191], v[60:63], v[140:143], v[188:191]\n"
"v_exp_f32 v181, v181\n"
"v_add_f32 v209, v180, v209\n"
"v_mfma_f32_16x16x32_bf16 v[192:195], v[32:35], v[144:147], 0\n"
"v_exp_f32 v182, v182\n"
"ds_read_b128 v[84:87], v12 offset:24576\n"
"v_add_f32 v209, v181, v209\n"
"v_cvt_pk_bf16_f32 v206, v180, v181\n"
"v_mfma_f32_16x16x32_bf16 v[196:199], v[36:39], v[144:147], 0\n"
"v_exp_f32 v183, v183\n"
"v_add_f32 v209, v182, v209\n"
"v_mfma_f32_16x16x32_bf16 v[192:195], v[40:43], v[148:151], v[192:195]\n"
"v_exp_f32 v184, v184\n"
"ds_read_b128 v[88:91], v13 offset:20480\n"
"v_add_f32 v209, v183, v209\n"
"v_cvt_pk_bf16_f32 v207, v182, v183\n"
"v_mfma_f32_16x16x32_bf16 v[196:199], v[44:47], v[148:151], v[196:199]\n"
"v_exp_f32 v185, v185\n"
"ds_write_b128 v30, v[204:207] offset:1024\n"
"v_add_f32 v210, v184, v210\n"
"v_mfma_f32_16x16x32_bf16 v[192:195], v[48:51], v[152:155], v[192:195]\n"
"v_exp_f32 v186, v186\n"
"ds_read_b128 v[92:95], v13 offset:24576\n"
"v_add_f32 v210, v185, v210\n"
"v_cvt_pk_bf16_f32 v200, v184, v185\n"
"v_mfma_f32_16x16x32_bf16 v[196:199], v[52:55], v[152:155], v[196:199]\n"
"v_exp_f32 v187, v187\n"
"v_add_f32 v210, v186, v210\n"
"v_mfma_f32_16x16x32_bf16 v[192:195], v[56:59], v[156:159], v[192:195]\n"
"v_exp_f32 v188, v188\n"
"v_add_f32 v210, v187, v210\n"
"v_cvt_pk_bf16_f32 v201, v186, v187\n"
"v_mfma_f32_16x16x32_bf16 v[196:199], v[60:63], v[156:159], v[196:199]\n"
"v_exp_f32 v189, v189\n"
"v_add_f32 v210, v188, v210\n"
"v_mfma_f32_16x16x32_bf16 v[176:179], v[32:35], v[160:163], 0\n"
"v_exp_f32 v190, v190\n"
"v_add_f32 v210, v189, v210\n"
"v_cvt_pk_bf16_f32 v202, v188, v189\n"
"v_mfma_f32_16x16x32_bf16 v[180:183], v[36:39], v[160:163], 0\n"
"v_exp_f32 v191, v191\n"
"v_add_f32 v210, v190, v210\n"
"v_mfma_f32_16x16x32_bf16 v[176:179], v[40:43], v[164:167], v[176:179]\n"
"v_exp_f32 v192, v192\n"
"v_add_f32 v210, v191, v210\n"
"v_cvt_pk_bf16_f32 v203, v190, v191\n"
"v_mfma_f32_16x16x32_bf16 v[180:183], v[44:47], v[164:167], v[180:183]\n"
"v_exp_f32 v193, v193\n"
"ds_write_b128 v30, v[200:203] offset:2048\n"
"v_add_f32 v211, v192, v211\n"
"v_mfma_f32_16x16x32_bf16 v[176:179], v[48:51], v[168:171], v[176:179]\n"
"v_exp_f32 v194, v194\n"
"v_add_f32 v211, v193, v211\n"
"v_cvt_pk_bf16_f32 v204, v192, v193\n"
"v_mfma_f32_16x16x32_bf16 v[180:183], v[52:55], v[168:171], v[180:183]\n"
"v_exp_f32 v195, v195\n"
"v_add_f32 v211, v194, v211\n"
"v_mfma_f32_16x16x32_bf16 v[176:179], v[56:59], v[172:175], v[176:179]\n"
"v_exp_f32 v196, v196\n"
"v_add_f32 v211, v195, v211\n"
"v_cvt_pk_bf16_f32 v205, v194, v195\n"
"v_mfma_f32_16x16x32_bf16 v[180:183], v[60:63], v[172:175], v[180:183]\n"
"v_exp_f32 v197, v197\n"
"v_add_f32 v211, v196, v211\n"
"s_waitcnt lgkmcnt(10)\n"
"v_mfma_f32_16x16x32_bf16 v[184:187], v[64:67], v[96:99], 0\n"
"v_exp_f32 v198, v198\n"
"v_add_f32 v211, v197, v211\n"
"v_cvt_pk_bf16_f32 v206, v196, v197\n"
"s_mov_b64 exec, 0xfff\n"
"s_waitcnt vmcnt(0)\n"
"global_store_dword v251, v248, s[52:53] offset:144 sc1\n"
"s_mov_b64 exec, -1\n"
"s_waitcnt lgkmcnt(9)\n"
"v_mfma_f32_16x16x32_bf16 v[188:191], v[68:71], v[96:99], 0\n"
"v_exp_f32 v199, v199\n"
"v_add_f32 v211, v198, v211\n"
"s_waitcnt lgkmcnt(8)\n"
"v_mfma_f32_16x16x32_bf16 v[184:187], v[72:75], v[100:103], v[184:187]\n"
"v_exp_f32 v176, v176\n"
"v_add_f32 v211, v199, v211\n"
"v_cvt_pk_bf16_f32 v207, v198, v199\n"
"s_waitcnt lgkmcnt(6)\n"
"v_mfma_f32_16x16x32_bf16 v[188:191], v[76:79], v[100:103], v[188:191]\n"
"v_exp_f32 v177, v177\n"
"ds_write_b128 v30, v[204:207] offset:3072\n"
"v_add_f32 v212, v176, v212\n"
"s_waitcnt lgkmcnt(6)\n"
"v_mfma_f32_16x16x32_bf16 v[184:187], v[80:83], v[104:107], v[184:187]\n"
"v_exp_f32 v178, v178\n"
"v_add_f32 v212, v177, v212\n"
"v_cvt_pk_bf16_f32 v200, v176, v177\n"
"s_waitcnt lgkmcnt(5)\n"
"v_mfma_f32_16x16x32_bf16 v[188:191], v[84:87], v[104:107], v[188:191]\n"
"v_exp_f32 v179, v179\n"
"v_add_f32 v212, v178, v212\n"
"s_waitcnt lgkmcnt(4)\n"
"v_mfma_f32_16x16x32_bf16 v[184:187], v[88:91], v[108:111], v[184:187]\n"
"v_exp_f32 v180, v180\n"
"v_add_f32 v212, v179, v212\n"
"v_cvt_pk_bf16_f32 v201, v178, v179\n"
"s_waitcnt lgkmcnt(2)\n"
"v_mfma_f32_16x16x32_bf16 v[188:191], v[92:95], v[108:111], v[188:191]\n"
"v_exp_f32 v181, v181\n"
"v_add_f32 v212, v180, v212\n"
"v_mfma_f32_16x16x32_bf16 v[192:195], v[64:67], v[112:115], 0\n"
"v_exp_f32 v182, v182\n"
"v_add_f32 v212, v181, v212\n"
"v_cvt_pk_bf16_f32 v202, v180, v181\n"
"v_mfma_f32_16x16x32_bf16 v[196:199], v[68:71], v[112:115], 0\n"
"v_exp_f32 v183, v183\n"
"v_add_f32 v212, v182, v212\n"
"v_mfma_f32_16x16x32_bf16 v[192:195], v[72:75], v[116:119], v[192:195]\n"
"v_exp_f32 v184, v184\n"
"v_add_f32 v212, v183, v212\n"
"v_cvt_pk_bf16_f32 v203, v182, v183\n"
"v_mfma_f32_16x16x32_bf16 v[196:199], v[76:79], v[116:119], v[196:199]\n"
"v_exp_f32 v185, v185\n"
"ds_write_b128 v30, v[200:203] offset:4096\n"
"v_add_f32 v208, v184, v208\n"
"v_mfma_f32_16x16x32_bf16 v[192:195], v[80:83], v[120:123], v[192:195]\n"
"v_exp_f32 v186, v186\n"
"v_add_f32 v208, v185, v208\n"
"v_cvt_pk_bf16_f32 v204, v184, v185\n"
"v_mfma_f32_16x16x32_bf16 v[196:199], v[84:87], v[120:123], v[196:199]\n"
"v_exp_f32 v187, v187\n"
"s_waitcnt lgkmcnt(0)\n"
"s_barrier\n"
"v_add_f32 v208, v186, v208\n"
"v_mfma_f32_16x16x32_bf16 v[192:195], v[88:91], v[124:127], v[192:195]\n"
"v_exp_f32 v188, v188\n"
"ds_read_b128 v[32:35], v18 offset:16384\n"
"v_add_f32 v208, v187, v208\n"
"v_cvt_pk_bf16_f32 v205, v186, v187\n"
"v_mfma_f32_16x16x32_bf16 v[196:199], v[92:95], v[124:127], v[196:199]\n"
"v_exp_f32 v189, v189\n"
"v_add_f32 v208, v188, v208\n"
"v_mfma_f32_16x16x32_bf16 v[176:179], v[64:67], v[128:131], 0\n"
"v_exp_f32 v190, v190\n"
"ds_read_b128 v[36:39], v18 offset:20480\n"
"v_add_f32 v208, v189, v208\n"
"v_cvt_pk_bf16_f32 v206, v188, v189\n"
"v_mfma_f32_16x16x32_bf16 v[180:183], v[68:71], v[128:131], 0\n"
"v_exp_f32 v191, v191\n"
"v_add_f32 v208, v190, v208\n"
"v_mfma_f32_16x16x32_bf16 v[176:179], v[72:75], v[132:135], v[176:179]\n"
"v_exp_f32 v192, v192\n"
"ds_read_b128 v[40:43], v19 offset:16384\n"
"v_add_f32 v208, v191, v208\n"
"v_cvt_pk_bf16_f32 v207, v190, v191\n"
"v_mfma_f32_16x16x32_bf16 v[180:183], v[76:79], v[132:135], v[180:183]\n"
"v_exp_f32 v193, v193\n"
"ds_write_b128 v30, v[204:207] offset:40960\n"
"v_add_f32 v209, v192, v209\n"
"v_mfma_f32_16x16x32_bf16 v[176:179], v[80:83], v[136:139], v[176:179]\n"
"v_exp_f32 v194, v194\n"
"ds_read_b128 v[44:47], v19 offset:20480\n"
"v_add_f32 v209, v193, v209\n"
"v_cvt_pk_bf16_f32 v200, v192, v193\n"
"v_mfma_f32_16x16x32_bf16 v[180:183], v[84:87], v[136:139], v[180:183]\n"
"v_exp_f32 v195, v195\n"
"v_add_f32 v209, v194, v209\n"
"v_mfma_f32_16x16x32_bf16 v[176:179], v[88:91], v[140:143], v[176:179]\n"
"v_exp_f32 v196, v196\n"
"ds_read_b128 v[48:51], v20 offset:16384\n"
"v_add_f32 v209, v195, v209\n"
"v_cvt_pk_bf16_f32 v201, v194, v195\n"
"v_mfma_f32_16x16x32_bf16 v[180:183], v[92:95], v[140:143], v[180:183]\n"
"v_exp_f32 v197, v197\n"
"v_add_f32 v209, v196, v209\n"
"v_mfma_f32_16x16x32_bf16 v[184:187], v[64:67], v[144:147], 0\n"
"v_exp_f32 v198, v198\n"
"ds_read_b128 v[52:55], v20 offset:20480\n"
"v_add_f32 v209, v197, v209\n"
"v_cvt_pk_bf16_f32 v202, v196, v197\n"
"v_mfma_f32_16x16x32_bf16 v[188:191], v[68:71], v[144:147], 0\n"
"v_exp_f32 v199, v199\n"
"v_add_f32 v209, v198, v209\n"
"v_mfma_f32_16x16x32_bf16 v[184:187], v[72:75], v[148:151], v[184:187]\n"
"v_exp_f32 v176, v176\n"
"ds_read_b128 v[56:59], v21 offset:16384\n"
"v_add_f32 v209, v199, v209\n"
"v_cvt_pk_bf16_f32 v203, v198, v199\n"
"v_mfma_f32_16x16x32_bf16 v[188:191], v[76:79], v[148:151], v[188:191]\n"
"v_exp_f32 v177, v177\n"
"ds_write_b128 v30, v[200:203] offset:41984\n"
"v_add_f32 v210, v176, v210\n"
"v_mfma_f32_16x16x32_bf16 v[184:187], v[80:83], v[152:155], v[184:187]\n"
"v_exp_f32 v178, v178\n"
"ds_read_b128 v[60:63], v21 offset:20480\n"
"v_add_f32 v210, v177, v210\n"
"v_cvt_pk_bf16_f32 v204, v176, v177\n"
"v_mfma_f32_16x16x32_bf16 v[188:191], v[84:87], v[152:155], v[188:191]\n"
"v_exp_f32 v179, v179\n"
"v_add_f32 v210, v178, v210\n"
"v_mfma_f32_16x16x32_bf16 v[184:187], v[88:91], v[156:159], v[184:187]\n"
"v_exp_f32 v180, v180\n"
"v_add_f32 v210, v179, v210\n"
"v_cvt_pk_bf16_f32 v205, v178, v179\n"
"v_mfma_f32_16x16x32_bf16 v[188:191], v[92:95], v[156:159], v[188:191]\n"
"v_exp_f32 v181, v181\n"
"v_add_f32 v210, v180, v210\n"
"v_mfma_f32_16x16x32_bf16 v[192:195], v[64:67], v[160:163], 0\n"
"v_exp_f32 v182, v182\n"
"v_add_f32 v210, v181, v210\n"
"v_cvt_pk_bf16_f32 v206, v180, v181\n"
"v_mfma_f32_16x16x32_bf16 v[196:199], v[68:71], v[160:163], 0\n"
"v_exp_f32 v183, v183\n"
"v_add_f32 v210, v182, v210\n"
"v_mfma_f32_16x16x32_bf16 v[192:195], v[72:75], v[164:167], v[192:195]\n"
"v_exp_f32 v184, v184\n"
"v_add_f32 v210, v183, v210\n"
"v_cvt_pk_bf16_f32 v207, v182, v183\n"
"v_mfma_f32_16x16x32_bf16 v[196:199], v[76:79], v[164:167], v[196:199]\n"
"v_exp_f32 v185, v185\n"
"ds_write_b128 v30, v[204:207] offset:43008\n"
"v_add_f32 v211, v184, v211\n"
"v_mfma_f32_16x16x32_bf16 v[192:195], v[80:83], v[168:171], v[192:195]\n"
"v_exp_f32 v186, v186\n"
"v_add_f32 v211, v185, v211\n"
"v_cvt_pk_bf16_f32 v200, v184, v185\n"
"v_mfma_f32_16x16x32_bf16 v[196:199], v[84:87], v[168:171], v[196:199]\n"
"v_exp_f32 v187, v187\n"
"v_add_f32 v211, v186, v211\n"
"v_mfma_f32_16x16x32_bf16 v[192:195], v[88:91], v[172:175], v[192:195]\n"
"v_exp_f32 v188, v188\n"
"v_add_f32 v211, v187, v211\n"
"v_cvt_pk_bf16_f32 v201, v186, v187\n"
"v_mfma_f32_16x16x32_bf16 v[196:199], v[92:95], v[172:175], v[196:199]\n"
"v_exp_f32 v189, v189\n"
"v_add_f32 v211, v188, v211\n"
"s_waitcnt lgkmcnt(10)\n"
"v_mfma_f32_16x16x32_bf16 v[176:179], v[32:35], v[96:99], 0\n"
"v_exp_f32 v190, v190\n"
"v_add_f32 v211, v189, v211\n"
"v_cvt_pk_bf16_f32 v202, v188, v189\n"
"s_waitcnt lgkmcnt(9)\n"
"v_mfma_f32_16x16x32_bf16 v[180:183], v[36:39], v[96:99], 0\n"
"v_exp_f32 v191, v191\n"
"v_add_f32 v211, v190, v211\n"
"s_waitcnt lgkmcnt(8)\n"
"v_mfma_f32_16x16x32_bf16 v[176:179], v[40:43], v[100:103], v[176:179]\n"
"v_exp_f32 v192, v192\n"
"v_add_f32 v211, v191, v211\n"
"v_cvt_pk_bf16_f32 v203, v190, v191\n"
"s_waitcnt lgkmcnt(6)\n"
"v_mfma_f32_16x16x32_bf16 v[180:183], v[44:47], v[100:103], v[180:183]\n"
"v_exp_f32 v193, v193\n"
"ds_write_b128 v30, v[200:203] offset:44032\n"
"v_add_f32 v212, v192, v212\n"
"s_waitcnt lgkmcnt(6)\n"
"v_mfma_f32_16x16x32_bf16 v[176:179], v[48:51], v[104:107], v[176:179]\n"
"v_exp_f32 v194, v194\n"
"v_add_f32 v212, v193, v212\n"
"v_cvt_pk_bf16_f32 v204, v192, v193\n"
"s_waitcnt lgkmcnt(5)\n"
"v_mfma_f32_16x16x32_bf16 v[180:183], v[52:55], v[104:107], v[180:183]\n"
"v_exp_f32 v195, v195\n"
"v_add_f32 v212, v194, v212\n"
"s_waitcnt lgkmcnt(4)\n"
"v_mfma_f32_16x16x32_bf16 v[176:179], v[56:59], v[108:111], v[176:179]\n"
"v_exp_f32 v196, v196\n"
"v_add_f32 v212, v195, v212\n"
"v_cvt_pk_bf16_f32 v205, v194, v195\n"
"s_waitcnt lgkmcnt(2)\n"
"v_mfma_f32_16x16x32_bf16 v[180:183], v[60:63], v[108:111], v[180:183]\n"
"v_exp_f32 v197, v197\n"
"v_add_f32 v212, v196, v212\n"
"v_mfma_f32_16x16x32_bf16 v[184:187], v[32:35], v[112:115], 0\n"
"v_exp_f32 v198, v198\n"
"v_add_f32 v212, v197, v212\n"
"v_cvt_pk_bf16_f32 v206, v196, v197\n"
"v_mfma_f32_16x16x32_bf16 v[188:191], v[36:39], v[112:115], 0\n"
"v_exp_f32 v199, v199\n"
"v_add_f32 v212, v198, v212\n"
"v_mfma_f32_16x16x32_bf16 v[184:187], v[40:43], v[116:119], v[184:187]\n"
"v_exp_f32 v176, v176\n"
"v_add_f32 v212, v199, v212\n"
"v_cvt_pk_bf16_f32 v207, v198, v199\n"
"v_mfma_f32_16x16x32_bf16 v[188:191], v[44:47], v[116:119], v[188:191]\n"
"v_exp_f32 v177, v177\n"
"ds_write_b128 v30, v[204:207] offset:45056\n"
"v_add_f32 v208, v176, v208\n"
"v_mfma_f32_16x16x32_bf16 v[184:187], v[48:51], v[120:123], v[184:187]\n"
"v_exp_f32 v178, v178\n"
"v_add_f32 v208, v177, v208\n"
"v_cvt_pk_bf16_f32 v200, v176, v177\n"
"v_mfma_f32_16x16x32_bf16 v[188:191], v[52:55], v[120:123], v[188:191]\n"
"v_exp_f32 v179, v179\n"
"s_waitcnt lgkmcnt(0)\n"
"s_barrier\n"
"v_add_f32 v208, v178, v208\n"
"v_mfma_f32_16x16x32_bf16 v[184:187], v[56:59], v[124:127], v[184:187]\n"
"v_exp_f32 v180, v180\n"
"ds_read_b128 v[64:67], v18 offset:0\n"
"v_add_f32 v208, v179, v208\n"
"v_cvt_pk_bf16_f32 v201, v178, v179\n"
"v_mfma_f32_16x16x32_bf16 v[188:191], v[60:63], v[124:127], v[188:191]\n"
"v_exp_f32 v181, v181\n"
"v_add_f32 v208, v180, v208\n"
"v_mfma_f32_16x16x32_bf16 v[192:195], v[32:35], v[128:131], 0\n"
"v_exp_f32 v182, v182\n"
"ds_read_b128 v[68:71], v18 offset:4096\n"
"v_add_f32 v208, v181, v208\n"
"v_cvt_pk_bf16_f32 v202, v180, v181\n"
"v_mfma_f32_16x16x32_bf16 v[196:199], v[36:39], v[128:131], 0\n"
"v_exp_f32 v183, v183\n"
"v_add_f32 v208, v182, v208\n"
"v_mfma_f32_16x16x32_bf16 v[192:195], v[40:43], v[132:135], v[192:195]\n"
"v_exp_f32 v184, v184\n"
"ds_read_b128 v[72:75], v19 offset:0\n"
"v_add_f32 v208, v183, v208\n"
"v_cvt_pk_bf16_f32 v203, v182, v183\n"
"v_mfma_f32_16x16x32_bf16 v[196:199], v[44:47], v[132:135], v[196:199]\n"
"v_exp_f32 v185, v185\n"
"ds_write_b128 v30, v[200:203] offset:0\n"
"v_add_f32 v209, v184, v209\n"
"v_mfma_f32_16x16x32_bf16 v[192:195], v[48:51], v[136:139], v[192:195]\n"
"v_exp_f32 v186, v186\n"
"ds_read_b128 v[76:79], v19 offset:4096\n"
"v_add_f32 v209, v185, v209\n"
"v_cvt_pk_bf16_f32 v204, v184, v185\n"
"v_mfma_f32_16x16x32_bf16 v[196:199], v[52:55], v[136:139], v[196:199]\n"
"v_exp_f32 v187, v187\n"
"v_add_f32 v209, v186, v209\n"
"v_mfma_f32_16x16x32_bf16 v[192:195], v[56:59], v[140:143], v[192:195]\n"
"v_exp_f32 v188, v188\n"
"ds_read_b128 v[80:83], v20 offset:0\n"
"v_add_f32 v209, v187, v209\n"
"v_cvt_pk_bf16_f32 v205, v186, v187\n"
"v_mfma_f32_16x16x32_bf16 v[196:199], v[60:63], v[140:143], v[196:199]\n"
"v_exp_f32 v189, v189\n"
"v_add_f32 v209, v188, v209\n"
"v_mfma_f32_16x16x32_bf16 v[176:179], v[32:35], v[144:147], 0\n"
"v_exp_f32 v190, v190\n"
"ds_read_b128 v[84:87], v20 offset:4096\n"
"v_add_f32 v209, v189, v209\n"
"v_cvt_pk_bf16_f32 v206, v188, v189\n"
"v_mfma_f32_16x16x32_bf16 v[180:183], v[36:39], v[144:147], 0\n"
"v_exp_f32 v191, v191\n"
"v_add_f32 v209, v190, v209\n"
"v_mfma_f32_16x16x32_bf16 v[176:179], v[40:43], v[148:151], v[176:179]\n"
"v_exp_f32 v192, v192\n"
"ds_read_b128 v[88:91], v21 offset:0\n"
"v_add_f32 v209, v191, v209\n"
"v_cvt_pk_bf16_f32 v207, v190, v191\n"
"v_mfma_f32_16x16x32_bf16 v[180:183], v[44:47], v[148:151], v[180:183]\n"
"v_exp_f32 v193, v193\n"
"ds_write_b128 v30, v[204:207] offset:1024\n"
"v_add_f32 v210, v192, v210\n"
"v_mfma_f32_16x16x32_bf16 v[176:179], v[48:51], v[152:155], v[176:179]\n"
"v_exp_f32 v194, v194\n"
"ds_read_b128 v[92:95], v21 offset:4096\n"
"v_add_f32 v210, v193, v210\n"
"v_cvt_pk_bf16_f32 v200, v192, v193\n"
"v_mfma_f32_16x16x32_bf16 v[180:183], v[52:55], v[152:155], v[180:183]\n"
"v_exp_f32 v195, v195\n"
"v_add_f32 v210, v194, v210\n"
"v_mfma_f32_16x16x32_bf16 v[176:179], v[56:59], v[156:159], v[176:179]\n"
"v_exp_f32 v196, v196\n"
"v_add_f32 v210, v195, v210\n"
"v_cvt_pk_bf16_f32 v201, v194, v195\n"
"v_mfma_f32_16x16x32_bf16 v[180:183], v[60:63], v[156:159], v[180:183]\n"
"v_exp_f32 v197, v197\n"
"v_add_f32 v210, v196, v210\n"
"v_mfma_f32_16x16x32_bf16 v[184:187], v[32:35], v[160:163], 0\n"
"v_exp_f32 v198, v198\n"
"v_add_f32 v210, v197, v210\n"
"v_cvt_pk_bf16_f32 v202, v196, v197\n"
"v_mfma_f32_16x16x32_bf16 v[188:191], v[36:39], v[160:163], 0\n"
"v_exp_f32 v199, v199\n"
"v_add_f32 v210, v198, v210\n"
"v_mfma_f32_16x16x32_bf16 v[184:187], v[40:43], v[164:167], v[184:187]\n"
"v_exp_f32 v176, v176\n"
"v_add_f32 v210, v199, v210\n"
"v_cvt_pk_bf16_f32 v203, v198, v199\n"
"v_mfma_f32_16x16x32_bf16 v[188:191], v[44:47], v[164:167], v[188:191]\n"
"v_exp_f32 v177, v177\n"
"ds_write_b128 v30, v[200:203] offset:2048\n"
"v_add_f32 v211, v176, v211\n"
"v_mfma_f32_16x16x32_bf16 v[184:187], v[48:51], v[168:171], v[184:187]\n"
"v_exp_f32 v178, v178\n"
"v_add_f32 v211, v177, v211\n"
"v_cvt_pk_bf16_f32 v204, v176, v177\n"
"v_mfma_f32_16x16x32_bf16 v[188:191], v[52:55], v[168:171], v[188:191]\n"
"v_exp_f32 v179, v179\n"
"v_add_f32 v211, v178, v211\n"
"v_mfma_f32_16x16x32_bf16 v[184:187], v[56:59], v[172:175], v[184:187]\n"
"v_exp_f32 v180, v180\n"
"v_add_f32 v211, v179, v211\n"
"v_cvt_pk_bf16_f32 v205, v178, v179\n"
"v_mfma_f32_16x16x32_bf16 v[188:191], v[60:63], v[172:175], v[188:191]\n"
"v_exp_f32 v181, v181\n"
"v_add_f32 v211, v180, v211\n"
"s_waitcnt lgkmcnt(10)\n"
"v_mfma_f32_16x16x32_bf16 v[192:195], v[64:67], v[96:99], 0\n"
"v_exp_f32 v182, v182\n"
"v_add_f32 v211, v181, v211\n"
"v_cvt_pk_bf16_f32 v206, v180, v181\n"
"s_waitcnt lgkmcnt(9)\n"
"v_mfma_f32_16x16x32_bf16 v[196:199], v[68:71], v[96:99], 0\n"
"v_exp_f32 v183, v183\n"
"v_add_f32 v211, v182, v211\n"
"s_waitcnt lgkmcnt(8)\n"
"v_mfma_f32_16x16x32_bf16 v[192:195], v[72:75], v[100:103], v[192:195]\n"
"v_exp_f32 v184, v184\n"
"v_add_f32 v211, v183, v211\n"
"v_cvt_pk_bf16_f32 v207, v182, v183\n"
"s_waitcnt lgkmcnt(6)\n"
"v_mfma_f32_16x16x32_bf16 v[196:199], v[76:79], v[100:103], v[196:199]\n"
"v_exp_f32 v185, v185\n"
"ds_write_b128 v30, v[204:207] offset:3072\n"
"v_add_f32 v212, v184, v212\n"
"s_waitcnt lgkmcnt(6)\n"
"v_mfma_f32_16x16x32_bf16 v[192:195], v[80:83], v[104:107], v[192:195]\n"
"v_exp_f32 v186, v186\n"
"v_add_f32 v212, v185, v212\n"
"v_cvt_pk_bf16_f32 v200, v184, v185\n"
"s_waitcnt lgkmcnt(5)\n"
"v_mfma_f32_16x16x32_bf16 v[196:199], v[84:87], v[104:107], v[196:199]\n"
"v_exp_f32 v187, v187\n"
"v_add_f32 v212, v186, v212\n"
"s_waitcnt lgkmcnt(4)\n"
"v_mfma_f32_16x16x32_bf16 v[192:195], v[88:91], v[108:111], v[192:195]\n"
"v_exp_f32 v188, v188\n"
"v_add_f32 v212, v187, v212\n"
"v_cvt_pk_bf16_f32 v201, v186, v187\n"
"s_waitcnt lgkmcnt(2)\n"
"v_mfma_f32_16x16x32_bf16 v[196:199], v[92:95], v[108:111], v[196:199]\n"
"v_exp_f32 v189, v189\n"
"v_add_f32 v212, v188, v212\n"
"v_mfma_f32_16x16x32_bf16 v[176:179], v[64:67], v[112:115], 0\n"
"v_exp_f32 v190, v190\n"
"v_add_f32 v212, v189, v212\n"
"v_cvt_pk_bf16_f32 v202, v188, v189\n"
"v_mfma_f32_16x16x32_bf16 v[180:183], v[68:71], v[112:115], 0\n"
"v_exp_f32 v191, v191\n"
"v_add_f32 v212, v190, v212\n"
"v_mfma_f32_16x16x32_bf16 v[176:179], v[72:75], v[116:119], v[176:179]\n"
"v_exp_f32 v192, v192\n"
"v_add_f32 v212, v191, v212\n"
"v_cvt_pk_bf16_f32 v203, v190, v191\n"
"v_mfma_f32_16x16x32_bf16 v[180:183], v[76:79], v[116:119], v[180:183]\n"
"v_exp_f32 v193, v193\n"
"ds_write_b128 v30, v[200:203] offset:4096\n"
"v_add_f32 v208, v192, v208\n"
"v_mfma_f32_16x16x32_bf16 v[176:179], v[80:83], v[120:123], v[176:179]\n"
"v_exp_f32 v194, v194\n"
"v_add_f32 v208, v193, v208\n"
"v_cvt_pk_bf16_f32 v204, v192, v193\n"
"v_mfma_f32_16x16x32_bf16 v[180:183], v[84:87], v[120:123], v[180:183]\n"
"v_exp_f32 v195, v195\n"
"s_waitcnt lgkmcnt(0)\n"
"s_barrier\n"
"v_add_f32 v208, v194, v208\n"
"v_mfma_f32_16x16x32_bf16 v[176:179], v[88:91], v[124:127], v[176:179]\n"
"v_exp_f32 v196, v196\n"
"v_add_f32 v208, v195, v208\n"
"v_cvt_pk_bf16_f32 v205, v194, v195\n"
"v_mfma_f32_16x16x32_bf16 v[180:183], v[92:95], v[124:127], v[180:183]\n"
"v_exp_f32 v197, v197\n"
"v_add_f32 v208, v196, v208\n"
"v_mfma_f32_16x16x32_bf16 v[184:187], v[64:67], v[128:131], 0\n"
"v_exp_f32 v198, v198\n"
"v_add_f32 v208, v197, v208\n"
"v_cvt_pk_bf16_f32 v206, v196, v197\n"
"v_mfma_f32_16x16x32_bf16 v[188:191], v[68:71], v[128:131], 0\n"
"v_exp_f32 v199, v199\n"
"v_add_f32 v208, v198, v208\n"
"v_mfma_f32_16x16x32_bf16 v[184:187], v[72:75], v[132:135], v[184:187]\n"
"v_exp_f32 v176, v176\n"
"v_add_f32 v208, v199, v208\n"
"v_cvt_pk_bf16_f32 v207, v198, v199\n"
"v_mfma_f32_16x16x32_bf16 v[188:191], v[76:79], v[132:135], v[188:191]\n"
"v_exp_f32 v177, v177\n"
"ds_write_b128 v30, v[204:207] offset:40960\n"
"v_add_f32 v209, v176, v209\n"
"v_mfma_f32_16x16x32_bf16 v[184:187], v[80:83], v[136:139], v[184:187]\n"
"v_exp_f32 v178, v178\n"
"v_add_f32 v209, v177, v209\n"
"v_cvt_pk_bf16_f32 v200, v176, v177\n"
"v_mfma_f32_16x16x32_bf16 v[188:191], v[84:87], v[136:139], v[188:191]\n"
"v_exp_f32 v179, v179\n"
"v_add_f32 v209, v178, v209\n"
"v_mfma_f32_16x16x32_bf16 v[184:187], v[88:91], v[140:143], v[184:187]\n"
"v_exp_f32 v180, v180\n"
"v_add_f32 v209, v179, v209\n"
"v_cvt_pk_bf16_f32 v201, v178, v179\n"
"v_mfma_f32_16x16x32_bf16 v[188:191], v[92:95], v[140:143], v[188:191]\n"
"v_exp_f32 v181, v181\n"
"v_add_f32 v209, v180, v209\n"
"v_mfma_f32_16x16x32_bf16 v[192:195], v[64:67], v[144:147], 0\n"
"v_exp_f32 v182, v182\n"
"v_add_f32 v209, v181, v209\n"
"v_cvt_pk_bf16_f32 v202, v180, v181\n"
"v_mfma_f32_16x16x32_bf16 v[196:199], v[68:71], v[144:147], 0\n"
"v_exp_f32 v183, v183\n"
"v_add_f32 v209, v182, v209\n"
"v_mfma_f32_16x16x32_bf16 v[192:195], v[72:75], v[148:151], v[192:195]\n"
"v_exp_f32 v184, v184\n"
"v_add_f32 v209, v183, v209\n"
"v_cvt_pk_bf16_f32 v203, v182, v183\n"
"v_mfma_f32_16x16x32_bf16 v[196:199], v[76:79], v[148:151], v[196:199]\n"
"v_exp_f32 v185, v185\n"
"ds_write_b128 v30, v[200:203] offset:41984\n"
"v_add_f32 v210, v184, v210\n"
"v_mfma_f32_16x16x32_bf16 v[192:195], v[80:83], v[152:155], v[192:195]\n"
"v_exp_f32 v186, v186\n"
"v_add_f32 v210, v185, v210\n"
"v_cvt_pk_bf16_f32 v204, v184, v185\n"
"v_mfma_f32_16x16x32_bf16 v[196:199], v[84:87], v[152:155], v[196:199]\n"
"v_exp_f32 v187, v187\n"
"v_add_f32 v210, v186, v210\n"
"v_mfma_f32_16x16x32_bf16 v[192:195], v[88:91], v[156:159], v[192:195]\n"
"v_exp_f32 v188, v188\n"
"v_add_f32 v210, v187, v210\n"
"v_cvt_pk_bf16_f32 v205, v186, v187\n"
"v_mfma_f32_16x16x32_bf16 v[196:199], v[92:95], v[156:159], v[196:199]\n"
"v_exp_f32 v189, v189\n"
"v_add_f32 v210, v188, v210\n"
"v_mfma_f32_16x16x32_bf16 v[176:179], v[64:67], v[160:163], 0\n"
"v_exp_f32 v190, v190\n"
"v_add_f32 v210, v189, v210\n"
"v_cvt_pk_bf16_f32 v206, v188, v189\n"
"v_mfma_f32_16x16x32_bf16 v[180:183], v[68:71], v[160:163], 0\n"
"v_exp_f32 v191, v191\n"
"v_add_f32 v210, v190, v210\n"
"v_mfma_f32_16x16x32_bf16 v[176:179], v[72:75], v[164:167], v[176:179]\n"
"v_exp_f32 v192, v192\n"
"v_add_f32 v210, v191, v210\n"
"v_cvt_pk_bf16_f32 v207, v190, v191\n"
"v_mfma_f32_16x16x32_bf16 v[180:183], v[76:79], v[164:167], v[180:183]\n"
"v_exp_f32 v193, v193\n"
"ds_write_b128 v30, v[204:207] offset:43008\n"
"v_add_f32 v211, v192, v211\n"
"v_mfma_f32_16x16x32_bf16 v[176:179], v[80:83], v[168:171], v[176:179]\n"
"v_exp_f32 v194, v194\n"
"v_add_f32 v211, v193, v211\n"
"v_cvt_pk_bf16_f32 v200, v192, v193\n"
"v_mfma_f32_16x16x32_bf16 v[180:183], v[84:87], v[168:171], v[180:183]\n"
"v_exp_f32 v195, v195\n"
"v_add_f32 v211, v194, v211\n"
"v_mfma_f32_16x16x32_bf16 v[176:179], v[88:91], v[172:175], v[176:179]\n"
"v_exp_f32 v196, v196\n"
"v_add_f32 v211, v195, v211\n"
"v_cvt_pk_bf16_f32 v201, v194, v195\n"
"v_mfma_f32_16x16x32_bf16 v[180:183], v[92:95], v[172:175], v[180:183]\n"
"v_exp_f32 v197, v197\n"
"v_add_f32 v211, v196, v211\n"
"v_exp_f32 v198, v198\n"
"v_add_f32 v211, v197, v211\n"
"v_cvt_pk_bf16_f32 v202, v196, v197\n"
"v_exp_f32 v199, v199\n"
"v_add_f32 v211, v198, v211\n"
"v_add_f32 v211, v199, v211\n"
"v_cvt_pk_bf16_f32 v203, v198, v199\n"
"ds_write_b128 v30, v[200:203] offset:44032\n"
"v_exp_f32 v176, v176\n"
"v_exp_f32 v177, v177\n"
"v_exp_f32 v178, v178\n"
"v_exp_f32 v179, v179\n"
"v_exp_f32 v180, v180\n"
"v_exp_f32 v181, v181\n"
"v_exp_f32 v182, v182\n"
"v_exp_f32 v183, v183\n"
"v_cvt_pk_bf16_f32 v204, v176, v177\n"
"v_cvt_pk_bf16_f32 v205, v178, v179\n"
"v_cvt_pk_bf16_f32 v206, v180, v181\n"
"v_cvt_pk_bf16_f32 v207, v182, v183\n"
"ds_write_b128 v30, v[204:207] offset:45056\n"
"v_add_f32 v176, v176, v180\n"
"v_add_f32 v177, v177, v181\n"
"v_add_f32 v178, v178, v182\n"
"v_add_f32 v179, v179, v183\n"
"v_add_f32 v176, v176, v177\n"
"v_add_f32 v178, v178, v179\n"
"v_add_f32 v212, v212, v176\n"
"v_add_f32 v212, v212, v178\n"
"v_mov_b32 v236, v208\n"
"v_mov_b32 v237, v209\n"
"v_mov_b32 v238, v210\n"
"v_mov_b32 v239, v211\n"
"v_mov_b32 v240, v212\n"
"v_permlane32_swap_b32 v208, v236\n"
"v_permlane32_swap_b32 v209, v237\n"
"v_permlane32_swap_b32 v210, v238\n"
"v_permlane32_swap_b32 v211, v239\n"
"v_permlane32_swap_b32 v212, v240\n"
"v_add_f32 v208, v208, v236\n"
"v_add_f32 v209, v209, v237\n"
"v_add_f32 v210, v210, v238\n"
"v_add_f32 v211, v211, v239\n"
"v_add_f32 v212, v212, v240\n"
"ds_write_b32 v31, v208 offset:0\n"
"ds_write_b32 v31, v209 offset:128\n"
"ds_write_b32 v31, v210 offset:256\n"
"ds_write_b32 v31, v211 offset:384\n"
"ds_write_b32 v31, v212 offset:512\n"
"s_waitcnt lgkmcnt(0)\n"
"s_barrier\n"
"s_setprio 0\n"
"s_mul_i32 s61, s67, 10240\n"
"s_mul_i32 s62, s66, 20480\n"
"s_add_u32 s61, s61, s62\n"
"v_lshlrev_b32 v33, 4, v0\n"
"v_add_u32 v32, s61, v33\n"
"s_mul_i32 s62, s58, 40960\n"
"s_add_u32 s72, s61, s62\n"
"v_and_b32 v38, 7, v0\n"
"v_mov_b32 v34, v248\n"
"s_add_u32 s61, s54, 8\n"
"s_nop 3\n"
"v_readlane_b32 s62, v248, s61\n"
"s_nop 1\n"
"v_mov_b32 v36, s62\n"
"s_and_b32 s61, s59, 31\n"
"s_lshl_b32 s61, s61, 9\n"
"s_lshl_b32 s62, s67, 3\n"
"s_add_u32 s62, s62, s58\n"
"s_lshl_b32 s62, s62, 1\n"
"s_add_u32 s62, s62, s66\n"
"s_lshl_b32 s62, s62, 2\n"
"s_add_u32 s61, s61, s62\n"
"v_lshl_add_u32 v35, v38, 14, s61\n"
"s_and_b32 s61, s59, 0xf9\n"
"s_lshl_b32 s62, s54, 1\n"
"s_or_b32 s61, s61, s62\n"
"s_lshl_b32 s61, s61, 9\n"
"s_lshl_b32 s62, s57, 2\n"
"s_add_u32 s61, s61, s62\n"
"v_mov_b32 v37, s61\n"
"s_mul_i32 s73, s54, 40960\n"
"s_mul_i32 s62, s57, 10240\n"
"s_add_u32 s73, s73, s62\n"
"v_mov_b32 v39, 162372\n"
"s_lshl_b32 s61, s54, 2\n"
"s_add_u32 s61, s61, 162384\n"
"v_mov_b32 v56, s61\n"
"s_lshl_b32 s61, s54, 2\n"
"s_add_u32 s61, s61, 162576\n"
"v_mov_b32 v57, s61\n"
"s_barrier\n"
"ds_read_b32 v38, v39\n"
"s_waitcnt lgkmcnt(0)\n"
"v_readfirstlane_b32 s61, v38\n"
"s_nop 0\n"
"s_cmp_lg_u32 s61, 0\n"
"s_cbranch_scc1 .Lk_SA_4_%=\n"
".Lk_w_62_%=:\n"
"ds_read_b32 v38, v56\n"
"s_waitcnt lgkmcnt(0)\n"
"v_readfirstlane_b32 s61, v38\n"
"s_add_u32 s18, s18, 1\n"
"s_cmp_gt_u32 s18, 0x400000\n"
"s_cselect_b32 s61, -1, s61\n"
"s_cmp_le_u32 s61, 0\n"
"s_cbranch_scc1 .Lk_w_62_%=\n"
"ds_read_b128 v[40:43], v32 offset:0\n"
"ds_read_b128 v[44:47], v32 offset:1024\n"
"s_add_u32 s65, s72, 0\n"
"s_waitcnt lgkmcnt(1)\n"
"buffer_store_dwordx4 v[40:43], v33, s[40:43], s65 offen offset:0 sc1\n"
"s_waitcnt lgkmcnt(0)\n"
"buffer_store_dwordx4 v[44:47], v33, s[40:43], s65 offen offset:1024 sc1\n"
".Lk_w_63_%=:\n"
"ds_read_b32 v38, v56\n"
"s_waitcnt lgkmcnt(0)\n"
"v_readfirstlane_b32 s61, v38\n"
"s_add_u32 s18, s18, 1\n"
"s_cmp_gt_u32 s18, 0x400000\n"
"s_cselect_b32 s61, -1, s61\n"
"s_cmp_le_u32 s61, 1\n"
"s_cbranch_scc1 .Lk_w_63_%=\n"
"ds_read_b128 v[48:51], v32 offset:2048\n"
"ds_read_b128 v[52:55], v32 offset:3072\n"
"s_add_u32 s65, s72, 2048\n"
"s_waitcnt lgkmcnt(1)\n"
"buffer_store_dwordx4 v[48:51], v33, s[40:43], s65 offen offset:0 sc1\n"
"s_waitcnt lgkmcnt(0)\n"
"buffer_store_dwordx4 v[52:55], v33, s[40:43], s65 offen offset:1024 sc1\n"
".Lk_w_64_%=:\n"
"ds_read_b32 v38, v56\n"
"s_waitcnt lgkmcnt(0)\n"
"v_readfirstlane_b32 s61, v38\n"
"s_add_u32 s18, s18, 1\n"
"s_cmp_gt_u32 s18, 0x400000\n"
"s_cselect_b32 s61, -1, s61\n"
"s_cmp_le_u32 s61, 2\n"
"s_cbranch_scc1 .Lk_w_64_%=\n"
"ds_read_b128 v[40:43], v32 offset:4096\n"
"ds_read_b128 v[44:47], v32 offset:5120\n"
"s_add_u32 s65, s72, 4096\n"
"s_waitcnt lgkmcnt(1)\n"
"buffer_store_dwordx4 v[40:43], v33, s[40:43], s65 offen offset:0 sc1\n"
"s_waitcnt lgkmcnt(0)\n"
"buffer_store_dwordx4 v[44:47], v33, s[40:43], s65 offen offset:1024 sc1\n"
".Lk_JA_6_%=:\n"
"s_mov_b32 s75, 0\n"
"s_barrier\n"
"v_mov_b32 v170, v57\n"
"ds_read_b128 v[96:99], v2 offset:0\n"
"ds_read_b64_tr_b16 v[136:137], v4 offset:0\n"
"ds_read_b64_tr_b16 v[138:139], v4 offset:1024\n"
"ds_read_b128 v[100:103], v2 offset:2048\n"
"ds_read_b128 v[104:107], v2 offset:4096\n"
"ds_read_b128 v[108:111], v2 offset:6144\n"
"ds_read_b128 v[112:115], v2 offset:8192\n"
"ds_read_b64_tr_b16 v[140:141], v5 offset:0\n"
"ds_read_b64_tr_b16 v[142:143], v5 offset:1024\n"
"ds_read_b64_tr_b16 v[144:145], v6 offset:0\n"
"ds_read_b64_tr_b16 v[146:147], v6 offset:1024\n"
"ds_read_b64_tr_b16 v[148:149], v7 offset:0\n"
"ds_read_b64_tr_b16 v[150:151], v7 offset:1024\n"
"ds_read_b128 v[48:51], v32 offset:6144\n"
"ds_read_b128 v[52:55], v32 offset:7168\n"
"ds_read_b128 v[40:43], v32 offset:8192\n"
"ds_read_b128 v[44:47], v32 offset:9216\n"
"s_waitcnt lgkmcnt(10)\n"
"v_mfma_f32_16x16x32_bf16 v[176:179], v[136:139], v[96:99], 0\n"
"ds_read_b128 v[116:119], v3 offset:0\n"
"v_mfma_f32_16x16x32_bf16 v[180:183], v[136:139], v[100:103], 0\n"
"ds_read_b64_tr_b16 v[152:153], v4 offset:8192\n"
"v_mfma_f32_16x16x32_bf16 v[184:187], v[136:139], v[104:107], 0\n"
"ds_read_b64_tr_b16 v[154:155], v4 offset:9216\n"
"v_mfma_f32_16x16x32_bf16 v[188:191], v[136:139], v[108:111], 0\n"
"ds_read_b128 v[120:123], v3 offset:2048\n"
"v_mfma_f32_16x16x32_bf16 v[192:195], v[136:139], v[112:115], 0\n"
"ds_read_b128 v[124:127], v3 offset:4096\n"
"s_waitcnt lgkmcnt(5)\n"
"v_mfma_f32_16x16x32_bf16 v[212:215], v[140:143], v[112:115], 0\n"
"ds_read_b128 v[128:131], v3 offset:6144\n"
"v_mfma_f32_16x16x32_bf16 v[208:211], v[140:143], v[108:111], 0\n"
"ds_read_b128 v[132:135], v3 offset:8192\n"
"s_add_u32 s65, s72, 6144\n"
"v_mfma_f32_16x16x32_bf16 v[204:207], v[140:143], v[104:107], 0\n"
"ds_read_b64_tr_b16 v[156:157], v5 offset:8192\n"
"buffer_store_dwordx4 v[48:51], v33, s[40:43], s65 offen offset:0 sc1\n"
"v_mfma_f32_16x16x32_bf16 v[200:203], v[140:143], v[100:103], 0\n"
"ds_read_b64_tr_b16 v[158:159], v5 offset:9216\n"
"buffer_store_dwordx4 v[52:55], v33, s[40:43], s65 offen offset:1024 sc1\n"
"v_mfma_f32_16x16x32_bf16 v[196:199], v[140:143], v[96:99], 0\n"
"ds_read_b64_tr_b16 v[160:161], v6 offset:8192\n"
"s_add_u32 s65, s72, 8192\n"
"v_mfma_f32_16x16x32_bf16 v[216:219], v[144:147], v[96:99], 0\n"
"ds_read_b64_tr_b16 v[162:163], v6 offset:9216\n"
"buffer_store_dwordx4 v[40:43], v33, s[40:43], s65 offen offset:0 sc1\n"
"v_mfma_f32_16x16x32_bf16 v[220:223], v[144:147], v[100:103], 0\n"
"ds_read_b64_tr_b16 v[164:165], v7 offset:8192\n"
"buffer_store_dwordx4 v[44:47], v33, s[40:43], s65 offen offset:1024 sc1\n"
"v_mfma_f32_16x16x32_bf16 v[224:227], v[144:147], v[104:107], 0\n"
"ds_read_b64_tr_b16 v[166:167], v7 offset:9216\n"
"v_mfma_f32_16x16x32_bf16 v[228:231], v[144:147], v[108:111], 0\n"
"v_mfma_f32_16x16x32_bf16 v[232:235], v[144:147], v[112:115], 0\n"
"v_mfma_f32_16x16x32_bf16 v[252:255], v[148:151], v[112:115], 0\n"
"v_mfma_f32_16x16x32_bf16 v[248:251], v[148:151], v[108:111], 0\n"
"v_mfma_f32_16x16x32_bf16 v[244:247], v[148:151], v[104:107], 0\n"
"v_mfma_f32_16x16x32_bf16 v[240:243], v[148:151], v[100:103], 0\n"
"v_mfma_f32_16x16x32_bf16 v[236:239], v[148:151], v[96:99], 0\n"
"s_waitcnt lgkmcnt(6)\n"
"v_mfma_f32_16x16x32_bf16 v[176:179], v[152:155], v[116:119], v[176:179]\n"
"ds_read_b128 v[96:99], v2 offset:20480\n"
"v_mfma_f32_16x16x32_bf16 v[180:183], v[152:155], v[120:123], v[180:183]\n"
"ds_read_b64_tr_b16 v[136:137], v4 offset:16384\n"
"v_mfma_f32_16x16x32_bf16 v[184:187], v[152:155], v[124:127], v[184:187]\n"
"ds_read_b64_tr_b16 v[138:139], v4 offset:17408\n"
"v_mfma_f32_16x16x32_bf16 v[188:191], v[152:155], v[128:131], v[188:191]\n"
"ds_read_b128 v[100:103], v2 offset:22528\n"
"v_mfma_f32_16x16x32_bf16 v[192:195], v[152:155], v[132:135], v[192:195]\n"
"ds_read_b128 v[104:107], v2 offset:24576\n"
"s_waitcnt lgkmcnt(5)\n"
"v_mfma_f32_16x16x32_bf16 v[212:215], v[156:159], v[132:135], v[212:215]\n"
"ds_read_b128 v[108:111], v2 offset:26624\n"
"v_mfma_f32_16x16x32_bf16 v[208:211], v[156:159], v[128:131], v[208:211]\n"
"ds_read_b128 v[112:115], v2 offset:28672\n"
"v_mfma_f32_16x16x32_bf16 v[204:207], v[156:159], v[124:127], v[204:207]\n"
"ds_read_b64_tr_b16 v[140:141], v5 offset:16384\n"
"v_mfma_f32_16x16x32_bf16 v[200:203], v[156:159], v[120:123], v[200:203]\n"
"ds_read_b64_tr_b16 v[142:143], v5 offset:17408\n"
"v_mfma_f32_16x16x32_bf16 v[196:199], v[156:159], v[116:119], v[196:199]\n"
"ds_read_b64_tr_b16 v[144:145], v6 offset:16384\n"
"v_mfma_f32_16x16x32_bf16 v[216:219], v[160:163], v[116:119], v[216:219]\n"
"ds_read_b64_tr_b16 v[146:147], v6 offset:17408\n"
"v_mfma_f32_16x16x32_bf16 v[220:223], v[160:163], v[120:123], v[220:223]\n"
"ds_read_b64_tr_b16 v[148:149], v7 offset:16384\n"
"v_mfma_f32_16x16x32_bf16 v[224:227], v[160:163], v[124:127], v[224:227]\n"
"ds_read_b64_tr_b16 v[150:151], v7 offset:17408\n"
"v_mfma_f32_16x16x32_bf16 v[228:231], v[160:163], v[128:131], v[228:231]\n"
"v_mfma_f32_16x16x32_bf16 v[232:235], v[160:163], v[132:135], v[232:235]\n"
"v_mfma_f32_16x16x32_bf16 v[252:255], v[164:167], v[132:135], v[252:255]\n"
"v_mfma_f32_16x16x32_bf16 v[248:251], v[164:167], v[128:131], v[248:251]\n"
"v_mfma_f32_16x16x32_bf16 v[244:247], v[164:167], v[124:127], v[244:247]\n"
"v_mfma_f32_16x16x32_bf16 v[240:243], v[164:167], v[120:123], v[240:243]\n"
"v_mfma_f32_16x16x32_bf16 v[236:239], v[164:167], v[116:119], v[236:239]\n"
"s_waitcnt lgkmcnt(6)\n"
"v_mfma_f32_16x16x32_bf16 v[176:179], v[136:139], v[96:99], v[176:179]\n"
"ds_read_b128 v[116:119], v3 offset:20480\n"
"v_mfma_f32_16x16x32_bf16 v[180:183], v[136:139], v[100:103], v[180:183]\n"
"ds_read_b64_tr_b16 v[152:153], v4 offset:24576\n"
"v_mfma_f32_16x16x32_bf16 v[184:187], v[136:139], v[104:107], v[184:187]\n"
"ds_read_b64_tr_b16 v[154:155], v4 offset:25600\n"
"v_mfma_f32_16x16x32_bf16 v[188:191], v[136:139], v[108:111], v[188:191]\n"
"ds_read_b128 v[120:123], v3 offset:22528\n"
"v_mfma_f32_16x16x32_bf16 v[192:195], v[136:139], v[112:115], v[192:195]\n"
"ds_read_b128 v[124:127], v3 offset:24576\n"
"s_waitcnt lgkmcnt(5)\n"
"v_mfma_f32_16x16x32_bf16 v[212:215], v[140:143], v[112:115], v[212:215]\n"
"ds_read_b128 v[128:131], v3 offset:26624\n"
"v_mfma_f32_16x16x32_bf16 v[208:211], v[140:143], v[108:111], v[208:211]\n"
"ds_read_b128 v[132:135], v3 offset:28672\n"
"v_mfma_f32_16x16x32_bf16 v[204:207], v[140:143], v[104:107], v[204:207]\n"
"ds_read_b64_tr_b16 v[156:157], v5 offset:24576\n"
"v_mfma_f32_16x16x32_bf16 v[200:203], v[140:143], v[100:103], v[200:203]\n"
"ds_read_b64_tr_b16 v[158:159], v5 offset:25600\n"
"v_mfma_f32_16x16x32_bf16 v[196:199], v[140:143], v[96:99], v[196:199]\n"
"ds_read_b64_tr_b16 v[160:161], v6 offset:24576\n"
"s_getreg_b32 s61, hwreg(HW_REG_IB_STS)\n"
"s_and_b32 s61, s61, 0xc0000f\n"
"s_or_b32 s61, s61, s75\n"
"s_cbranch_scc1 .Lk_r_65_%=\n"
"s_waitcnt vmcnt(0)\n"
"s_mov_b64 exec, 0xff\n"
"global_store_dword v35, v34, s[52:53] sc1\n"
"s_mov_b64 exec, -1\n"
"s_mov_b32 s75, 1\n"
".Lk_r_65_%=:\n"
"v_mfma_f32_16x16x32_bf16 v[216:219], v[144:147], v[96:99], v[216:219]\n"
"ds_read_b64_tr_b16 v[162:163], v6 offset:25600\n"
"v_mfma_f32_16x16x32_bf16 v[220:223], v[144:147], v[100:103], v[220:223]\n"
"ds_read_b64_tr_b16 v[164:165], v7 offset:24576\n"
"v_mfma_f32_16x16x32_bf16 v[224:227], v[144:147], v[104:107], v[224:227]\n"
"ds_read_b64_tr_b16 v[166:167], v7 offset:25600\n"
"v_mfma_f32_16x16x32_bf16 v[228:231], v[144:147], v[108:111], v[228:231]\n"
"v_mfma_f32_16x16x32_bf16 v[232:235], v[144:147], v[112:115], v[232:235]\n"
"v_mfma_f32_16x16x32_bf16 v[252:255], v[148:151], v[112:115], v[252:255]\n"
"v_mfma_f32_16x16x32_bf16 v[248:251], v[148:151], v[108:111], v[248:251]\n"
"v_mfma_f32_16x16x32_bf16 v[244:247], v[148:151], v[104:107], v[244:247]\n"
"v_mfma_f32_16x16x32_bf16 v[240:243], v[148:151], v[100:103], v[240:243]\n"
"v_mfma_f32_16x16x32_bf16 v[236:239], v[148:151], v[96:99], v[236:239]\n"
"s_getreg_b32 s61, hwreg(HW_REG_IB_STS)\n"
"s_and_b32 s61, s61, 0xc0000f\n"
"s_or_b32 s61, s61, s75\n"
"s_cbranch_scc1 .Lk_r_66_%=\n"
"s_waitcnt vmcnt(0)\n"
"s_mov_b64 exec, 0xff\n"
"global_store_dword v35, v34, s[52:53] sc1\n"
"s_mov_b64 exec, -1\n"
"s_mov_b32 s75, 1\n"
".Lk_r_66_%=:\n"
"s_waitcnt lgkmcnt(6)\n"
"v_mfma_f32_16x16x32_bf16 v[176:179], v[152:155], v[116:119], v[176:179]\n"
"v_mfma_f32_16x16x32_bf16 v[180:183], v[152:155], v[120:123], v[180:183]\n"
"v_mfma_f32_16x16x32_bf16 v[184:187], v[152:155], v[124:127], v[184:187]\n"
"v_mfma_f32_16x16x32_bf16 v[188:191], v[152:155], v[128:131], v[188:191]\n"
"v_mfma_f32_16x16x32_bf16 v[192:195], v[152:155], v[132:135], v[192:195]\n"
"s_waitcnt lgkmcnt(0)\n"
"v_mfma_f32_16x16x32_bf16 v[212:215], v[156:159], v[132:135], v[212:215]\n"
"v_mfma_f32_16x16x32_bf16 v[208:211], v[156:159], v[128:131], v[208:211]\n"
"v_mfma_f32_16x16x32_bf16 v[204:207], v[156:159], v[124:127], v[204:207]\n"
"v_mfma_f32_16x16x32_bf16 v[200:203], v[156:159], v[120:123], v[200:203]\n"
"v_mfma_f32_16x16x32_bf16 v[196:199], v[156:159], v[116:119], v[196:199]\n"
"s_getreg_b32 s61, hwreg(HW_REG_IB_STS)\n"
"s_and_b32 s61, s61, 0xc0000f\n"
"s_or_b32 s61, s61, s75\n"
"s_cbranch_scc1 .Lk_r_67_%=\n"
"s_waitcnt vmcnt(0)\n"
"s_mov_b64 exec, 0xff\n"
"global_store_dword v35, v34, s[52:53] sc1\n"
"s_mov_b64 exec, -1\n"
"s_mov_b32 s75, 1\n"
".Lk_r_67_%=:\n"
"v_mfma_f32_16x16x32_bf16 v[216:219], v[160:163], v[116:119], v[216:219]\n"
"v_mfma_f32_16x16x32_bf16 v[220:223], v[160:163], v[120:123], v[220:223]\n"
"v_mfma_f32_16x16x32_bf16 v[224:227], v[160:163], v[124:127], v[224:227]\n"
"v_mfma_f32_16x16x32_bf16 v[228:231], v[160:163], v[128:131], v[228:231]\n"
"v_mfma_f32_16x16x32_bf16 v[232:235], v[160:163], v[132:135], v[232:235]\n"
"v_mfma_f32_16x16x32_bf16 v[252:255], v[164:167], v[132:135], v[252:255]\n"
"v_mfma_f32_16x16x32_bf16 v[248:251], v[164:167], v[128:131], v[248:251]\n"
"v_mfma_f32_16x16x32_bf16 v[244:247], v[164:167], v[124:127], v[244:247]\n"
"v_mfma_f32_16x16x32_bf16 v[240:243], v[164:167], v[120:123], v[240:243]\n"
"v_mfma_f32_16x16x32_bf16 v[236:239], v[164:167], v[116:119], v[236:239]\n"
"s_getreg_b32 s61, hwreg(HW_REG_IB_STS)\n"
"s_and_b32 s61, s61, 0xc0000f\n"
"s_or_b32 s61, s61, s75\n"
"s_cbranch_scc1 .Lk_r_68_%=\n"
"s_waitcnt vmcnt(0)\n"
"s_mov_b64 exec, 0xff\n"
"global_store_dword v35, v34, s[52:53] sc1\n"
"s_mov_b64 exec, -1\n"
"s_mov_b32 s75, 1\n"
".Lk_r_68_%=:\n"
"s_cmp_lg_u32 s75, 0\n"
"s_cbranch_scc1 .Lk_sk_69_%=\n"
"s_waitcnt vmcnt(0)\n"
"s_mov_b64 exec, 0xff\n"
"global_store_dword v35, v34, s[52:53] sc1\n"
"s_mov_b64 exec, -1\n"
"s_mov_b32 s75, 1\n"
".Lk_sk_69_%=:\n"
"s_barrier\n"
"ds_read_b128 v[96:99], v2 offset:40960\n"
"ds_read_b64_tr_b16 v[136:137], v4 offset:32768\n"
"ds_read_b64_tr_b16 v[138:139], v4 offset:33792\n"
"ds_read_b128 v[100:103], v2 offset:43008\n"
"ds_read_b128 v[104:107], v2 offset:45056\n"
"ds_read_b128 v[108:111], v2 offset:47104\n"
"ds_read_b128 v[112:115], v2 offset:49152\n"
"ds_read_b64_tr_b16 v[140:141], v5 offset:32768\n"
"ds_read_b64_tr_b16 v[142:143], v5 offset:33792\n"
"ds_read_b64_tr_b16 v[144:145], v6 offset:32768\n"
"ds_read_b64_tr_b16 v[146:147], v6 offset:33792\n"
"ds_read_b64_tr_b16 v[148:149], v7 offset:32768\n"
"ds_read_b64_tr_b16 v[150:151], v7 offset:33792\n"
"s_waitcnt lgkmcnt(6)\n"
"v_mfma_f32_16x16x32_bf16 v[176:179], v[136:139], v[96:99], v[176:179]\n"
"ds_read_b128 v[116:119], v3 offset:40960\n"
"v_mfma_f32_16x16x32_bf16 v[180:183], v[136:139], v[100:103], v[180:183]\n"
"ds_read_b64_tr_b16 v[152:153], v4 offset:40960\n"
"v_mfma_f32_16x16x32_bf16 v[184:187], v[136:139], v[104:107], v[184:187]\n"
"ds_read_b64_tr_b16 v[154:155], v4 offset:41984\n"
"v_mfma_f32_16x16x32_bf16 v[188:191], v[136:139], v[108:111], v[188:191]\n"
"ds_read_b128 v[120:123], v3 offset:43008\n"
"v_mfma_f32_16x16x32_bf16 v[192:195], v[136:139], v[112:115], v[192:195]\n"
"ds_read_b128 v[124:127], v3 offset:45056\n"
"s_waitcnt lgkmcnt(5)\n"
"v_mfma_f32_16x16x32_bf16 v[212:215], v[140:143], v[112:115], v[212:215]\n"
"ds_read_b128 v[128:131], v3 offset:47104\n"
"v_mfma_f32_16x16x32_bf16 v[208:211], v[140:143], v[108:111], v[208:211]\n"
"ds_read_b128 v[132:135], v3 offset:49152\n"
"v_mfma_f32_16x16x32_bf16 v[204:207], v[140:143], v[104:107], v[204:207]\n"
"ds_read_b64_tr_b16 v[156:157], v5 offset:40960\n"
"v_mfma_f32_16x16x32_bf16 v[200:203], v[140:143], v[100:103], v[200:203]\n"
"ds_read_b64_tr_b16 v[158:159], v5 offset:41984\n"
"v_mfma_f32_16x16x32_bf16 v[196:199], v[140:143], v[96:99], v[196:199]\n"
"ds_read_b64_tr_b16 v[160:161], v6 offset:40960\n"
"v_mfma_f32_16x16x32_bf16 v[216:219], v[144:147], v[96:99], v[216:219]\n"
"ds_read_b64_tr_b16 v[162:163], v6 offset:41984\n"
"v_mfma_f32_16x16x32_bf16 v[220:223], v[144:147], v[100:103], v[220:223]\n"
"ds_read_b64_tr_b16 v[164:165], v7 offset:40960\n"
"v_mfma_f32_16x16x32_bf16 v[224:227], v[144:147], v[104:107], v[224:227]\n"
"ds_read_b64_tr_b16 v[166:167], v7 offset:41984\n"
"v_mfma_f32_16x16x32_bf16 v[228:231], v[144:147], v[108:111], v[228:231]\n"
"v_mfma_f32_16x16x32_bf16 v[232:235], v[144:147], v[112:115], v[232:235]\n"
"v_mfma_f32_16x16x32_bf16 v[252:255], v[148:151], v[112:115], v[252:255]\n"
"v_mfma_f32_16x16x32_bf16 v[248:251], v[148:151], v[108:111], v[248:251]\n"
"v_mfma_f32_16x16x32_bf16 v[244:247], v[148:151], v[104:107], v[244:247]\n"
"v_mfma_f32_16x16x32_bf16 v[240:243], v[148:151], v[100:103], v[240:243]\n"
"v_mfma_f32_16x16x32_bf16 v[236:239], v[148:151], v[96:99], v[236:239]\n"
"s_waitcnt lgkmcnt(6)\n"
"v_mfma_f32_16x16x32_bf16 v[176:179], v[152:155], v[116:119], v[176:179]\n"
"v_mfma_f32_16x16x32_bf16 v[180:183], v[152:155], v[120:123], v[180:183]\n"
"v_mfma_f32_16x16x32_bf16 v[184:187], v[152:155], v[124:127], v[184:187]\n"
"s_waitcnt lgkmcnt(0)\n"
"s_barrier\n"
"v_mfma_f32_16x16x32_bf16 v[188:191], v[152:155], v[128:131], v[188:191]\n"
"ds_read_b128 v[96:99], v8 offset:0\n"
"v_mfma_f32_16x16x32_bf16 v[192:195], v[152:155], v[132:135], v[192:195]\n"
"ds_read_b64_tr_b16 v[136:137], v4 offset:49152\n"
"v_mfma_f32_16x16x32_bf16 v[212:215], v[156:159], v[132:135], v[212:215]\n"
"ds_read_b64_tr_b16 v[138:139], v4 offset:50176\n"
"v_mfma_f32_16x16x32_bf16 v[208:211], v[156:159], v[128:131], v[208:211]\n"
"ds_read_b128 v[100:103], v8 offset:2048\n"
"v_mfma_f32_16x16x32_bf16 v[204:207], v[156:159], v[124:127], v[204:207]\n"
"ds_read_b128 v[104:107], v8 offset:4096\n"
"v_mfma_f32_16x16x32_bf16 v[200:203], v[156:159], v[120:123], v[200:203]\n"
"ds_read_b128 v[108:111], v8 offset:6144\n"
"v_mfma_f32_16x16x32_bf16 v[196:199], v[156:159], v[116:119], v[196:199]\n"
"ds_read_b128 v[112:115], v8 offset:8192\n"
"v_mfma_f32_16x16x32_bf16 v[216:219], v[160:163], v[116:119], v[216:219]\n"
"ds_read_b64_tr_b16 v[140:141], v5 offset:49152\n"
"v_mfma_f32_16x16x32_bf16 v[220:223], v[160:163], v[120:123], v[220:223]\n"
"ds_read_b64_tr_b16 v[142:143], v5 offset:50176\n"
"v_mfma_f32_16x16x32_bf16 v[224:227], v[160:163], v[124:127], v[224:227]\n"
"ds_read_b64_tr_b16 v[144:145], v6 offset:49152\n"
"v_mfma_f32_16x16x32_bf16 v[228:231], v[160:163], v[128:131], v[228:231]\n"
"ds_read_b64_tr_b16 v[146:147], v6 offset:50176\n"
"v_mfma_f32_16x16x32_bf16 v[232:235], v[160:163], v[132:135], v[232:235]\n"
"ds_read_b64_tr_b16 v[148:149], v7 offset:49152\n"
"v_mfma_f32_16x16x32_bf16 v[252:255], v[164:167], v[132:135], v[252:255]\n"
"ds_read_b64_tr_b16 v[150:151], v7 offset:50176\n"
"v_mfma_f32_16x16x32_bf16 v[248:251], v[164:167], v[128:131], v[248:251]\n"
"v_mfma_f32_16x16x32_bf16 v[244:247], v[164:167], v[124:127], v[244:247]\n"
"v_mfma_f32_16x16x32_bf16 v[240:243], v[164:167], v[120:123], v[240:243]\n"
"v_mfma_f32_16x16x32_bf16 v[236:239], v[164:167], v[116:119], v[236:239]\n"
"s_waitcnt lgkmcnt(6)\n"
"v_mfma_f32_16x16x32_bf16 v[176:179], v[136:139], v[96:99], v[176:179]\n"
"ds_read_b128 v[116:119], v9 offset:0\n"
"v_mfma_f32_16x16x32_bf16 v[180:183], v[136:139], v[100:103], v[180:183]\n"
"ds_read_b64_tr_b16 v[152:153], v4 offset:57344\n"
"v_mfma_f32_16x16x32_bf16 v[184:187], v[136:139], v[104:107], v[184:187]\n"
"ds_read_b64_tr_b16 v[154:155], v4 offset:58368\n"
"v_mfma_f32_16x16x32_bf16 v[188:191], v[136:139], v[108:111], v[188:191]\n"
"ds_read_b128 v[120:123], v9 offset:2048\n"
"v_mfma_f32_16x16x32_bf16 v[192:195], v[136:139], v[112:115], v[192:195]\n"
"ds_read_b128 v[124:127], v9 offset:4096\n"
"s_waitcnt lgkmcnt(5)\n"
"v_mfma_f32_16x16x32_bf16 v[212:215], v[140:143], v[112:115], v[212:215]\n"
"ds_read_b128 v[128:131], v9 offset:6144\n"
"v_mfma_f32_16x16x32_bf16 v[208:211], v[140:143], v[108:111], v[208:211]\n"
"ds_read_b128 v[132:135], v9 offset:8192\n"
"v_mfma_f32_16x16x32_bf16 v[204:207], v[140:143], v[104:107], v[204:207]\n"
"ds_read_b64_tr_b16 v[156:157], v5 offset:57344\n"
"v_mfma_f32_16x16x32_bf16 v[200:203], v[140:143], v[100:103], v[200:203]\n"
"ds_read_b64_tr_b16 v[158:159], v5 offset:58368\n"
"v_mfma_f32_16x16x32_bf16 v[196:199], v[140:143], v[96:99], v[196:199]\n"
"ds_read_b64_tr_b16 v[160:161], v6 offset:57344\n"
"v_mfma_f32_16x16x32_bf16 v[216:219], v[144:147], v[96:99], v[216:219]\n"
"ds_read_b64_tr_b16 v[162:163], v6 offset:58368\n"
"v_mfma_f32_16x16x32_bf16 v[220:223], v[144:147], v[100:103], v[220:223]\n"
"ds_read_b64_tr_b16 v[164:165], v7 offset:57344\n"
"v_mfma_f32_16x16x32_bf16 v[224:227], v[144:147], v[104:107], v[224:227]\n"
"ds_read_b64_tr_b16 v[166:167], v7 offset:58368\n"
"v_mfma_f32_16x16x32_bf16 v[228:231], v[144:147], v[108:111], v[228:231]\n"
"v_mfma_f32_16x16x32_bf16 v[232:235], v[144:147], v[112:115], v[232:235]\n"
"v_mfma_f32_16x16x32_bf16 v[252:255], v[148:151], v[112:115], v[252:255]\n"
"v_mfma_f32_16x16x32_bf16 v[248:251], v[148:151], v[108:111], v[248:251]\n"
"v_mfma_f32_16x16x32_bf16 v[244:247], v[148:151], v[104:107], v[244:247]\n"
"v_mfma_f32_16x16x32_bf16 v[240:243], v[148:151], v[100:103], v[240:243]\n"
"v_mfma_f32_16x16x32_bf16 v[236:239], v[148:151], v[96:99], v[236:239]\n"
"s_waitcnt lgkmcnt(6)\n"
"v_mfma_f32_16x16x32_bf16 v[176:179], v[152:155], v[116:119], v[176:179]\n"
"v_mfma_f32_16x16x32_bf16 v[180:183], v[152:155], v[120:123], v[180:183]\n"
"v_mfma_f32_16x16x32_bf16 v[184:187], v[152:155], v[124:127], v[184:187]\n"
"s_waitcnt lgkmcnt(0)\n"
"s_barrier\n"
"v_mfma_f32_16x16x32_bf16 v[188:191], v[152:155], v[128:131], v[188:191]\n"
"ds_read_b128 v[96:99], v2 offset:0\n"
"v_mfma_f32_16x16x32_bf16 v[192:195], v[152:155], v[132:135], v[192:195]\n"
"ds_read_b64_tr_b16 v[136:137], v4 offset:0\n"
"v_mfma_f32_16x16x32_bf16 v[212:215], v[156:159], v[132:135], v[212:215]\n"
"ds_read_b64_tr_b16 v[138:139], v4 offset:1024\n"
"v_mfma_f32_16x16x32_bf16 v[208:211], v[156:159], v[128:131], v[208:211]\n"
"ds_read_b128 v[100:103], v2 offset:2048\n"
"v_mfma_f32_16x16x32_bf16 v[204:207], v[156:159], v[124:127], v[204:207]\n"
"ds_read_b128 v[104:107], v2 offset:4096\n"
"v_mfma_f32_16x16x32_bf16 v[200:203], v[156:159], v[120:123], v[200:203]\n"
"ds_read_b128 v[108:111], v2 offset:6144\n"
"v_mfma_f32_16x16x32_bf16 v[196:199], v[156:159], v[116:119], v[196:199]\n"
"ds_read_b128 v[112:115], v2 offset:8192\n"
"v_mfma_f32_16x16x32_bf16 v[216:219], v[160:163], v[116:119], v[216:219]\n"
"ds_read_b64_tr_b16 v[140:141], v5 offset:0\n"
"v_mfma_f32_16x16x32_bf16 v[220:223], v[160:163], v[120:123], v[220:223]\n"
"ds_read_b64_tr_b16 v[142:143], v5 offset:1024\n"
"v_mfma_f32_16x16x32_bf16 v[224:227], v[160:163], v[124:127], v[224:227]\n"
"ds_read_b64_tr_b16 v[144:145], v6 offset:0\n"
"v_mfma_f32_16x16x32_bf16 v[228:231], v[160:163], v[128:131], v[228:231]\n"
"ds_read_b64_tr_b16 v[146:147], v6 offset:1024\n"
"v_mfma_f32_16x16x32_bf16 v[232:235], v[160:163], v[132:135], v[232:235]\n"
"ds_read_b64_tr_b16 v[148:149], v7 offset:0\n"
"v_mfma_f32_16x16x32_bf16 v[252:255], v[164:167], v[132:135], v[252:255]\n"
"ds_read_b64_tr_b16 v[150:151], v7 offset:1024\n"
"v_mfma_f32_16x16x32_bf16 v[248:251], v[164:167], v[128:131], v[248:251]\n"
"v_mfma_f32_16x16x32_bf16 v[244:247], v[164:167], v[124:127], v[244:247]\n"
"v_mfma_f32_16x16x32_bf16 v[240:243], v[164:167], v[120:123], v[240:243]\n"
"v_mfma_f32_16x16x32_bf16 v[236:239], v[164:167], v[116:119], v[236:239]\n"
"s_waitcnt lgkmcnt(6)\n"
"v_mfma_f32_16x16x32_bf16 v[176:179], v[136:139], v[96:99], v[176:179]\n"
"ds_read_b128 v[116:119], v3 offset:0\n"
"v_mfma_f32_16x16x32_bf16 v[180:183], v[136:139], v[100:103], v[180:183]\n"
"ds_read_b64_tr_b16 v[152:153], v4 offset:8192\n"
"v_mfma_f32_16x16x32_bf16 v[184:187], v[136:139], v[104:107], v[184:187]\n"
"ds_read_b64_tr_b16 v[154:155], v4 offset:9216\n"
"v_mfma_f32_16x16x32_bf16 v[188:191], v[136:139], v[108:111], v[188:191]\n"
"ds_read_b128 v[120:123], v3 offset:2048\n"
"v_mfma_f32_16x16x32_bf16 v[192:195], v[136:139], v[112:115], v[192:195]\n"
"ds_read_b128 v[124:127], v3 offset:4096\n"
"s_waitcnt lgkmcnt(5)\n"
"v_mfma_f32_16x16x32_bf16 v[212:215], v[140:143], v[112:115], v[212:215]\n"
"ds_read_b128 v[128:131], v3 offset:6144\n"
"v_mfma_f32_16x16x32_bf16 v[208:211], v[140:143], v[108:111], v[208:211]\n"
"ds_read_b128 v[132:135], v3 offset:8192\n"
"v_mfma_f32_16x16x32_bf16 v[204:207], v[140:143], v[104:107], v[204:207]\n"
"ds_read_b64_tr_b16 v[156:157], v5 offset:8192\n"
"v_mfma_f32_16x16x32_bf16 v[200:203], v[140:143], v[100:103], v[200:203]\n"
"ds_read_b64_tr_b16 v[158:159], v5 offset:9216\n"
"v_mfma_f32_16x16x32_bf16 v[196:199], v[140:143], v[96:99], v[196:199]\n"
"ds_read_b64_tr_b16 v[160:161], v6 offset:8192\n"
"v_mfma_f32_16x16x32_bf16 v[216:219], v[144:147], v[96:99], v[216:219]\n"
"ds_read_b64_tr_b16 v[162:163], v6 offset:9216\n"
"v_mfma_f32_16x16x32_bf16 v[220:223], v[144:147], v[100:103], v[220:223]\n"
"ds_read_b64_tr_b16 v[164:165], v7 offset:8192\n"
"v_mfma_f32_16x16x32_bf16 v[224:227], v[144:147], v[104:107], v[224:227]\n"
"ds_read_b64_tr_b16 v[166:167], v7 offset:9216\n"
"v_mfma_f32_16x16x32_bf16 v[228:231], v[144:147], v[108:111], v[228:231]\n"
"v_mfma_f32_16x16x32_bf16 v[232:235], v[144:147], v[112:115], v[232:235]\n"
"v_mfma_f32_16x16x32_bf16 v[252:255], v[148:151], v[112:115], v[252:255]\n"
"v_mfma_f32_16x16x32_bf16 v[248:251], v[148:151], v[108:111], v[248:251]\n"
"v_mfma_f32_16x16x32_bf16 v[244:247], v[148:151], v[104:107], v[244:247]\n"
"v_mfma_f32_16x16x32_bf16 v[240:243], v[148:151], v[100:103], v[240:243]\n"
"v_mfma_f32_16x16x32_bf16 v[236:239], v[148:151], v[96:99], v[236:239]\n"
"s_waitcnt lgkmcnt(6)\n"
"v_mfma_f32_16x16x32_bf16 v[176:179], v[152:155], v[116:119], v[176:179]\n"
"v_mfma_f32_16x16x32_bf16 v[180:183], v[152:155], v[120:123], v[180:183]\n"
"v_mfma_f32_16x16x32_bf16 v[184:187], v[152:155], v[124:127], v[184:187]\n"
"s_waitcnt lgkmcnt(0)\n"
"s_barrier\n"
"v_mfma_f32_16x16x32_bf16 v[188:191], v[152:155], v[128:131], v[188:191]\n"
"ds_read_b128 v[96:99], v2 offset:20480\n"
"v_mfma_f32_16x16x32_bf16 v[192:195], v[152:155], v[132:135], v[192:195]\n"
"ds_read_b64_tr_b16 v[136:137], v4 offset:16384\n"
"v_mfma_f32_16x16x32_bf16 v[212:215], v[156:159], v[132:135], v[212:215]\n"
"ds_read_b64_tr_b16 v[138:139], v4 offset:17408\n"
"v_mfma_f32_16x16x32_bf16 v[208:211], v[156:159], v[128:131], v[208:211]\n"
"ds_read_b128 v[100:103], v2 offset:22528\n"
"v_mfma_f32_16x16x32_bf16 v[204:207], v[156:159], v[124:127], v[204:207]\n"
"ds_read_b128 v[104:107], v2 offset:24576\n"
"v_mfma_f32_16x16x32_bf16 v[200:203], v[156:159], v[120:123], v[200:203]\n"
"ds_read_b128 v[108:111], v2 offset:26624\n"
"v_mfma_f32_16x16x32_bf16 v[196:199], v[156:159], v[116:119], v[196:199]\n"
"ds_read_b128 v[112:115], v2 offset:28672\n"
"v_mfma_f32_16x16x32_bf16 v[216:219], v[160:163], v[116:119], v[216:219]\n"
"ds_read_b64_tr_b16 v[140:141], v5 offset:16384\n"
"v_mfma_f32_16x16x32_bf16 v[220:223], v[160:163], v[120:123], v[220:223]\n"
"ds_read_b64_tr_b16 v[142:143], v5 offset:17408\n"
"v_mfma_f32_16x16x32_bf16 v[224:227], v[160:163], v[124:127], v[224:227]\n"
"ds_read_b64_tr_b16 v[144:145], v6 offset:16384\n"
"v_mfma_f32_16x16x32_bf16 v[228:231], v[160:163], v[128:131], v[228:231]\n"
"ds_read_b64_tr_b16 v[146:147], v6 offset:17408\n"
"v_mfma_f32_16x16x32_bf16 v[232:235], v[160:163], v[132:135], v[232:235]\n"
"ds_read_b64_tr_b16 v[148:149], v7 offset:16384\n"
"v_mfma_f32_16x16x32_bf16 v[252:255], v[164:167], v[132:135], v[252:255]\n"
"ds_read_b64_tr_b16 v[150:151], v7 offset:17408\n"
"v_mfma_f32_16x16x32_bf16 v[248:251], v[164:167], v[128:131], v[248:251]\n"
"v_mfma_f32_16x16x32_bf16 v[244:247], v[164:167], v[124:127], v[244:247]\n"
"v_mfma_f32_16x16x32_bf16 v[240:243], v[164:167], v[120:123], v[240:243]\n"
"v_mfma_f32_16x16x32_bf16 v[236:239], v[164:167], v[116:119], v[236:239]\n"
"s_waitcnt lgkmcnt(6)\n"
"v_mfma_f32_16x16x32_bf16 v[176:179], v[136:139], v[96:99], v[176:179]\n"
"ds_read_b128 v[116:119], v3 offset:20480\n"
"v_mfma_f32_16x16x32_bf16 v[180:183], v[136:139], v[100:103], v[180:183]\n"
"ds_read_b64_tr_b16 v[152:153], v4 offset:24576\n"
"v_mfma_f32_16x16x32_bf16 v[184:187], v[136:139], v[104:107], v[184:187]\n"
"ds_read_b64_tr_b16 v[154:155], v4 offset:25600\n"
"v_mfma_f32_16x16x32_bf16 v[188:191], v[136:139], v[108:111], v[188:191]\n"
"ds_read_b128 v[120:123], v3 offset:22528\n"
"v_mfma_f32_16x16x32_bf16 v[192:195], v[136:139], v[112:115], v[192:195]\n"
"ds_read_b128 v[124:127], v3 offset:24576\n"
"s_waitcnt lgkmcnt(5)\n"
"v_mfma_f32_16x16x32_bf16 v[212:215], v[140:143], v[112:115], v[212:215]\n"
"ds_read_b128 v[128:131], v3 offset:26624\n"
"v_mfma_f32_16x16x32_bf16 v[208:211], v[140:143], v[108:111], v[208:211]\n"
"ds_read_b128 v[132:135], v3 offset:28672\n"
"v_mfma_f32_16x16x32_bf16 v[204:207], v[140:143], v[104:107], v[204:207]\n"
"ds_read_b64_tr_b16 v[156:157], v5 offset:24576\n"
"v_mfma_f32_16x16x32_bf16 v[200:203], v[140:143], v[100:103], v[200:203]\n"
"ds_read_b64_tr_b16 v[158:159], v5 offset:25600\n"
"v_mfma_f32_16x16x32_bf16 v[196:199], v[140:143], v[96:99], v[196:199]\n"
"ds_read_b64_tr_b16 v[160:161], v6 offset:24576\n"
"v_mfma_f32_16x16x32_bf16 v[216:219], v[144:147], v[96:99], v[216:219]\n"
"ds_read_b64_tr_b16 v[162:163], v6 offset:25600\n"
"v_mfma_f32_16x16x32_bf16 v[220:223], v[144:147], v[100:103], v[220:223]\n"
"ds_read_b64_tr_b16 v[164:165], v7 offset:24576\n"
"v_mfma_f32_16x16x32_bf16 v[224:227], v[144:147], v[104:107], v[224:227]\n"
"ds_read_b64_tr_b16 v[166:167], v7 offset:25600\n"
"v_mfma_f32_16x16x32_bf16 v[228:231], v[144:147], v[108:111], v[228:231]\n"
"v_mfma_f32_16x16x32_bf16 v[232:235], v[144:147], v[112:115], v[232:235]\n"
"v_mfma_f32_16x16x32_bf16 v[252:255], v[148:151], v[112:115], v[252:255]\n"
"v_mfma_f32_16x16x32_bf16 v[248:251], v[148:151], v[108:111], v[248:251]\n"
"v_mfma_f32_16x16x32_bf16 v[244:247], v[148:151], v[104:107], v[244:247]\n"
"v_mfma_f32_16x16x32_bf16 v[240:243], v[148:151], v[100:103], v[240:243]\n"
"v_mfma_f32_16x16x32_bf16 v[236:239], v[148:151], v[96:99], v[236:239]\n"
"s_waitcnt lgkmcnt(6)\n"
"v_mfma_f32_16x16x32_bf16 v[176:179], v[152:155], v[116:119], v[176:179]\n"
"v_mfma_f32_16x16x32_bf16 v[180:183], v[152:155], v[120:123], v[180:183]\n"
"v_mfma_f32_16x16x32_bf16 v[184:187], v[152:155], v[124:127], v[184:187]\n"
"s_waitcnt lgkmcnt(0)\n"
"s_barrier\n"
"v_mfma_f32_16x16x32_bf16 v[188:191], v[152:155], v[128:131], v[188:191]\n"
"ds_read_b128 v[96:99], v2 offset:40960\n"
"v_mfma_f32_16x16x32_bf16 v[192:195], v[152:155], v[132:135], v[192:195]\n"
"ds_read_b64_tr_b16 v[136:137], v4 offset:32768\n"
"v_mfma_f32_16x16x32_bf16 v[212:215], v[156:159], v[132:135], v[212:215]\n"
"ds_read_b64_tr_b16 v[138:139], v4 offset:33792\n"
"v_mfma_f32_16x16x32_bf16 v[208:211], v[156:159], v[128:131], v[208:211]\n"
"ds_read_b128 v[100:103], v2 offset:43008\n"
"v_mfma_f32_16x16x32_bf16 v[204:207], v[156:159], v[124:127], v[204:207]\n"
"ds_read_b128 v[104:107], v2 offset:45056\n"
"v_mfma_f32_16x16x32_bf16 v[200:203], v[156:159], v[120:123], v[200:203]\n"
"ds_read_b128 v[108:111], v2 offset:47104\n"
"v_mfma_f32_16x16x32_bf16 v[196:199], v[156:159], v[116:119], v[196:199]\n"
"ds_read_b128 v[112:115], v2 offset:49152\n"
"v_mfma_f32_16x16x32_bf16 v[216:219], v[160:163], v[116:119], v[216:219]\n"
"ds_read_b64_tr_b16 v[140:141], v5 offset:32768\n"
"v_mfma_f32_16x16x32_bf16 v[220:223], v[160:163], v[120:123], v[220:223]\n"
"ds_read_b64_tr_b16 v[142:143], v5 offset:33792\n"
"v_mfma_f32_16x16x32_bf16 v[224:227], v[160:163], v[124:127], v[224:227]\n"
"ds_read_b64_tr_b16 v[144:145], v6 offset:32768\n"
"v_mfma_f32_16x16x32_bf16 v[228:231], v[160:163], v[128:131], v[228:231]\n"
"ds_read_b64_tr_b16 v[146:147], v6 offset:33792\n"
"v_mfma_f32_16x16x32_bf16 v[232:235], v[160:163], v[132:135], v[232:235]\n"
"ds_read_b64_tr_b16 v[148:149], v7 offset:32768\n"
"v_mfma_f32_16x16x32_bf16 v[252:255], v[164:167], v[132:135], v[252:255]\n"
"ds_read_b64_tr_b16 v[150:151], v7 offset:33792\n"
"v_mfma_f32_16x16x32_bf16 v[248:251], v[164:167], v[128:131], v[248:251]\n"
"v_mfma_f32_16x16x32_bf16 v[244:247], v[164:167], v[124:127], v[244:247]\n"
"v_mfma_f32_16x16x32_bf16 v[240:243], v[164:167], v[120:123], v[240:243]\n"
"v_mfma_f32_16x16x32_bf16 v[236:239], v[164:167], v[116:119], v[236:239]\n"
"s_waitcnt lgkmcnt(6)\n"
"v_mfma_f32_16x16x32_bf16 v[176:179], v[136:139], v[96:99], v[176:179]\n"
"ds_read_b128 v[116:119], v3 offset:40960\n"
"v_mfma_f32_16x16x32_bf16 v[180:183], v[136:139], v[100:103], v[180:183]\n"
"ds_read_b64_tr_b16 v[152:153], v4 offset:40960\n"
"v_mfma_f32_16x16x32_bf16 v[184:187], v[136:139], v[104:107], v[184:187]\n"
"ds_read_b64_tr_b16 v[154:155], v4 offset:41984\n"
"v_mfma_f32_16x16x32_bf16 v[188:191], v[136:139], v[108:111], v[188:191]\n"
"ds_read_b128 v[120:123], v3 offset:43008\n"
"v_mfma_f32_16x16x32_bf16 v[192:195], v[136:139], v[112:115], v[192:195]\n"
"ds_read_b128 v[124:127], v3 offset:45056\n"
"s_waitcnt lgkmcnt(5)\n"
"v_mfma_f32_16x16x32_bf16 v[212:215], v[140:143], v[112:115], v[212:215]\n"
"ds_read_b128 v[128:131], v3 offset:47104\n"
"v_mfma_f32_16x16x32_bf16 v[208:211], v[140:143], v[108:111], v[208:211]\n"
"ds_read_b128 v[132:135], v3 offset:49152\n"
"v_mfma_f32_16x16x32_bf16 v[204:207], v[140:143], v[104:107], v[204:207]\n"
"ds_read_b64_tr_b16 v[156:157], v5 offset:40960\n"
"v_mfma_f32_16x16x32_bf16 v[200:203], v[140:143], v[100:103], v[200:203]\n"
"ds_read_b64_tr_b16 v[158:159], v5 offset:41984\n"
"v_mfma_f32_16x16x32_bf16 v[196:199], v[140:143], v[96:99], v[196:199]\n"
"ds_read_b64_tr_b16 v[160:161], v6 offset:40960\n"
"v_mfma_f32_16x16x32_bf16 v[216:219], v[144:147], v[96:99], v[216:219]\n"
"ds_read_b64_tr_b16 v[162:163], v6 offset:41984\n"
"v_mfma_f32_16x16x32_bf16 v[220:223], v[144:147], v[100:103], v[220:223]\n"
"ds_read_b64_tr_b16 v[164:165], v7 offset:40960\n"
"v_mfma_f32_16x16x32_bf16 v[224:227], v[144:147], v[104:107], v[224:227]\n"
"ds_read_b64_tr_b16 v[166:167], v7 offset:41984\n"
"v_mfma_f32_16x16x32_bf16 v[228:231], v[144:147], v[108:111], v[228:231]\n"
"v_mfma_f32_16x16x32_bf16 v[232:235], v[144:147], v[112:115], v[232:235]\n"
"v_mfma_f32_16x16x32_bf16 v[252:255], v[148:151], v[112:115], v[252:255]\n"
"v_mfma_f32_16x16x32_bf16 v[248:251], v[148:151], v[108:111], v[248:251]\n"
"v_mfma_f32_16x16x32_bf16 v[244:247], v[148:151], v[104:107], v[244:247]\n"
"v_mfma_f32_16x16x32_bf16 v[240:243], v[148:151], v[100:103], v[240:243]\n"
"v_mfma_f32_16x16x32_bf16 v[236:239], v[148:151], v[96:99], v[236:239]\n"
"s_waitcnt lgkmcnt(6)\n"
"v_mfma_f32_16x16x32_bf16 v[176:179], v[152:155], v[116:119], v[176:179]\n"
"v_mfma_f32_16x16x32_bf16 v[180:183], v[152:155], v[120:123], v[180:183]\n"
"v_mfma_f32_16x16x32_bf16 v[184:187], v[152:155], v[124:127], v[184:187]\n"
"s_waitcnt lgkmcnt(0)\n"
"s_barrier\n"
"v_mfma_f32_16x16x32_bf16 v[188:191], v[152:155], v[128:131], v[188:191]\n"
"ds_read_b128 v[96:99], v8 offset:0\n"
"v_mfma_f32_16x16x32_bf16 v[192:195], v[152:155], v[132:135], v[192:195]\n"
"ds_read_b64_tr_b16 v[136:137], v4 offset:49152\n"
"v_mfma_f32_16x16x32_bf16 v[212:215], v[156:159], v[132:135], v[212:215]\n"
"ds_read_b64_tr_b16 v[138:139], v4 offset:50176\n"
"v_mfma_f32_16x16x32_bf16 v[208:211], v[156:159], v[128:131], v[208:211]\n"
"ds_read_b128 v[100:103], v8 offset:2048\n"
"v_mfma_f32_16x16x32_bf16 v[204:207], v[156:159], v[124:127], v[204:207]\n"
"ds_read_b128 v[104:107], v8 offset:4096\n"
"v_mfma_f32_16x16x32_bf16 v[200:203], v[156:159], v[120:123], v[200:203]\n"
"ds_read_b128 v[108:111], v8 offset:6144\n"
"v_mfma_f32_16x16x32_bf16 v[196:199], v[156:159], v[116:119], v[196:199]\n"
"ds_read_b128 v[112:115], v8 offset:8192\n"
"v_mfma_f32_16x16x32_bf16 v[216:219], v[160:163], v[116:119], v[216:219]\n"
"ds_read_b64_tr_b16 v[140:141], v5 offset:49152\n"
"v_mfma_f32_16x16x32_bf16 v[220:223], v[160:163], v[120:123], v[220:223]\n"
"ds_read_b64_tr_b16 v[142:143], v5 offset:50176\n"
"v_mfma_f32_16x16x32_bf16 v[224:227], v[160:163], v[124:127], v[224:227]\n"
"ds_read_b64_tr_b16 v[144:145], v6 offset:49152\n"
"v_mfma_f32_16x16x32_bf16 v[228:231], v[160:163], v[128:131], v[228:231]\n"
"ds_read_b64_tr_b16 v[146:147], v6 offset:50176\n"
"v_mfma_f32_16x16x32_bf16 v[232:235], v[160:163], v[132:135], v[232:235]\n"
"ds_read_b64_tr_b16 v[148:149], v7 offset:49152\n"
"v_mfma_f32_16x16x32_bf16 v[252:255], v[164:167], v[132:135], v[252:255]\n"
"ds_read_b64_tr_b16 v[150:151], v7 offset:50176\n"
"v_mfma_f32_16x16x32_bf16 v[248:251], v[164:167], v[128:131], v[248:251]\n"
"v_mfma_f32_16x16x32_bf16 v[244:247], v[164:167], v[124:127], v[244:247]\n"
"v_mfma_f32_16x16x32_bf16 v[240:243], v[164:167], v[120:123], v[240:243]\n"
"v_mfma_f32_16x16x32_bf16 v[236:239], v[164:167], v[116:119], v[236:239]\n"
"s_waitcnt lgkmcnt(6)\n"
"v_mfma_f32_16x16x32_bf16 v[176:179], v[136:139], v[96:99], v[176:179]\n"
"ds_read_b128 v[116:119], v9 offset:0\n"
"v_mfma_f32_16x16x32_bf16 v[180:183], v[136:139], v[100:103], v[180:183]\n"
"ds_read_b64_tr_b16 v[152:153], v4 offset:57344\n"
"v_mfma_f32_16x16x32_bf16 v[184:187], v[136:139], v[104:107], v[184:187]\n"
"ds_read_b64_tr_b16 v[154:155], v4 offset:58368\n"
"v_mfma_f32_16x16x32_bf16 v[188:191], v[136:139], v[108:111], v[188:191]\n"
"ds_read_b128 v[120:123], v9 offset:2048\n"
"v_mfma_f32_16x16x32_bf16 v[192:195], v[136:139], v[112:115], v[192:195]\n"
"ds_read_b128 v[124:127], v9 offset:4096\n"
"s_waitcnt lgkmcnt(5)\n"
"v_mfma_f32_16x16x32_bf16 v[212:215], v[140:143], v[112:115], v[212:215]\n"
"ds_read_b128 v[128:131], v9 offset:6144\n"
"v_mfma_f32_16x16x32_bf16 v[208:211], v[140:143], v[108:111], v[208:211]\n"
"ds_read_b128 v[132:135], v9 offset:8192\n"
"v_mfma_f32_16x16x32_bf16 v[204:207], v[140:143], v[104:107], v[204:207]\n"
"ds_read_b64_tr_b16 v[156:157], v5 offset:57344\n"
"v_mfma_f32_16x16x32_bf16 v[200:203], v[140:143], v[100:103], v[200:203]\n"
"ds_read_b64_tr_b16 v[158:159], v5 offset:58368\n"
"v_mfma_f32_16x16x32_bf16 v[196:199], v[140:143], v[96:99], v[196:199]\n"
"ds_read_b64_tr_b16 v[160:161], v6 offset:57344\n"
"v_mfma_f32_16x16x32_bf16 v[216:219], v[144:147], v[96:99], v[216:219]\n"
"ds_read_b64_tr_b16 v[162:163], v6 offset:58368\n"
"v_mfma_f32_16x16x32_bf16 v[220:223], v[144:147], v[100:103], v[220:223]\n"
"ds_read_b64_tr_b16 v[164:165], v7 offset:57344\n"
"v_mfma_f32_16x16x32_bf16 v[224:227], v[144:147], v[104:107], v[224:227]\n"
"ds_read_b64_tr_b16 v[166:167], v7 offset:58368\n"
"v_mfma_f32_16x16x32_bf16 v[228:231], v[144:147], v[108:111], v[228:231]\n"
"v_mfma_f32_16x16x32_bf16 v[232:235], v[144:147], v[112:115], v[232:235]\n"
"v_mfma_f32_16x16x32_bf16 v[252:255], v[148:151], v[112:115], v[252:255]\n"
"v_mfma_f32_16x16x32_bf16 v[248:251], v[148:151], v[108:111], v[248:251]\n"
"v_mfma_f32_16x16x32_bf16 v[244:247], v[148:151], v[104:107], v[244:247]\n"
"v_mfma_f32_16x16x32_bf16 v[240:243], v[148:151], v[100:103], v[240:243]\n"
"v_mfma_f32_16x16x32_bf16 v[236:239], v[148:151], v[96:99], v[236:239]\n"
"s_waitcnt lgkmcnt(6)\n"
"v_mfma_f32_16x16x32_bf16 v[176:179], v[152:155], v[116:119], v[176:179]\n"
"v_mfma_f32_16x16x32_bf16 v[180:183], v[152:155], v[120:123], v[180:183]\n"
"v_mfma_f32_16x16x32_bf16 v[184:187], v[152:155], v[124:127], v[184:187]\n"
"s_waitcnt lgkmcnt(0)\n"
"s_barrier\n"
"v_mfma_f32_16x16x32_bf16 v[188:191], v[152:155], v[128:131], v[188:191]\n"
"ds_read_b128 v[96:99], v2 offset:0\n"
"v_mfma_f32_16x16x32_bf16 v[192:195], v[152:155], v[132:135], v[192:195]\n"
"ds_read_b64_tr_b16 v[136:137], v4 offset:0\n"
"v_mfma_f32_16x16x32_bf16 v[212:215], v[156:159], v[132:135], v[212:215]\n"
"ds_read_b64_tr_b16 v[138:139], v4 offset:1024\n"
"v_mfma_f32_16x16x32_bf16 v[208:211], v[156:159], v[128:131], v[208:211]\n"
"ds_read_b128 v[100:103], v2 offset:2048\n"
"v_mfma_f32_16x16x32_bf16 v[204:207], v[156:159], v[124:127], v[204:207]\n"
"ds_read_b128 v[104:107], v2 offset:4096\n"
"v_mfma_f32_16x16x32_bf16 v[200:203], v[156:159], v[120:123], v[200:203]\n"
"ds_read_b128 v[108:111], v2 offset:6144\n"
"v_mfma_f32_16x16x32_bf16 v[196:199], v[156:159], v[116:119], v[196:199]\n"
"ds_read_b128 v[112:115], v2 offset:8192\n"
"v_mfma_f32_16x16x32_bf16 v[216:219], v[160:163], v[116:119], v[216:219]\n"
"ds_read_b64_tr_b16 v[140:141], v5 offset:0\n"
"v_mfma_f32_16x16x32_bf16 v[220:223], v[160:163], v[120:123], v[220:223]\n"
"ds_read_b64_tr_b16 v[142:143], v5 offset:1024\n"
"v_mfma_f32_16x16x32_bf16 v[224:227], v[160:163], v[124:127], v[224:227]\n"
"ds_read_b64_tr_b16 v[144:145], v6 offset:0\n"
"v_mfma_f32_16x16x32_bf16 v[228:231], v[160:163], v[128:131], v[228:231]\n"
"ds_read_b64_tr_b16 v[146:147], v6 offset:1024\n"
"v_mfma_f32_16x16x32_bf16 v[232:235], v[160:163], v[132:135], v[232:235]\n"
"ds_read_b64_tr_b16 v[148:149], v7 offset:0\n"
"v_mfma_f32_16x16x32_bf16 v[252:255], v[164:167], v[132:135], v[252:255]\n"
"ds_read_b64_tr_b16 v[150:151], v7 offset:1024\n"
"v_mfma_f32_16x16x32_bf16 v[248:251], v[164:167], v[128:131], v[248:251]\n"
"v_mfma_f32_16x16x32_bf16 v[244:247], v[164:167], v[124:127], v[244:247]\n"
"v_mfma_f32_16x16x32_bf16 v[240:243], v[164:167], v[120:123], v[240:243]\n"
"v_mfma_f32_16x16x32_bf16 v[236:239], v[164:167], v[116:119], v[236:239]\n"
"s_waitcnt lgkmcnt(6)\n"
"v_mfma_f32_16x16x32_bf16 v[176:179], v[136:139], v[96:99], v[176:179]\n"
"ds_read_b128 v[116:119], v3 offset:0\n"
"v_mfma_f32_16x16x32_bf16 v[180:183], v[136:139], v[100:103], v[180:183]\n"
"ds_read_b64_tr_b16 v[152:153], v4 offset:8192\n"
"v_mfma_f32_16x16x32_bf16 v[184:187], v[136:139], v[104:107], v[184:187]\n"
"ds_read_b64_tr_b16 v[154:155], v4 offset:9216\n"
"v_mfma_f32_16x16x32_bf16 v[188:191], v[136:139], v[108:111], v[188:191]\n"
"ds_read_b128 v[120:123], v3 offset:2048\n"
"v_mfma_f32_16x16x32_bf16 v[192:195], v[136:139], v[112:115], v[192:195]\n"
"ds_read_b128 v[124:127], v3 offset:4096\n"
"s_waitcnt lgkmcnt(5)\n"
"v_mfma_f32_16x16x32_bf16 v[212:215], v[140:143], v[112:115], v[212:215]\n"
"ds_read_b128 v[128:131], v3 offset:6144\n"
"v_mfma_f32_16x16x32_bf16 v[208:211], v[140:143], v[108:111], v[208:211]\n"
"ds_read_b128 v[132:135], v3 offset:8192\n"
"v_mfma_f32_16x16x32_bf16 v[204:207], v[140:143], v[104:107], v[204:207]\n"
"ds_read_b64_tr_b16 v[156:157], v5 offset:8192\n"
"v_mfma_f32_16x16x32_bf16 v[200:203], v[140:143], v[100:103], v[200:203]\n"
"ds_read_b64_tr_b16 v[158:159], v5 offset:9216\n"
"v_mfma_f32_16x16x32_bf16 v[196:199], v[140:143], v[96:99], v[196:199]\n"
"ds_read_b64_tr_b16 v[160:161], v6 offset:8192\n"
"v_mfma_f32_16x16x32_bf16 v[216:219], v[144:147], v[96:99], v[216:219]\n"
"ds_read_b64_tr_b16 v[162:163], v6 offset:9216\n"
"v_mfma_f32_16x16x32_bf16 v[220:223], v[144:147], v[100:103], v[220:223]\n"
"ds_read_b64_tr_b16 v[164:165], v7 offset:8192\n"
"v_mfma_f32_16x16x32_bf16 v[224:227], v[144:147], v[104:107], v[224:227]\n"
"ds_read_b64_tr_b16 v[166:167], v7 offset:9216\n"
"v_mfma_f32_16x16x32_bf16 v[228:231], v[144:147], v[108:111], v[228:231]\n"
"v_mfma_f32_16x16x32_bf16 v[232:235], v[144:147], v[112:115], v[232:235]\n"
"v_mfma_f32_16x16x32_bf16 v[252:255], v[148:151], v[112:115], v[252:255]\n"
"v_mfma_f32_16x16x32_bf16 v[248:251], v[148:151], v[108:111], v[248:251]\n"
"v_mfma_f32_16x16x32_bf16 v[244:247], v[148:151], v[104:107], v[244:247]\n"
"v_mfma_f32_16x16x32_bf16 v[240:243], v[148:151], v[100:103], v[240:243]\n"
"v_mfma_f32_16x16x32_bf16 v[236:239], v[148:151], v[96:99], v[236:239]\n"
"s_waitcnt lgkmcnt(6)\n"
"v_mfma_f32_16x16x32_bf16 v[176:179], v[152:155], v[116:119], v[176:179]\n"
"v_mfma_f32_16x16x32_bf16 v[180:183], v[152:155], v[120:123], v[180:183]\n"
"v_mfma_f32_16x16x32_bf16 v[184:187], v[152:155], v[124:127], v[184:187]\n"
"s_waitcnt lgkmcnt(0)\n"
"s_barrier\n"
"v_mfma_f32_16x16x32_bf16 v[188:191], v[152:155], v[128:131], v[188:191]\n"
"ds_read_b128 v[96:99], v2 offset:20480\n"
"v_mfma_f32_16x16x32_bf16 v[192:195], v[152:155], v[132:135], v[192:195]\n"
"ds_read_b64_tr_b16 v[136:137], v4 offset:16384\n"
"v_mfma_f32_16x16x32_bf16 v[212:215], v[156:159], v[132:135], v[212:215]\n"
"ds_read_b64_tr_b16 v[138:139], v4 offset:17408\n"
"v_mfma_f32_16x16x32_bf16 v[208:211], v[156:159], v[128:131], v[208:211]\n"
"ds_read_b128 v[100:103], v2 offset:22528\n"
"v_mfma_f32_16x16x32_bf16 v[204:207], v[156:159], v[124:127], v[204:207]\n"
"ds_read_b128 v[104:107], v2 offset:24576\n"
"v_mfma_f32_16x16x32_bf16 v[200:203], v[156:159], v[120:123], v[200:203]\n"
"ds_read_b128 v[108:111], v2 offset:26624\n"
"v_mfma_f32_16x16x32_bf16 v[196:199], v[156:159], v[116:119], v[196:199]\n"
"ds_read_b128 v[112:115], v2 offset:28672\n"
"v_mfma_f32_16x16x32_bf16 v[216:219], v[160:163], v[116:119], v[216:219]\n"
"ds_read_b64_tr_b16 v[140:141], v5 offset:16384\n"
"v_mfma_f32_16x16x32_bf16 v[220:223], v[160:163], v[120:123], v[220:223]\n"
"ds_read_b64_tr_b16 v[142:143], v5 offset:17408\n"
"v_mfma_f32_16x16x32_bf16 v[224:227], v[160:163], v[124:127], v[224:227]\n"
"ds_read_b64_tr_b16 v[144:145], v6 offset:16384\n"
"v_mfma_f32_16x16x32_bf16 v[228:231], v[160:163], v[128:131], v[228:231]\n"
"ds_read_b64_tr_b16 v[146:147], v6 offset:17408\n"
"v_mfma_f32_16x16x32_bf16 v[232:235], v[160:163], v[132:135], v[232:235]\n"
"ds_read_b64_tr_b16 v[148:149], v7 offset:16384\n"
"v_mfma_f32_16x16x32_bf16 v[252:255], v[164:167], v[132:135], v[252:255]\n"
"ds_read_b64_tr_b16 v[150:151], v7 offset:17408\n"
"v_mfma_f32_16x16x32_bf16 v[248:251], v[164:167], v[128:131], v[248:251]\n"
"v_mfma_f32_16x16x32_bf16 v[244:247], v[164:167], v[124:127], v[244:247]\n"
"v_mfma_f32_16x16x32_bf16 v[240:243], v[164:167], v[120:123], v[240:243]\n"
"v_mfma_f32_16x16x32_bf16 v[236:239], v[164:167], v[116:119], v[236:239]\n"
"s_waitcnt lgkmcnt(6)\n"
"v_mfma_f32_16x16x32_bf16 v[176:179], v[136:139], v[96:99], v[176:179]\n"
"ds_read_b128 v[116:119], v3 offset:20480\n"
"v_mfma_f32_16x16x32_bf16 v[180:183], v[136:139], v[100:103], v[180:183]\n"
"ds_read_b64_tr_b16 v[152:153], v4 offset:24576\n"
"v_mfma_f32_16x16x32_bf16 v[184:187], v[136:139], v[104:107], v[184:187]\n"
"ds_read_b64_tr_b16 v[154:155], v4 offset:25600\n"
"v_mfma_f32_16x16x32_bf16 v[188:191], v[136:139], v[108:111], v[188:191]\n"
"ds_read_b128 v[120:123], v3 offset:22528\n"
"v_mfma_f32_16x16x32_bf16 v[192:195], v[136:139], v[112:115], v[192:195]\n"
"ds_read_b128 v[124:127], v3 offset:24576\n"
"s_waitcnt lgkmcnt(5)\n"
"v_mfma_f32_16x16x32_bf16 v[212:215], v[140:143], v[112:115], v[212:215]\n"
"ds_read_b128 v[128:131], v3 offset:26624\n"
"v_mfma_f32_16x16x32_bf16 v[208:211], v[140:143], v[108:111], v[208:211]\n"
"ds_read_b128 v[132:135], v3 offset:28672\n"
"v_mfma_f32_16x16x32_bf16 v[204:207], v[140:143], v[104:107], v[204:207]\n"
"ds_read_b64_tr_b16 v[156:157], v5 offset:24576\n"
"v_mfma_f32_16x16x32_bf16 v[200:203], v[140:143], v[100:103], v[200:203]\n"
"ds_read_b64_tr_b16 v[158:159], v5 offset:25600\n"
"v_mfma_f32_16x16x32_bf16 v[196:199], v[140:143], v[96:99], v[196:199]\n"
"ds_read_b64_tr_b16 v[160:161], v6 offset:24576\n"
"v_mfma_f32_16x16x32_bf16 v[216:219], v[144:147], v[96:99], v[216:219]\n"
"ds_read_b64_tr_b16 v[162:163], v6 offset:25600\n"
"v_mfma_f32_16x16x32_bf16 v[220:223], v[144:147], v[100:103], v[220:223]\n"
"ds_read_b64_tr_b16 v[164:165], v7 offset:24576\n"
"v_mfma_f32_16x16x32_bf16 v[224:227], v[144:147], v[104:107], v[224:227]\n"
"ds_read_b64_tr_b16 v[166:167], v7 offset:25600\n"
"v_mfma_f32_16x16x32_bf16 v[228:231], v[144:147], v[108:111], v[228:231]\n"
"v_mfma_f32_16x16x32_bf16 v[232:235], v[144:147], v[112:115], v[232:235]\n"
"v_mfma_f32_16x16x32_bf16 v[252:255], v[148:151], v[112:115], v[252:255]\n"
"v_mfma_f32_16x16x32_bf16 v[248:251], v[148:151], v[108:111], v[248:251]\n"
"v_mfma_f32_16x16x32_bf16 v[244:247], v[148:151], v[104:107], v[244:247]\n"
"v_mfma_f32_16x16x32_bf16 v[240:243], v[148:151], v[100:103], v[240:243]\n"
"v_mfma_f32_16x16x32_bf16 v[236:239], v[148:151], v[96:99], v[236:239]\n"
"s_waitcnt lgkmcnt(6)\n"
"v_mfma_f32_16x16x32_bf16 v[176:179], v[152:155], v[116:119], v[176:179]\n"
"v_mfma_f32_16x16x32_bf16 v[180:183], v[152:155], v[120:123], v[180:183]\n"
"v_mfma_f32_16x16x32_bf16 v[184:187], v[152:155], v[124:127], v[184:187]\n"
"s_waitcnt lgkmcnt(0)\n"
"s_barrier\n"
"v_mfma_f32_16x16x32_bf16 v[188:191], v[152:155], v[128:131], v[188:191]\n"
"ds_read_b128 v[96:99], v2 offset:40960\n"
"v_mfma_f32_16x16x32_bf16 v[192:195], v[152:155], v[132:135], v[192:195]\n"
"ds_read_b64_tr_b16 v[136:137], v4 offset:32768\n"
"v_mfma_f32_16x16x32_bf16 v[212:215], v[156:159], v[132:135], v[212:215]\n"
"ds_read_b64_tr_b16 v[138:139], v4 offset:33792\n"
"v_mfma_f32_16x16x32_bf16 v[208:211], v[156:159], v[128:131], v[208:211]\n"
"ds_read_b128 v[100:103], v2 offset:43008\n"
"v_mfma_f32_16x16x32_bf16 v[204:207], v[156:159], v[124:127], v[204:207]\n"
"ds_read_b128 v[104:107], v2 offset:45056\n"
"v_mfma_f32_16x16x32_bf16 v[200:203], v[156:159], v[120:123], v[200:203]\n"
"ds_read_b128 v[108:111], v2 offset:47104\n"
"v_mfma_f32_16x16x32_bf16 v[196:199], v[156:159], v[116:119], v[196:199]\n"
"ds_read_b128 v[112:115], v2 offset:49152\n"
"v_mfma_f32_16x16x32_bf16 v[216:219], v[160:163], v[116:119], v[216:219]\n"
"ds_read_b64_tr_b16 v[140:141], v5 offset:32768\n"
"v_mfma_f32_16x16x32_bf16 v[220:223], v[160:163], v[120:123], v[220:223]\n"
"ds_read_b64_tr_b16 v[142:143], v5 offset:33792\n"
"v_mfma_f32_16x16x32_bf16 v[224:227], v[160:163], v[124:127], v[224:227]\n"
"ds_read_b64_tr_b16 v[144:145], v6 offset:32768\n"
"v_mfma_f32_16x16x32_bf16 v[228:231], v[160:163], v[128:131], v[228:231]\n"
"ds_read_b64_tr_b16 v[146:147], v6 offset:33792\n"
"v_mfma_f32_16x16x32_bf16 v[232:235], v[160:163], v[132:135], v[232:235]\n"
"ds_read_b64_tr_b16 v[148:149], v7 offset:32768\n"
"v_mfma_f32_16x16x32_bf16 v[252:255], v[164:167], v[132:135], v[252:255]\n"
"ds_read_b64_tr_b16 v[150:151], v7 offset:33792\n"
"v_mfma_f32_16x16x32_bf16 v[248:251], v[164:167], v[128:131], v[248:251]\n"
"v_mfma_f32_16x16x32_bf16 v[244:247], v[164:167], v[124:127], v[244:247]\n"
"v_mfma_f32_16x16x32_bf16 v[240:243], v[164:167], v[120:123], v[240:243]\n"
"v_mfma_f32_16x16x32_bf16 v[236:239], v[164:167], v[116:119], v[236:239]\n"
"s_waitcnt lgkmcnt(6)\n"
"v_mfma_f32_16x16x32_bf16 v[176:179], v[136:139], v[96:99], v[176:179]\n"
"ds_read_b128 v[116:119], v3 offset:40960\n"
"v_mfma_f32_16x16x32_bf16 v[180:183], v[136:139], v[100:103], v[180:183]\n"
"ds_read_b64_tr_b16 v[152:153], v4 offset:40960\n"
"v_mfma_f32_16x16x32_bf16 v[184:187], v[136:139], v[104:107], v[184:187]\n"
"ds_read_b64_tr_b16 v[154:155], v4 offset:41984\n"
"v_mfma_f32_16x16x32_bf16 v[188:191], v[136:139], v[108:111], v[188:191]\n"
"ds_read_b128 v[120:123], v3 offset:43008\n"
"v_mfma_f32_16x16x32_bf16 v[192:195], v[136:139], v[112:115], v[192:195]\n"
"ds_read_b128 v[124:127], v3 offset:45056\n"
"s_waitcnt lgkmcnt(5)\n"
"v_mfma_f32_16x16x32_bf16 v[212:215], v[140:143], v[112:115], v[212:215]\n"
"ds_read_b128 v[128:131], v3 offset:47104\n"
"v_mfma_f32_16x16x32_bf16 v[208:211], v[140:143], v[108:111], v[208:211]\n"
"ds_read_b128 v[132:135], v3 offset:49152\n"
"v_mfma_f32_16x16x32_bf16 v[204:207], v[140:143], v[104:107], v[204:207]\n"
"ds_read_b64_tr_b16 v[156:157], v5 offset:40960\n"
"v_mfma_f32_16x16x32_bf16 v[200:203], v[140:143], v[100:103], v[200:203]\n"
"ds_read_b64_tr_b16 v[158:159], v5 offset:41984\n"
"v_mfma_f32_16x16x32_bf16 v[196:199], v[140:143], v[96:99], v[196:199]\n"
"ds_read_b64_tr_b16 v[160:161], v6 offset:40960\n"
"v_mfma_f32_16x16x32_bf16 v[216:219], v[144:147], v[96:99], v[216:219]\n"
"ds_read_b64_tr_b16 v[162:163], v6 offset:41984\n"
"v_mfma_f32_16x16x32_bf16 v[220:223], v[144:147], v[100:103], v[220:223]\n"
"ds_read_b64_tr_b16 v[164:165], v7 offset:40960\n"
"v_mfma_f32_16x16x32_bf16 v[224:227], v[144:147], v[104:107], v[224:227]\n"
"ds_read_b64_tr_b16 v[166:167], v7 offset:41984\n"
"v_mfma_f32_16x16x32_bf16 v[228:231], v[144:147], v[108:111], v[228:231]\n"
"v_mfma_f32_16x16x32_bf16 v[232:235], v[144:147], v[112:115], v[232:235]\n"
"v_mfma_f32_16x16x32_bf16 v[252:255], v[148:151], v[112:115], v[252:255]\n"
"v_mfma_f32_16x16x32_bf16 v[248:251], v[148:151], v[108:111], v[248:251]\n"
"v_mfma_f32_16x16x32_bf16 v[244:247], v[148:151], v[104:107], v[244:247]\n"
"v_mfma_f32_16x16x32_bf16 v[240:243], v[148:151], v[100:103], v[240:243]\n"
"v_mfma_f32_16x16x32_bf16 v[236:239], v[148:151], v[96:99], v[236:239]\n"
"s_waitcnt lgkmcnt(6)\n"
"v_mfma_f32_16x16x32_bf16 v[176:179], v[152:155], v[116:119], v[176:179]\n"
"v_mfma_f32_16x16x32_bf16 v[180:183], v[152:155], v[120:123], v[180:183]\n"
"v_mfma_f32_16x16x32_bf16 v[184:187], v[152:155], v[124:127], v[184:187]\n"
"s_waitcnt lgkmcnt(0)\n"
"s_barrier\n"
"v_mfma_f32_16x16x32_bf16 v[188:191], v[152:155], v[128:131], v[188:191]\n"
"ds_read_b128 v[96:99], v8 offset:0\n"
"v_mfma_f32_16x16x32_bf16 v[192:195], v[152:155], v[132:135], v[192:195]\n"
"ds_read_b64_tr_b16 v[136:137], v4 offset:49152\n"
"v_mfma_f32_16x16x32_bf16 v[212:215], v[156:159], v[132:135], v[212:215]\n"
"ds_read_b64_tr_b16 v[138:139], v4 offset:50176\n"
"v_mfma_f32_16x16x32_bf16 v[208:211], v[156:159], v[128:131], v[208:211]\n"
"ds_read_b128 v[100:103], v8 offset:2048\n"
"v_mfma_f32_16x16x32_bf16 v[204:207], v[156:159], v[124:127], v[204:207]\n"
"ds_read_b128 v[104:107], v8 offset:4096\n"
"v_mfma_f32_16x16x32_bf16 v[200:203], v[156:159], v[120:123], v[200:203]\n"
"ds_read_b128 v[108:111], v8 offset:6144\n"
"v_mfma_f32_16x16x32_bf16 v[196:199], v[156:159], v[116:119], v[196:199]\n"
"ds_read_b128 v[112:115], v8 offset:8192\n"
"v_mfma_f32_16x16x32_bf16 v[216:219], v[160:163], v[116:119], v[216:219]\n"
"ds_read_b64_tr_b16 v[140:141], v5 offset:49152\n"
"v_mfma_f32_16x16x32_bf16 v[220:223], v[160:163], v[120:123], v[220:223]\n"
"ds_read_b64_tr_b16 v[142:143], v5 offset:50176\n"
"v_mfma_f32_16x16x32_bf16 v[224:227], v[160:163], v[124:127], v[224:227]\n"
"ds_read_b64_tr_b16 v[144:145], v6 offset:49152\n"
"v_mfma_f32_16x16x32_bf16 v[228:231], v[160:163], v[128:131], v[228:231]\n"
"ds_read_b64_tr_b16 v[146:147], v6 offset:50176\n"
"v_mfma_f32_16x16x32_bf16 v[232:235], v[160:163], v[132:135], v[232:235]\n"
"ds_read_b64_tr_b16 v[148:149], v7 offset:49152\n"
"v_mfma_f32_16x16x32_bf16 v[252:255], v[164:167], v[132:135], v[252:255]\n"
"ds_read_b64_tr_b16 v[150:151], v7 offset:50176\n"
"v_mfma_f32_16x16x32_bf16 v[248:251], v[164:167], v[128:131], v[248:251]\n"
"v_mfma_f32_16x16x32_bf16 v[244:247], v[164:167], v[124:127], v[244:247]\n"
"v_mfma_f32_16x16x32_bf16 v[240:243], v[164:167], v[120:123], v[240:243]\n"
"v_mfma_f32_16x16x32_bf16 v[236:239], v[164:167], v[116:119], v[236:239]\n"
"s_waitcnt lgkmcnt(6)\n"
"v_mfma_f32_16x16x32_bf16 v[176:179], v[136:139], v[96:99], v[176:179]\n"
"ds_read_b128 v[116:119], v9 offset:0\n"
"v_mfma_f32_16x16x32_bf16 v[180:183], v[136:139], v[100:103], v[180:183]\n"
"ds_read_b64_tr_b16 v[152:153], v4 offset:57344\n"
"v_mfma_f32_16x16x32_bf16 v[184:187], v[136:139], v[104:107], v[184:187]\n"
"ds_read_b64_tr_b16 v[154:155], v4 offset:58368\n"
"v_mfma_f32_16x16x32_bf16 v[188:191], v[136:139], v[108:111], v[188:191]\n"
"ds_read_b128 v[120:123], v9 offset:2048\n"
"v_mfma_f32_16x16x32_bf16 v[192:195], v[136:139], v[112:115], v[192:195]\n"
"ds_read_b128 v[124:127], v9 offset:4096\n"
"s_waitcnt lgkmcnt(5)\n"
"v_mfma_f32_16x16x32_bf16 v[212:215], v[140:143], v[112:115], v[212:215]\n"
"ds_read_b128 v[128:131], v9 offset:6144\n"
"v_mfma_f32_16x16x32_bf16 v[208:211], v[140:143], v[108:111], v[208:211]\n"
"ds_read_b128 v[132:135], v9 offset:8192\n"
"v_mfma_f32_16x16x32_bf16 v[204:207], v[140:143], v[104:107], v[204:207]\n"
"ds_read_b64_tr_b16 v[156:157], v5 offset:57344\n"
"v_mfma_f32_16x16x32_bf16 v[200:203], v[140:143], v[100:103], v[200:203]\n"
"ds_read_b64_tr_b16 v[158:159], v5 offset:58368\n"
"v_mfma_f32_16x16x32_bf16 v[196:199], v[140:143], v[96:99], v[196:199]\n"
"ds_read_b64_tr_b16 v[160:161], v6 offset:57344\n"
"v_mfma_f32_16x16x32_bf16 v[216:219], v[144:147], v[96:99], v[216:219]\n"
"ds_read_b64_tr_b16 v[162:163], v6 offset:58368\n"
"v_mfma_f32_16x16x32_bf16 v[220:223], v[144:147], v[100:103], v[220:223]\n"
"ds_read_b64_tr_b16 v[164:165], v7 offset:57344\n"
"v_mfma_f32_16x16x32_bf16 v[224:227], v[144:147], v[104:107], v[224:227]\n"
"ds_read_b64_tr_b16 v[166:167], v7 offset:58368\n"
"v_mfma_f32_16x16x32_bf16 v[228:231], v[144:147], v[108:111], v[228:231]\n"
"v_mfma_f32_16x16x32_bf16 v[232:235], v[144:147], v[112:115], v[232:235]\n"
"v_mfma_f32_16x16x32_bf16 v[252:255], v[148:151], v[112:115], v[252:255]\n"
"v_mfma_f32_16x16x32_bf16 v[248:251], v[148:151], v[108:111], v[248:251]\n"
"v_mfma_f32_16x16x32_bf16 v[244:247], v[148:151], v[104:107], v[244:247]\n"
"v_mfma_f32_16x16x32_bf16 v[240:243], v[148:151], v[100:103], v[240:243]\n"
"v_mfma_f32_16x16x32_bf16 v[236:239], v[148:151], v[96:99], v[236:239]\n"
"s_waitcnt lgkmcnt(6)\n"
"v_mfma_f32_16x16x32_bf16 v[176:179], v[152:155], v[116:119], v[176:179]\n"
"v_mfma_f32_16x16x32_bf16 v[180:183], v[152:155], v[120:123], v[180:183]\n"
"v_mfma_f32_16x16x32_bf16 v[184:187], v[152:155], v[124:127], v[184:187]\n"
"s_waitcnt lgkmcnt(0)\n"
"s_barrier\n"
"v_mfma_f32_16x16x32_bf16 v[188:191], v[152:155], v[128:131], v[188:191]\n"
"ds_read_b128 v[96:99], v2 offset:0\n"
"v_mfma_f32_16x16x32_bf16 v[192:195], v[152:155], v[132:135], v[192:195]\n"
"ds_read_b64_tr_b16 v[136:137], v4 offset:0\n"
"v_mfma_f32_16x16x32_bf16 v[212:215], v[156:159], v[132:135], v[212:215]\n"
"ds_read_b64_tr_b16 v[138:139], v4 offset:1024\n"
"v_mfma_f32_16x16x32_bf16 v[208:211], v[156:159], v[128:131], v[208:211]\n"
"ds_read_b128 v[100:103], v2 offset:2048\n"
"v_mfma_f32_16x16x32_bf16 v[204:207], v[156:159], v[124:127], v[204:207]\n"
"ds_read_b128 v[104:107], v2 offset:4096\n"
"v_mfma_f32_16x16x32_bf16 v[200:203], v[156:159], v[120:123], v[200:203]\n"
"ds_read_b128 v[108:111], v2 offset:6144\n"
"v_mfma_f32_16x16x32_bf16 v[196:199], v[156:159], v[116:119], v[196:199]\n"
"ds_read_b128 v[112:115], v2 offset:8192\n"
"v_mfma_f32_16x16x32_bf16 v[216:219], v[160:163], v[116:119], v[216:219]\n"
"ds_read_b64_tr_b16 v[140:141], v5 offset:0\n"
"v_mfma_f32_16x16x32_bf16 v[220:223], v[160:163], v[120:123], v[220:223]\n"
"ds_read_b64_tr_b16 v[142:143], v5 offset:1024\n"
"v_mfma_f32_16x16x32_bf16 v[224:227], v[160:163], v[124:127], v[224:227]\n"
"ds_read_b64_tr_b16 v[144:145], v6 offset:0\n"
"v_mfma_f32_16x16x32_bf16 v[228:231], v[160:163], v[128:131], v[228:231]\n"
"ds_read_b64_tr_b16 v[146:147], v6 offset:1024\n"
"v_mfma_f32_16x16x32_bf16 v[232:235], v[160:163], v[132:135], v[232:235]\n"
"ds_read_b64_tr_b16 v[148:149], v7 offset:0\n"
"v_mfma_f32_16x16x32_bf16 v[252:255], v[164:167], v[132:135], v[252:255]\n"
"ds_read_b64_tr_b16 v[150:151], v7 offset:1024\n"
"v_mfma_f32_16x16x32_bf16 v[248:251], v[164:167], v[128:131], v[248:251]\n"
"v_mfma_f32_16x16x32_bf16 v[244:247], v[164:167], v[124:127], v[244:247]\n"
"v_mfma_f32_16x16x32_bf16 v[240:243], v[164:167], v[120:123], v[240:243]\n"
"v_mfma_f32_16x16x32_bf16 v[236:239], v[164:167], v[116:119], v[236:239]\n"
"s_waitcnt lgkmcnt(6)\n"
"v_mfma_f32_16x16x32_bf16 v[176:179], v[136:139], v[96:99], v[176:179]\n"
"ds_read_b128 v[116:119], v3 offset:0\n"
"v_mfma_f32_16x16x32_bf16 v[180:183], v[136:139], v[100:103], v[180:183]\n"
"ds_read_b64_tr_b16 v[152:153], v4 offset:8192\n"
"v_mfma_f32_16x16x32_bf16 v[184:187], v[136:139], v[104:107], v[184:187]\n"
"ds_read_b64_tr_b16 v[154:155], v4 offset:9216\n"
"v_mfma_f32_16x16x32_bf16 v[188:191], v[136:139], v[108:111], v[188:191]\n"
"ds_read_b128 v[120:123], v3 offset:2048\n"
"v_mfma_f32_16x16x32_bf16 v[192:195], v[136:139], v[112:115], v[192:195]\n"
"ds_read_b128 v[124:127], v3 offset:4096\n"
"s_waitcnt lgkmcnt(5)\n"
"v_mfma_f32_16x16x32_bf16 v[212:215], v[140:143], v[112:115], v[212:215]\n"
"ds_read_b128 v[128:131], v3 offset:6144\n"
"v_mfma_f32_16x16x32_bf16 v[208:211], v[140:143], v[108:111], v[208:211]\n"
"ds_read_b128 v[132:135], v3 offset:8192\n"
"v_mfma_f32_16x16x32_bf16 v[204:207], v[140:143], v[104:107], v[204:207]\n"
"ds_read_b64_tr_b16 v[156:157], v5 offset:8192\n"
"v_mfma_f32_16x16x32_bf16 v[200:203], v[140:143], v[100:103], v[200:203]\n"
"ds_read_b64_tr_b16 v[158:159], v5 offset:9216\n"
"v_mfma_f32_16x16x32_bf16 v[196:199], v[140:143], v[96:99], v[196:199]\n"
"ds_read_b64_tr_b16 v[160:161], v6 offset:8192\n"
"v_mfma_f32_16x16x32_bf16 v[216:219], v[144:147], v[96:99], v[216:219]\n"
"ds_read_b64_tr_b16 v[162:163], v6 offset:9216\n"
"v_mfma_f32_16x16x32_bf16 v[220:223], v[144:147], v[100:103], v[220:223]\n"
"ds_read_b64_tr_b16 v[164:165], v7 offset:8192\n"
"v_mfma_f32_16x16x32_bf16 v[224:227], v[144:147], v[104:107], v[224:227]\n"
"ds_read_b64_tr_b16 v[166:167], v7 offset:9216\n"
"v_mfma_f32_16x16x32_bf16 v[228:231], v[144:147], v[108:111], v[228:231]\n"
"v_mfma_f32_16x16x32_bf16 v[232:235], v[144:147], v[112:115], v[232:235]\n"
"v_mfma_f32_16x16x32_bf16 v[252:255], v[148:151], v[112:115], v[252:255]\n"
"v_mfma_f32_16x16x32_bf16 v[248:251], v[148:151], v[108:111], v[248:251]\n"
"v_mfma_f32_16x16x32_bf16 v[244:247], v[148:151], v[104:107], v[244:247]\n"
"v_mfma_f32_16x16x32_bf16 v[240:243], v[148:151], v[100:103], v[240:243]\n"
"v_mfma_f32_16x16x32_bf16 v[236:239], v[148:151], v[96:99], v[236:239]\n"
"s_waitcnt lgkmcnt(6)\n"
"v_mfma_f32_16x16x32_bf16 v[176:179], v[152:155], v[116:119], v[176:179]\n"
"v_mfma_f32_16x16x32_bf16 v[180:183], v[152:155], v[120:123], v[180:183]\n"
"v_mfma_f32_16x16x32_bf16 v[184:187], v[152:155], v[124:127], v[184:187]\n"
"s_waitcnt lgkmcnt(0)\n"
"s_barrier\n"
"v_mfma_f32_16x16x32_bf16 v[188:191], v[152:155], v[128:131], v[188:191]\n"
"ds_read_b128 v[96:99], v2 offset:20480\n"
"v_mfma_f32_16x16x32_bf16 v[192:195], v[152:155], v[132:135], v[192:195]\n"
"ds_read_b64_tr_b16 v[136:137], v4 offset:16384\n"
"v_mfma_f32_16x16x32_bf16 v[212:215], v[156:159], v[132:135], v[212:215]\n"
"ds_read_b64_tr_b16 v[138:139], v4 offset:17408\n"
"v_mfma_f32_16x16x32_bf16 v[208:211], v[156:159], v[128:131], v[208:211]\n"
"ds_read_b128 v[100:103], v2 offset:22528\n"
"v_mfma_f32_16x16x32_bf16 v[204:207], v[156:159], v[124:127], v[204:207]\n"
"ds_read_b128 v[104:107], v2 offset:24576\n"
"v_mfma_f32_16x16x32_bf16 v[200:203], v[156:159], v[120:123], v[200:203]\n"
"ds_read_b128 v[108:111], v2 offset:26624\n"
"v_mfma_f32_16x16x32_bf16 v[196:199], v[156:159], v[116:119], v[196:199]\n"
"ds_read_b128 v[112:115], v2 offset:28672\n"
"v_mfma_f32_16x16x32_bf16 v[216:219], v[160:163], v[116:119], v[216:219]\n"
"ds_read_b64_tr_b16 v[140:141], v5 offset:16384\n"
"v_mfma_f32_16x16x32_bf16 v[220:223], v[160:163], v[120:123], v[220:223]\n"
"ds_read_b64_tr_b16 v[142:143], v5 offset:17408\n"
"v_mfma_f32_16x16x32_bf16 v[224:227], v[160:163], v[124:127], v[224:227]\n"
"ds_read_b64_tr_b16 v[144:145], v6 offset:16384\n"
"v_mfma_f32_16x16x32_bf16 v[228:231], v[160:163], v[128:131], v[228:231]\n"
"ds_read_b64_tr_b16 v[146:147], v6 offset:17408\n"
"v_mfma_f32_16x16x32_bf16 v[232:235], v[160:163], v[132:135], v[232:235]\n"
"ds_read_b64_tr_b16 v[148:149], v7 offset:16384\n"
"v_mfma_f32_16x16x32_bf16 v[252:255], v[164:167], v[132:135], v[252:255]\n"
"ds_read_b64_tr_b16 v[150:151], v7 offset:17408\n"
"v_mfma_f32_16x16x32_bf16 v[248:251], v[164:167], v[128:131], v[248:251]\n"
"v_mfma_f32_16x16x32_bf16 v[244:247], v[164:167], v[124:127], v[244:247]\n"
"v_mfma_f32_16x16x32_bf16 v[240:243], v[164:167], v[120:123], v[240:243]\n"
"v_mfma_f32_16x16x32_bf16 v[236:239], v[164:167], v[116:119], v[236:239]\n"
"s_waitcnt lgkmcnt(6)\n"
"v_mfma_f32_16x16x32_bf16 v[176:179], v[136:139], v[96:99], v[176:179]\n"
"ds_read_b128 v[116:119], v3 offset:20480\n"
"v_mfma_f32_16x16x32_bf16 v[180:183], v[136:139], v[100:103], v[180:183]\n"
"ds_read_b64_tr_b16 v[152:153], v4 offset:24576\n"
"v_mfma_f32_16x16x32_bf16 v[184:187], v[136:139], v[104:107], v[184:187]\n"
"ds_read_b64_tr_b16 v[154:155], v4 offset:25600\n"
"v_mfma_f32_16x16x32_bf16 v[188:191], v[136:139], v[108:111], v[188:191]\n"
"ds_read_b128 v[120:123], v3 offset:22528\n"
"v_mfma_f32_16x16x32_bf16 v[192:195], v[136:139], v[112:115], v[192:195]\n"
"ds_read_b128 v[124:127], v3 offset:24576\n"
"s_waitcnt lgkmcnt(5)\n"
"v_mfma_f32_16x16x32_bf16 v[212:215], v[140:143], v[112:115], v[212:215]\n"
"ds_read_b128 v[128:131], v3 offset:26624\n"
"v_mfma_f32_16x16x32_bf16 v[208:211], v[140:143], v[108:111], v[208:211]\n"
"ds_read_b128 v[132:135], v3 offset:28672\n"
"v_mfma_f32_16x16x32_bf16 v[204:207], v[140:143], v[104:107], v[204:207]\n"
"ds_read_b64_tr_b16 v[156:157], v5 offset:24576\n"
"v_mfma_f32_16x16x32_bf16 v[200:203], v[140:143], v[100:103], v[200:203]\n"
"ds_read_b64_tr_b16 v[158:159], v5 offset:25600\n"
"v_mfma_f32_16x16x32_bf16 v[196:199], v[140:143], v[96:99], v[196:199]\n"
"ds_read_b64_tr_b16 v[160:161], v6 offset:24576\n"
"v_mfma_f32_16x16x32_bf16 v[216:219], v[144:147], v[96:99], v[216:219]\n"
"ds_read_b64_tr_b16 v[162:163], v6 offset:25600\n"
"v_mfma_f32_16x16x32_bf16 v[220:223], v[144:147], v[100:103], v[220:223]\n"
"ds_read_b64_tr_b16 v[164:165], v7 offset:24576\n"
"v_mfma_f32_16x16x32_bf16 v[224:227], v[144:147], v[104:107], v[224:227]\n"
"ds_read_b64_tr_b16 v[166:167], v7 offset:25600\n"
"v_mfma_f32_16x16x32_bf16 v[228:231], v[144:147], v[108:111], v[228:231]\n"
"v_mfma_f32_16x16x32_bf16 v[232:235], v[144:147], v[112:115], v[232:235]\n"
"v_mfma_f32_16x16x32_bf16 v[252:255], v[148:151], v[112:115], v[252:255]\n"
"v_mfma_f32_16x16x32_bf16 v[248:251], v[148:151], v[108:111], v[248:251]\n"
"v_mfma_f32_16x16x32_bf16 v[244:247], v[148:151], v[104:107], v[244:247]\n"
"v_mfma_f32_16x16x32_bf16 v[240:243], v[148:151], v[100:103], v[240:243]\n"
"v_mfma_f32_16x16x32_bf16 v[236:239], v[148:151], v[96:99], v[236:239]\n"
"s_waitcnt lgkmcnt(6)\n"
"v_mfma_f32_16x16x32_bf16 v[176:179], v[152:155], v[116:119], v[176:179]\n"
"v_mfma_f32_16x16x32_bf16 v[180:183], v[152:155], v[120:123], v[180:183]\n"
"v_mfma_f32_16x16x32_bf16 v[184:187], v[152:155], v[124:127], v[184:187]\n"
"s_waitcnt lgkmcnt(0)\n"
"s_barrier\n"
"v_mfma_f32_16x16x32_bf16 v[188:191], v[152:155], v[128:131], v[188:191]\n"
"ds_read_b128 v[96:99], v2 offset:40960\n"
"v_mfma_f32_16x16x32_bf16 v[192:195], v[152:155], v[132:135], v[192:195]\n"
"ds_read_b64_tr_b16 v[136:137], v4 offset:32768\n"
"v_mfma_f32_16x16x32_bf16 v[212:215], v[156:159], v[132:135], v[212:215]\n"
"ds_read_b64_tr_b16 v[138:139], v4 offset:33792\n"
"v_mfma_f32_16x16x32_bf16 v[208:211], v[156:159], v[128:131], v[208:211]\n"
"ds_read_b128 v[100:103], v2 offset:43008\n"
"v_mfma_f32_16x16x32_bf16 v[204:207], v[156:159], v[124:127], v[204:207]\n"
"ds_read_b128 v[104:107], v2 offset:45056\n"
"v_mfma_f32_16x16x32_bf16 v[200:203], v[156:159], v[120:123], v[200:203]\n"
"ds_read_b128 v[108:111], v2 offset:47104\n"
"v_mfma_f32_16x16x32_bf16 v[196:199], v[156:159], v[116:119], v[196:199]\n"
"ds_read_b128 v[112:115], v2 offset:49152\n"
"v_mfma_f32_16x16x32_bf16 v[216:219], v[160:163], v[116:119], v[216:219]\n"
"ds_read_b64_tr_b16 v[140:141], v5 offset:32768\n"
"v_mfma_f32_16x16x32_bf16 v[220:223], v[160:163], v[120:123], v[220:223]\n"
"ds_read_b64_tr_b16 v[142:143], v5 offset:33792\n"
"v_mfma_f32_16x16x32_bf16 v[224:227], v[160:163], v[124:127], v[224:227]\n"
"v_mfma_f32_16x16x32_bf16 v[228:231], v[160:163], v[128:131], v[228:231]\n"
"v_mfma_f32_16x16x32_bf16 v[232:235], v[160:163], v[132:135], v[232:235]\n"
"v_mfma_f32_16x16x32_bf16 v[252:255], v[164:167], v[132:135], v[252:255]\n"
"v_mfma_f32_16x16x32_bf16 v[248:251], v[164:167], v[128:131], v[248:251]\n"
"v_mfma_f32_16x16x32_bf16 v[244:247], v[164:167], v[124:127], v[244:247]\n"
"v_mfma_f32_16x16x32_bf16 v[240:243], v[164:167], v[120:123], v[240:243]\n"
"v_mfma_f32_16x16x32_bf16 v[236:239], v[164:167], v[116:119], v[236:239]\n"
"s_waitcnt lgkmcnt(6)\n"
"v_mfma_f32_16x16x32_bf16 v[176:179], v[136:139], v[96:99], v[176:179]\n"
"ds_read_b128 v[116:119], v3 offset:40960\n"
"ds_read_b64_tr_b16 v[152:153], v4 offset:40960\n"
"s_waitcnt lgkmcnt(7)\n"
"v_mfma_f32_16x16x32_bf16 v[180:183], v[136:139], v[100:103], v[180:183]\n"
"ds_read_b64_tr_b16 v[154:155], v4 offset:41984\n"
"s_waitcnt lgkmcnt(7)\n"
"v_mfma_f32_16x16x32_bf16 v[184:187], v[136:139], v[104:107], v[184:187]\n"
"ds_read_b128 v[120:123], v3 offset:43008\n"
"s_waitcnt lgkmcnt(7)\n"
"v_mfma_f32_16x16x32_bf16 v[188:191], v[136:139], v[108:111], v[188:191]\n"
"ds_read_b128 v[124:127], v3 offset:45056\n"
"s_waitcnt lgkmcnt(7)\n"
"v_mfma_f32_16x16x32_bf16 v[192:195], v[136:139], v[112:115], v[192:195]\n"
"ds_read_b128 v[128:131], v3 offset:47104\n"
"s_waitcnt lgkmcnt(6)\n"
"v_mfma_f32_16x16x32_bf16 v[196:199], v[140:143], v[96:99], v[196:199]\n"
"ds_read_b128 v[132:135], v3 offset:49152\n"
"v_mfma_f32_16x16x32_bf16 v[200:203], v[140:143], v[100:103], v[200:203]\n"
"ds_read_b64_tr_b16 v[156:157], v5 offset:40960\n"
"v_mfma_f32_16x16x32_bf16 v[204:207], v[140:143], v[104:107], v[204:207]\n"
"ds_read_b64_tr_b16 v[158:159], v5 offset:41984\n"
"v_mfma_f32_16x16x32_bf16 v[208:211], v[140:143], v[108:111], v[208:211]\n"
"v_mfma_f32_16x16x32_bf16 v[212:215], v[140:143], v[112:115], v[212:215]\n"
"s_waitcnt lgkmcnt(6)\n"
"v_mfma_f32_16x16x32_bf16 v[176:179], v[152:155], v[116:119], v[176:179]\n"
"s_waitcnt lgkmcnt(5)\n"
"v_mfma_f32_16x16x32_bf16 v[180:183], v[152:155], v[120:123], v[180:183]\n"
"s_waitcnt lgkmcnt(0)\n"
"s_barrier\n"
"v_mfma_f32_16x16x32_bf16 v[184:187], v[152:155], v[124:127], v[184:187]\n"
"ds_read_b128 v[56:59], v8 offset:0\n"
"ds_read_b64_tr_b16 v[136:137], v4 offset:49152\n"
"v_mfma_f32_16x16x32_bf16 v[188:191], v[152:155], v[128:131], v[188:191]\n"
"ds_read_b64_tr_b16 v[138:139], v4 offset:50176\n"
"ds_read_b128 v[60:63], v8 offset:2048\n"
"v_mfma_f32_16x16x32_bf16 v[192:195], v[152:155], v[132:135], v[192:195]\n"
"ds_read_b128 v[64:67], v8 offset:4096\n"
"ds_read_b128 v[68:71], v8 offset:6144\n"
"v_mfma_f32_16x16x32_bf16 v[196:199], v[156:159], v[116:119], v[196:199]\n"
"ds_read_b128 v[72:75], v8 offset:8192\n"
"v_mfma_f32_16x16x32_bf16 v[200:203], v[156:159], v[120:123], v[200:203]\n"
"ds_read_b64_tr_b16 v[140:141], v5 offset:49152\n"
"v_mfma_f32_16x16x32_bf16 v[204:207], v[156:159], v[124:127], v[204:207]\n"
"ds_read_b64_tr_b16 v[142:143], v5 offset:50176\n"
"v_mfma_f32_16x16x32_bf16 v[208:211], v[156:159], v[128:131], v[208:211]\n"
"v_mfma_f32_16x16x32_bf16 v[212:215], v[156:159], v[132:135], v[212:215]\n"
"s_waitcnt lgkmcnt(6)\n"
"v_mfma_f32_16x16x32_bf16 v[176:179], v[136:139], v[56:59], v[176:179]\n"
"ds_read_b128 v[76:79], v9 offset:0\n"
"ds_read_b64_tr_b16 v[152:153], v4 offset:57344\n"
"s_waitcnt lgkmcnt(7)\n"
"v_mfma_f32_16x16x32_bf16 v[180:183], v[136:139], v[60:63], v[180:183]\n"
"ds_read_b64_tr_b16 v[154:155], v4 offset:58368\n"
"s_waitcnt lgkmcnt(7)\n"
"v_mfma_f32_16x16x32_bf16 v[184:187], v[136:139], v[64:67], v[184:187]\n"
"ds_read_b128 v[80:83], v9 offset:2048\n"
"s_waitcnt lgkmcnt(7)\n"
"v_mfma_f32_16x16x32_bf16 v[188:191], v[136:139], v[68:71], v[188:191]\n"
"ds_read_b128 v[84:87], v9 offset:4096\n"
"s_waitcnt lgkmcnt(7)\n"
"v_mfma_f32_16x16x32_bf16 v[192:195], v[136:139], v[72:75], v[192:195]\n"
"ds_read_b128 v[88:91], v9 offset:6144\n"
"s_waitcnt lgkmcnt(6)\n"
"v_mfma_f32_16x16x32_bf16 v[196:199], v[140:143], v[56:59], v[196:199]\n"
"ds_read_b128 v[92:95], v9 offset:8192\n"
"v_mfma_f32_16x16x32_bf16 v[200:203], v[140:143], v[60:63], v[200:203]\n"
"ds_read_b64_tr_b16 v[156:157], v5 offset:57344\n"
"v_mfma_f32_16x16x32_bf16 v[204:207], v[140:143], v[64:67], v[204:207]\n"
"ds_read_b64_tr_b16 v[158:159], v5 offset:58368\n"
"v_mfma_f32_16x16x32_bf16 v[208:211], v[140:143], v[68:71], v[208:211]\n"
"v_mfma_f32_16x16x32_bf16 v[212:215], v[140:143], v[72:75], v[212:215]\n"
"s_waitcnt lgkmcnt(6)\n"
"v_mfma_f32_16x16x32_bf16 v[176:179], v[152:155], v[76:79], v[176:179]\n"
"ds_read_b64_tr_b16 v[136:137], v6 offset:32768\n"
"s_waitcnt lgkmcnt(6)\n"
"v_mfma_f32_16x16x32_bf16 v[180:183], v[152:155], v[80:83], v[180:183]\n"
"ds_read_b64_tr_b16 v[138:139], v6 offset:33792\n"
"s_waitcnt lgkmcnt(6)\n"
"v_mfma_f32_16x16x32_bf16 v[184:187], v[152:155], v[84:87], v[184:187]\n"
"ds_read_b64_tr_b16 v[140:141], v7 offset:32768\n"
"s_waitcnt lgkmcnt(6)\n"
"v_mfma_f32_16x16x32_bf16 v[188:191], v[152:155], v[88:91], v[188:191]\n"
"ds_read_b64_tr_b16 v[142:143], v7 offset:33792\n"
"s_waitcnt lgkmcnt(6)\n"
"v_mfma_f32_16x16x32_bf16 v[192:195], v[152:155], v[92:95], v[192:195]\n"
"s_waitcnt lgkmcnt(4)\n"
"v_mfma_f32_16x16x32_bf16 v[196:199], v[156:159], v[76:79], v[196:199]\n"
"v_mfma_f32_16x16x32_bf16 v[200:203], v[156:159], v[80:83], v[200:203]\n"
"v_mfma_f32_16x16x32_bf16 v[204:207], v[156:159], v[84:87], v[204:207]\n"
"v_mfma_f32_16x16x32_bf16 v[208:211], v[156:159], v[88:91], v[208:211]\n"
"v_mfma_f32_16x16x32_bf16 v[212:215], v[156:159], v[92:95], v[212:215]\n"
"s_cmp_eq_u32 s54, s57\n"
"s_cbranch_scc1 .Lk_own_70_%=\n"
"s_waitcnt lgkmcnt(2)\n"
"v_mfma_f32_16x16x32_bf16 v[216:219], v[136:139], v[96:99], v[216:219]\n"
"ds_read_b64_tr_b16 v[152:153], v6 offset:40960\n"
"v_mfma_f32_16x16x32_bf16 v[220:223], v[136:139], v[100:103], v[220:223]\n"
"ds_read_b64_tr_b16 v[154:155], v6 offset:41984\n"
"v_mfma_f32_16x16x32_bf16 v[224:227], v[136:139], v[104:107], v[224:227]\n"
"ds_read_b64_tr_b16 v[156:157], v7 offset:40960\n"
"v_mfma_f32_16x16x32_bf16 v[228:231], v[136:139], v[108:111], v[228:231]\n"
"ds_read_b64_tr_b16 v[158:159], v7 offset:41984\n"
"v_cvt_pk_bf16_f32 v176, v176, v177\n"
"v_mfma_f32_16x16x32_bf16 v[232:235], v[136:139], v[112:115], v[232:235]\n"
"v_cvt_pk_bf16_f32 v177, v178, v179\n"
"s_waitcnt lgkmcnt(4)\n"
"v_mfma_f32_16x16x32_bf16 v[236:239], v[140:143], v[96:99], v[236:239]\n"
"v_cvt_pk_bf16_f32 v178, v196, v197\n"
"v_mfma_f32_16x16x32_bf16 v[240:243], v[140:143], v[100:103], v[240:243]\n"
"v_cvt_pk_bf16_f32 v179, v198, v199\n"
"v_mfma_f32_16x16x32_bf16 v[244:247], v[140:143], v[104:107], v[244:247]\n"
"s_add_u32 s65, s73, 0\n"
"v_mfma_f32_16x16x32_bf16 v[248:251], v[140:143], v[108:111], v[248:251]\n"
"buffer_store_dwordx4 v[176:179], v33, s[44:47], s65 offen sc1\n"
"v_mfma_f32_16x16x32_bf16 v[252:255], v[140:143], v[112:115], v[252:255]\n"
"v_cvt_pk_bf16_f32 v180, v180, v181\n"
"s_waitcnt lgkmcnt(2)\n"
"v_mfma_f32_16x16x32_bf16 v[216:219], v[152:155], v[116:119], v[216:219]\n"
"ds_read_b64_tr_b16 v[136:137], v6 offset:49152\n"
"v_cvt_pk_bf16_f32 v181, v182, v183\n"
"v_mfma_f32_16x16x32_bf16 v[220:223], v[152:155], v[120:123], v[220:223]\n"
"ds_read_b64_tr_b16 v[138:139], v6 offset:50176\n"
"v_cvt_pk_bf16_f32 v182, v200, v201\n"
"v_mfma_f32_16x16x32_bf16 v[224:227], v[152:155], v[124:127], v[224:227]\n"
"ds_read_b64_tr_b16 v[140:141], v7 offset:49152\n"
"v_cvt_pk_bf16_f32 v183, v202, v203\n"
"v_mfma_f32_16x16x32_bf16 v[228:231], v[152:155], v[128:131], v[228:231]\n"
"ds_read_b64_tr_b16 v[142:143], v7 offset:50176\n"
"s_add_u32 s65, s73, 2048\n"
"v_mfma_f32_16x16x32_bf16 v[232:235], v[152:155], v[132:135], v[232:235]\n"
"buffer_store_dwordx4 v[180:183], v33, s[44:47], s65 offen sc1\n"
"s_waitcnt lgkmcnt(4)\n"
"v_mfma_f32_16x16x32_bf16 v[236:239], v[156:159], v[116:119], v[236:239]\n"
"v_cvt_pk_bf16_f32 v184, v184, v185\n"
"v_mfma_f32_16x16x32_bf16 v[240:243], v[156:159], v[120:123], v[240:243]\n"
"v_cvt_pk_bf16_f32 v185, v186, v187\n"
"v_mfma_f32_16x16x32_bf16 v[244:247], v[156:159], v[124:127], v[244:247]\n"
"v_cvt_pk_bf16_f32 v186, v204, v205\n"
"v_mfma_f32_16x16x32_bf16 v[248:251], v[156:159], v[128:131], v[248:251]\n"
"v_cvt_pk_bf16_f32 v187, v206, v207\n"
"v_mfma_f32_16x16x32_bf16 v[252:255], v[156:159], v[132:135], v[252:255]\n"
"s_add_u32 s65, s73, 4096\n"
"s_waitcnt lgkmcnt(2)\n"
"v_mfma_f32_16x16x32_bf16 v[216:219], v[136:139], v[56:59], v[216:219]\n"
"ds_read_b64_tr_b16 v[152:153], v6 offset:57344\n"
"buffer_store_dwordx4 v[184:187], v33, s[44:47], s65 offen sc1\n"
"v_mfma_f32_16x16x32_bf16 v[220:223], v[136:139], v[60:63], v[220:223]\n"
"ds_read_b64_tr_b16 v[154:155], v6 offset:58368\n"
"v_cvt_pk_bf16_f32 v188, v188, v189\n"
"v_mfma_f32_16x16x32_bf16 v[224:227], v[136:139], v[64:67], v[224:227]\n"
"ds_read_b64_tr_b16 v[156:157], v7 offset:57344\n"
"v_cvt_pk_bf16_f32 v189, v190, v191\n"
"v_mfma_f32_16x16x32_bf16 v[228:231], v[136:139], v[68:71], v[228:231]\n"
"ds_read_b64_tr_b16 v[158:159], v7 offset:58368\n"
"v_cvt_pk_bf16_f32 v190, v208, v209\n"
"v_mfma_f32_16x16x32_bf16 v[232:235], v[136:139], v[72:75], v[232:235]\n"
"v_cvt_pk_bf16_f32 v191, v210, v211\n"
"s_waitcnt lgkmcnt(4)\n"
"v_mfma_f32_16x16x32_bf16 v[236:239], v[140:143], v[56:59], v[236:239]\n"
"s_add_u32 s65, s73, 6144\n"
"v_mfma_f32_16x16x32_bf16 v[240:243], v[140:143], v[60:63], v[240:243]\n"
"buffer_store_dwordx4 v[188:191], v33, s[44:47], s65 offen sc1\n"
"v_mfma_f32_16x16x32_bf16 v[244:247], v[140:143], v[64:67], v[244:247]\n"
"v_cvt_pk_bf16_f32 v192, v192, v193\n"
"v_mfma_f32_16x16x32_bf16 v[248:251], v[140:143], v[68:71], v[248:251]\n"
"v_cvt_pk_bf16_f32 v193, v194, v195\n"
"v_mfma_f32_16x16x32_bf16 v[252:255], v[140:143], v[72:75], v[252:255]\n"
"v_cvt_pk_bf16_f32 v194, v212, v213\n"
"s_waitcnt lgkmcnt(2)\n"
"v_mfma_f32_16x16x32_bf16 v[216:219], v[152:155], v[76:79], v[216:219]\n"
"v_cvt_pk_bf16_f32 v195, v214, v215\n"
"v_mfma_f32_16x16x32_bf16 v[220:223], v[152:155], v[80:83], v[220:223]\n"
"s_add_u32 s65, s73, 8192\n"
"v_mfma_f32_16x16x32_bf16 v[224:227], v[152:155], v[84:87], v[224:227]\n"
"buffer_store_dwordx4 v[192:195], v33, s[44:47], s65 offen sc1\n"
"v_mfma_f32_16x16x32_bf16 v[228:231], v[152:155], v[88:91], v[228:231]\n"
"v_mfma_f32_16x16x32_bf16 v[232:235], v[152:155], v[92:95], v[232:235]\n"
"s_waitcnt lgkmcnt(0)\n"
"v_mfma_f32_16x16x32_bf16 v[236:239], v[156:159], v[76:79], v[236:239]\n"
"v_mfma_f32_16x16x32_bf16 v[240:243], v[156:159], v[80:83], v[240:243]\n"
"v_mfma_f32_16x16x32_bf16 v[244:247], v[156:159], v[84:87], v[244:247]\n"
"v_mfma_f32_16x16x32_bf16 v[248:251], v[156:159], v[88:91], v[248:251]\n"
"v_mfma_f32_16x16x32_bf16 v[252:255], v[156:159], v[92:95], v[252:255]\n"
"v_cvt_pk_bf16_f32 v216, v216, v217\n"
"v_cvt_pk_bf16_f32 v217, v218, v219\n"
"s_nop 1\n"
"v_cvt_pk_bf16_f32 v218, v236, v237\n"
"v_cvt_pk_bf16_f32 v219, v238, v239\n"
"s_add_u32 s65, s73, 1024\n"
"buffer_store_dwordx4 v[216:219], v33, s[44:47], s65 offen sc1\n"
"v_cvt_pk_bf16_f32 v220, v220, v221\n"
"v_cvt_pk_bf16_f32 v221, v222, v223\n"
"v_cvt_pk_bf16_f32 v222, v240, v241\n"
"v_cvt_pk_bf16_f32 v223, v242, v243\n"
"s_add_u32 s65, s73, 3072\n"
"buffer_store_dwordx4 v[220:223], v33, s[44:47], s65 offen sc1\n"
"v_cvt_pk_bf16_f32 v224, v224, v225\n"
"v_cvt_pk_bf16_f32 v225, v226, v227\n"
"v_cvt_pk_bf16_f32 v226, v244, v245\n"
"v_cvt_pk_bf16_f32 v227, v246, v247\n"
"s_add_u32 s65, s73, 5120\n"
"buffer_store_dwordx4 v[224:227], v33, s[44:47], s65 offen sc1\n"
"v_cvt_pk_bf16_f32 v228, v228, v229\n"
"v_cvt_pk_bf16_f32 v229, v230, v231\n"
"v_cvt_pk_bf16_f32 v230, v248, v249\n"
"v_cvt_pk_bf16_f32 v231, v250, v251\n"
"s_add_u32 s65, s73, 7168\n"
"buffer_store_dwordx4 v[228:231], v33, s[44:47], s65 offen sc1\n"
"v_cvt_pk_bf16_f32 v232, v232, v233\n"
"v_cvt_pk_bf16_f32 v233, v234, v235\n"
"v_cvt_pk_bf16_f32 v234, v252, v253\n"
"v_cvt_pk_bf16_f32 v235, v254, v255\n"
"s_add_u32 s65, s73, 9216\n"
"buffer_store_dwordx4 v[232:235], v33, s[44:47], s65 offen sc1\n"
"s_barrier\n"
"s_waitcnt vmcnt(0)\n"
"s_mov_b64 exec, 1\n"
"global_store_dword v37, v36, s[52:53] offset:128 sc1\n"
"s_mov_b64 exec, -1\n"
"s_branch .Lk_join_71_%=\n"
".Lk_own_70_%=:\n"
"s_waitcnt lgkmcnt(2)\n"
"v_mfma_f32_16x16x32_bf16 v[216:219], v[136:139], v[96:99], v[216:219]\n"
"ds_read_b64_tr_b16 v[152:153], v6 offset:40960\n"
"v_mfma_f32_16x16x32_bf16 v[220:223], v[136:139], v[100:103], v[220:223]\n"
"ds_read_b64_tr_b16 v[154:155], v6 offset:41984\n"
"v_mfma_f32_16x16x32_bf16 v[224:227], v[136:139], v[104:107], v[224:227]\n"
"ds_read_b64_tr_b16 v[156:157], v7 offset:40960\n"
"v_mfma_f32_16x16x32_bf16 v[228:231], v[136:139], v[108:111], v[228:231]\n"
"ds_read_b64_tr_b16 v[158:159], v7 offset:41984\n"
"ds_write_b128 v33, v[176:179] offset:20480\n"
"v_mfma_f32_16x16x32_bf16 v[232:235], v[136:139], v[112:115], v[232:235]\n"
"ds_write_b128 v33, v[180:183] offset:21504\n"
"s_waitcnt lgkmcnt(6)\n"
"v_mfma_f32_16x16x32_bf16 v[236:239], v[140:143], v[96:99], v[236:239]\n"
"ds_write_b128 v33, v[184:187] offset:22528\n"
"v_mfma_f32_16x16x32_bf16 v[240:243], v[140:143], v[100:103], v[240:243]\n"
"ds_write_b128 v33, v[188:191] offset:23552\n"
"v_mfma_f32_16x16x32_bf16 v[244:247], v[140:143], v[104:107], v[244:247]\n"
"ds_write_b128 v33, v[192:195] offset:24576\n"
"v_mfma_f32_16x16x32_bf16 v[248:251], v[140:143], v[108:111], v[248:251]\n"
"ds_write_b128 v33, v[196:199] offset:25600\n"
"v_mfma_f32_16x16x32_bf16 v[252:255], v[140:143], v[112:115], v[252:255]\n"
"ds_write_b128 v33, v[200:203] offset:26624\n"
"s_waitcnt lgkmcnt(9)\n"
"v_mfma_f32_16x16x32_bf16 v[216:219], v[152:155], v[116:119], v[216:219]\n"
"ds_read_b64_tr_b16 v[136:137], v6 offset:49152\n"
"ds_write_b128 v33, v[204:207] offset:27648\n"
"v_mfma_f32_16x16x32_bf16 v[220:223], v[152:155], v[120:123], v[220:223]\n"
"ds_read_b64_tr_b16 v[138:139], v6 offset:50176\n"
"ds_write_b128 v33, v[208:211] offset:28672\n"
"v_mfma_f32_16x16x32_bf16 v[224:227], v[152:155], v[124:127], v[224:227]\n"
"ds_read_b64_tr_b16 v[140:141], v7 offset:49152\n"
"ds_write_b128 v33, v[212:215] offset:29696\n"
"v_mfma_f32_16x16x32_bf16 v[228:231], v[152:155], v[128:131], v[228:231]\n"
"ds_read_b64_tr_b16 v[142:143], v7 offset:50176\n"
"v_mfma_f32_16x16x32_bf16 v[232:235], v[152:155], v[132:135], v[232:235]\n"
"s_waitcnt lgkmcnt(14)\n"
"v_mfma_f32_16x16x32_bf16 v[236:239], v[156:159], v[116:119], v[236:239]\n"
"v_mfma_f32_16x16x32_bf16 v[240:243], v[156:159], v[120:123], v[240:243]\n"
"v_mfma_f32_16x16x32_bf16 v[244:247], v[156:159], v[124:127], v[244:247]\n"
"v_mfma_f32_16x16x32_bf16 v[248:251], v[156:159], v[128:131], v[248:251]\n"
"v_mfma_f32_16x16x32_bf16 v[252:255], v[156:159], v[132:135], v[252:255]\n"
"s_waitcnt lgkmcnt(4)\n"
"v_mfma_f32_16x16x32_bf16 v[216:219], v[136:139], v[56:59], v[216:219]\n"
"ds_read_b64_tr_b16 v[152:153], v6 offset:57344\n"
"v_mfma_f32_16x16x32_bf16 v[220:223], v[136:139], v[60:63], v[220:223]\n"
"ds_read_b64_tr_b16 v[154:155], v6 offset:58368\n"
"v_mfma_f32_16x16x32_bf16 v[224:227], v[136:139], v[64:67], v[224:227]\n"
"ds_read_b64_tr_b16 v[156:157], v7 offset:57344\n"
"v_mfma_f32_16x16x32_bf16 v[228:231], v[136:139], v[68:71], v[228:231]\n"
"ds_read_b64_tr_b16 v[158:159], v7 offset:58368\n"
"v_mfma_f32_16x16x32_bf16 v[232:235], v[136:139], v[72:75], v[232:235]\n"
"s_waitcnt lgkmcnt(4)\n"
"v_mfma_f32_16x16x32_bf16 v[236:239], v[140:143], v[56:59], v[236:239]\n"
"v_mfma_f32_16x16x32_bf16 v[240:243], v[140:143], v[60:63], v[240:243]\n"
"v_mfma_f32_16x16x32_bf16 v[244:247], v[140:143], v[64:67], v[244:247]\n"
"v_mfma_f32_16x16x32_bf16 v[248:251], v[140:143], v[68:71], v[248:251]\n"
"v_mfma_f32_16x16x32_bf16 v[252:255], v[140:143], v[72:75], v[252:255]\n"
"s_waitcnt lgkmcnt(2)\n"
"v_mfma_f32_16x16x32_bf16 v[216:219], v[152:155], v[76:79], v[216:219]\n"
"v_mfma_f32_16x16x32_bf16 v[220:223], v[152:155], v[80:83], v[220:223]\n"
"v_mfma_f32_16x16x32_bf16 v[224:227], v[152:155], v[84:87], v[224:227]\n"
"v_mfma_f32_16x16x32_bf16 v[228:231], v[152:155], v[88:91], v[228:231]\n"
"v_mfma_f32_16x16x32_bf16 v[232:235], v[152:155], v[92:95], v[232:235]\n"
"s_waitcnt lgkmcnt(0)\n"
"v_mfma_f32_16x16x32_bf16 v[236:239], v[156:159], v[76:79], v[236:239]\n"
"v_mfma_f32_16x16x32_bf16 v[240:243], v[156:159], v[80:83], v[240:243]\n"
"v_mfma_f32_16x16x32_bf16 v[244:247], v[156:159], v[84:87], v[244:247]\n"
"v_mfma_f32_16x16x32_bf16 v[248:251], v[156:159], v[88:91], v[248:251]\n"
"v_mfma_f32_16x16x32_bf16 v[252:255], v[156:159], v[92:95], v[252:255]\n"
"ds_write_b128 v33, v[216:219] offset:30720\n"
"ds_write_b128 v33, v[220:223] offset:31744\n"
"ds_write_b128 v33, v[224:227] offset:32768\n"
"ds_write_b128 v33, v[228:231] offset:33792\n"
"ds_write_b128 v33, v[232:235] offset:34816\n"
"ds_write_b128 v33, v[236:239] offset:35840\n"
"ds_write_b128 v33, v[240:243] offset:36864\n"
"ds_write_b128 v33, v[244:247] offset:37888\n"
"ds_write_b128 v33, v[248:251] offset:38912\n"
"ds_write_b128 v33, v[252:255] offset:39936\n"
"s_waitcnt lgkmcnt(0)\n"
"s_barrier\n"
".Lk_join_71_%=:\n"
"s_waitcnt vmcnt(0)\n"
"s_branch .Lk_E_3_%=\n"
".Lk_SA_4_%=:\n"
"s_nop 7\n"
"s_waitcnt vmcnt(0) lgkmcnt(0)\n"
"s_barrier\n"
"v_mov_b32 v236, 0xff800000\n"
"v_mov_b32 v237, 0xff800000\n"
"v_mov_b32 v238, 0xff800000\n"
"v_mov_b32 v239, 0xff800000\n"
"v_mov_b32 v240, 0xff800000\n"
"s_mov_b32 s74, 0\n"
".Lk_s1_72_%=:\n"
"s_nop 7\n"
"s_barrier\n"
"ds_read_b128 v[64:67], v14 offset:0\n"
"ds_read_b128 v[72:75], v15 offset:0\n"
"ds_read_b128 v[80:83], v16 offset:0\n"
"ds_read_b128 v[88:91], v17 offset:0\n"
"ds_read_b128 v[68:71], v14 offset:4096\n"
"ds_read_b128 v[76:79], v15 offset:4096\n"
"ds_read_b128 v[84:87], v16 offset:4096\n"
"ds_read_b128 v[92:95], v17 offset:4096\n"
"s_waitcnt lgkmcnt(7)\n"
"v_mfma_f32_16x16x32_bf16 v[176:179], v[64:67], v[96:99], 0\n"
"s_waitcnt lgkmcnt(3)\n"
"v_mfma_f32_16x16x32_bf16 v[180:183], v[68:71], v[96:99], 0\n"
"v_mfma_f32_16x16x32_bf16 v[176:179], v[72:75], v[100:103], v[176:179]\n"
"s_waitcnt lgkmcnt(2)\n"
"v_mfma_f32_16x16x32_bf16 v[180:183], v[76:79], v[100:103], v[180:183]\n"
"v_mfma_f32_16x16x32_bf16 v[176:179], v[80:83], v[104:107], v[176:179]\n"
"s_waitcnt lgkmcnt(1)\n"
"v_mfma_f32_16x16x32_bf16 v[180:183], v[84:87], v[104:107], v[180:183]\n"
"v_mfma_f32_16x16x32_bf16 v[176:179], v[88:91], v[108:111], v[176:179]\n"
"s_waitcnt lgkmcnt(0)\n"
"v_mfma_f32_16x16x32_bf16 v[180:183], v[92:95], v[108:111], v[180:183]\n"
"s_nop 5\n"
"v_max3_f32 v236, v176, v177, v236\n"
"v_max3_f32 v236, v178, v179, v236\n"
"v_max3_f32 v236, v180, v181, v236\n"
"v_max3_f32 v236, v182, v183, v236\n"
"v_mfma_f32_16x16x32_bf16 v[176:179], v[64:67], v[112:115], 0\n"
"v_mfma_f32_16x16x32_bf16 v[180:183], v[68:71], v[112:115], 0\n"
"v_mfma_f32_16x16x32_bf16 v[176:179], v[72:75], v[116:119], v[176:179]\n"
"v_mfma_f32_16x16x32_bf16 v[180:183], v[76:79], v[116:119], v[180:183]\n"
"v_mfma_f32_16x16x32_bf16 v[176:179], v[80:83], v[120:123], v[176:179]\n"
"v_mfma_f32_16x16x32_bf16 v[180:183], v[84:87], v[120:123], v[180:183]\n"
"v_mfma_f32_16x16x32_bf16 v[176:179], v[88:91], v[124:127], v[176:179]\n"
"v_mfma_f32_16x16x32_bf16 v[180:183], v[92:95], v[124:127], v[180:183]\n"
"s_nop 6\n"
"v_max3_f32 v237, v176, v177, v237\n"
"v_max3_f32 v237, v178, v179, v237\n"
"v_max3_f32 v237, v180, v181, v237\n"
"v_max3_f32 v237, v182, v183, v237\n"
"v_mfma_f32_16x16x32_bf16 v[176:179], v[64:67], v[128:131], 0\n"
"v_mfma_f32_16x16x32_bf16 v[180:183], v[68:71], v[128:131], 0\n"
"v_mfma_f32_16x16x32_bf16 v[176:179], v[72:75], v[132:135], v[176:179]\n"
"v_mfma_f32_16x16x32_bf16 v[180:183], v[76:79], v[132:135], v[180:183]\n"
"v_mfma_f32_16x16x32_bf16 v[176:179], v[80:83], v[136:139], v[176:179]\n"
"v_mfma_f32_16x16x32_bf16 v[180:183], v[84:87], v[136:139], v[180:183]\n"
"v_mfma_f32_16x16x32_bf16 v[176:179], v[88:91], v[140:143], v[176:179]\n"
"v_mfma_f32_16x16x32_bf16 v[180:183], v[92:95], v[140:143], v[180:183]\n"
"s_nop 6\n"
"v_max3_f32 v238, v176, v177, v238\n"
"v_max3_f32 v238, v178, v179, v238\n"
"v_max3_f32 v238, v180, v181, v238\n"
"v_max3_f32 v238, v182, v183, v238\n"
"v_mfma_f32_16x16x32_bf16 v[176:179], v[64:67], v[144:147], 0\n"
"v_mfma_f32_16x16x32_bf16 v[180:183], v[68:71], v[144:147], 0\n"
"v_mfma_f32_16x16x32_bf16 v[176:179], v[72:75], v[148:151], v[176:179]\n"
"v_mfma_f32_16x16x32_bf16 v[180:183], v[76:79], v[148:151], v[180:183]\n"
"v_mfma_f32_16x16x32_bf16 v[176:179], v[80:83], v[152:155], v[176:179]\n"
"v_mfma_f32_16x16x32_bf16 v[180:183], v[84:87], v[152:155], v[180:183]\n"
"v_mfma_f32_16x16x32_bf16 v[176:179], v[88:91], v[156:159], v[176:179]\n"
"v_mfma_f32_16x16x32_bf16 v[180:183], v[92:95], v[156:159], v[180:183]\n"
"s_nop 6\n"
"v_max3_f32 v239, v176, v177, v239\n"
"v_max3_f32 v239, v178, v179, v239\n"
"v_max3_f32 v239, v180, v181, v239\n"
"v_max3_f32 v239, v182, v183, v239\n"
"v_mfma_f32_16x16x32_bf16 v[176:179], v[64:67], v[160:163], 0\n"
"v_mfma_f32_16x16x32_bf16 v[180:183], v[68:71], v[160:163], 0\n"
"v_mfma_f32_16x16x32_bf16 v[176:179], v[72:75], v[164:167], v[176:179]\n"
"v_mfma_f32_16x16x32_bf16 v[180:183], v[76:79], v[164:167], v[180:183]\n"
"v_mfma_f32_16x16x32_bf16 v[176:179], v[80:83], v[168:171], v[176:179]\n"
"v_mfma_f32_16x16x32_bf16 v[180:183], v[84:87], v[168:171], v[180:183]\n"
"v_mfma_f32_16x16x32_bf16 v[176:179], v[88:91], v[172:175], v[176:179]\n"
"v_mfma_f32_16x16x32_bf16 v[180:183], v[92:95], v[172:175], v[180:183]\n"
"s_nop 6\n"
"v_max3_f32 v240, v176, v177, v240\n"
"v_max3_f32 v240, v178, v179, v240\n"
"v_max3_f32 v240, v180, v181, v240\n"
"v_max3_f32 v240, v182, v183, v240\n"
"s_barrier\n"
"s_add_u32 s74, s74, 1\n"
"s_cmp_lt_u32 s74, 8\n"
"s_cbranch_scc1 .Lk_s1_72_%=\n"
"v_mov_b32 v241, v236\n"
"v_mov_b32 v242, v237\n"
"v_mov_b32 v243, v238\n"
"v_mov_b32 v244, v239\n"
"v_mov_b32 v245, v240\n"
"v_permlane32_swap_b32 v236, v241\n"
"v_permlane32_swap_b32 v237, v242\n"
"v_permlane32_swap_b32 v238, v243\n"
"v_permlane32_swap_b32 v239, v244\n"
"v_permlane32_swap_b32 v240, v245\n"
"v_max_f32 v236, v236, v241\n"
"v_max_f32 v237, v237, v242\n"
"v_max_f32 v238, v238, v243\n"
"v_max_f32 v239, v239, v244\n"
"v_max_f32 v240, v240, v245\n"
"v_mov_b32 v241, v236\n"
"v_mov_b32 v242, v237\n"
"v_mov_b32 v243, v238\n"
"v_mov_b32 v244, v239\n"
"v_mov_b32 v245, v240\n"
"v_permlane16_swap_b32 v236, v241\n"
"v_permlane16_swap_b32 v237, v242\n"
"v_permlane16_swap_b32 v238, v243\n"
"v_permlane16_swap_b32 v239, v244\n"
"v_permlane16_swap_b32 v240, v245\n"
"v_max_f32 v236, v236, v241\n"
"v_max_f32 v237, v237, v242\n"
"v_max_f32 v238, v238, v243\n"
"v_max_f32 v239, v239, v244\n"
"v_max_f32 v240, v240, v245\n"
"ds_write_b32 v31, v236 offset:0\n"
"ds_write_b32 v31, v237 offset:128\n"
"ds_write_b32 v31, v238 offset:256\n"
"ds_write_b32 v31, v239 offset:384\n"
"ds_write_b32 v31, v240 offset:512\n"
"s_waitcnt lgkmcnt(0)\n"
"s_barrier\n"
"s_xor_b32 s61, s54, 1\n"
"s_mul_i32 s61, s61, 640\n"
"s_add_u32 s61, s61, 159744\n"
"v_and_b32 v246, 31, v0\n"
"v_lshl_add_u32 v246, v246, 2, s61\n"
"ds_read_b32 v241, v246 offset:0\n"
"ds_read_b32 v242, v246 offset:128\n"
"ds_read_b32 v243, v246 offset:256\n"
"ds_read_b32 v244, v246 offset:384\n"
"ds_read_b32 v245, v246 offset:512\n"
"s_waitcnt lgkmcnt(4)\n"
"v_max_f32 v236, v236, v241\n"
"s_waitcnt lgkmcnt(3)\n"
"v_max_f32 v237, v237, v242\n"
"s_waitcnt lgkmcnt(2)\n"
"v_max_f32 v238, v238, v243\n"
"s_waitcnt lgkmcnt(1)\n"
"v_max_f32 v239, v239, v244\n"
"s_waitcnt lgkmcnt(0)\n"
"v_max_f32 v240, v240, v245\n"
"v_sub_f32 v216, 0, v236\n"
"v_sub_f32 v217, 0, v236\n"
"v_sub_f32 v218, 0, v236\n"
"v_sub_f32 v219, 0, v236\n"
"v_mov_b32 v208, 0\n"
"v_sub_f32 v220, 0, v237\n"
"v_sub_f32 v221, 0, v237\n"
"v_sub_f32 v222, 0, v237\n"
"v_sub_f32 v223, 0, v237\n"
"v_mov_b32 v209, 0\n"
"v_sub_f32 v224, 0, v238\n"
"v_sub_f32 v225, 0, v238\n"
"v_sub_f32 v226, 0, v238\n"
"v_sub_f32 v227, 0, v238\n"
"v_mov_b32 v210, 0\n"
"v_sub_f32 v228, 0, v239\n"
"v_sub_f32 v229, 0, v239\n"
"v_sub_f32 v230, 0, v239\n"
"v_sub_f32 v231, 0, v239\n"
"v_mov_b32 v211, 0\n"
"v_sub_f32 v232, 0, v240\n"
"v_sub_f32 v233, 0, v240\n"
"v_sub_f32 v234, 0, v240\n"
"v_sub_f32 v235, 0, v240\n"
"v_mov_b32 v212, 0\n"
"s_mov_b32 s74, 0\n"
".Lk_s2_73_%=:\n"
"s_nop 7\n"
"s_barrier\n"
"ds_read_b128 v[64:67], v14 offset:0\n"
"ds_read_b128 v[72:75], v15 offset:0\n"
"ds_read_b128 v[80:83], v16 offset:0\n"
"ds_read_b128 v[88:91], v17 offset:0\n"
"ds_read_b128 v[68:71], v14 offset:4096\n"
"ds_read_b128 v[76:79], v15 offset:4096\n"
"ds_read_b128 v[84:87], v16 offset:4096\n"
"ds_read_b128 v[92:95], v17 offset:4096\n"
"s_waitcnt lgkmcnt(7)\n"
"v_mfma_f32_16x16x32_bf16 v[176:179], v[64:67], v[96:99], v[216:219]\n"
"s_waitcnt lgkmcnt(3)\n"
"v_mfma_f32_16x16x32_bf16 v[180:183], v[68:71], v[96:99], v[216:219]\n"
"v_mfma_f32_16x16x32_bf16 v[176:179], v[72:75], v[100:103], v[176:179]\n"
"s_waitcnt lgkmcnt(2)\n"
"v_mfma_f32_16x16x32_bf16 v[180:183], v[76:79], v[100:103], v[180:183]\n"
"v_mfma_f32_16x16x32_bf16 v[176:179], v[80:83], v[104:107], v[176:179]\n"
"s_waitcnt lgkmcnt(1)\n"
"v_mfma_f32_16x16x32_bf16 v[180:183], v[84:87], v[104:107], v[180:183]\n"
"v_mfma_f32_16x16x32_bf16 v[176:179], v[88:91], v[108:111], v[176:179]\n"
"s_waitcnt lgkmcnt(0)\n"
"v_mfma_f32_16x16x32_bf16 v[180:183], v[92:95], v[108:111], v[180:183]\n"
"s_nop 5\n"
"v_exp_f32 v176, v176\n"
"v_exp_f32 v177, v177\n"
"v_exp_f32 v178, v178\n"
"v_exp_f32 v179, v179\n"
"v_exp_f32 v180, v180\n"
"v_exp_f32 v181, v181\n"
"v_exp_f32 v182, v182\n"
"v_exp_f32 v183, v183\n"
"v_add_f32 v208, v176, v208\n"
"v_add_f32 v208, v177, v208\n"
"v_add_f32 v208, v178, v208\n"
"v_add_f32 v208, v179, v208\n"
"v_add_f32 v208, v180, v208\n"
"v_add_f32 v208, v181, v208\n"
"v_add_f32 v208, v182, v208\n"
"v_add_f32 v208, v183, v208\n"
"v_cvt_pk_bf16_f32 v200, v176, v177\n"
"v_cvt_pk_bf16_f32 v201, v178, v179\n"
"v_cvt_pk_bf16_f32 v202, v180, v181\n"
"v_cvt_pk_bf16_f32 v203, v182, v183\n"
"ds_write_b128 v30, v[200:203] offset:40960\n"
"s_waitcnt lgkmcnt(0)\n"
"v_mfma_f32_16x16x32_bf16 v[176:179], v[64:67], v[112:115], v[220:223]\n"
"v_mfma_f32_16x16x32_bf16 v[180:183], v[68:71], v[112:115], v[220:223]\n"
"v_mfma_f32_16x16x32_bf16 v[176:179], v[72:75], v[116:119], v[176:179]\n"
"v_mfma_f32_16x16x32_bf16 v[180:183], v[76:79], v[116:119], v[180:183]\n"
"v_mfma_f32_16x16x32_bf16 v[176:179], v[80:83], v[120:123], v[176:179]\n"
"v_mfma_f32_16x16x32_bf16 v[180:183], v[84:87], v[120:123], v[180:183]\n"
"v_mfma_f32_16x16x32_bf16 v[176:179], v[88:91], v[124:127], v[176:179]\n"
"v_mfma_f32_16x16x32_bf16 v[180:183], v[92:95], v[124:127], v[180:183]\n"
"s_nop 6\n"
"v_exp_f32 v176, v176\n"
"v_exp_f32 v177, v177\n"
"v_exp_f32 v178, v178\n"
"v_exp_f32 v179, v179\n"
"v_exp_f32 v180, v180\n"
"v_exp_f32 v181, v181\n"
"v_exp_f32 v182, v182\n"
"v_exp_f32 v183, v183\n"
"v_add_f32 v209, v176, v209\n"
"v_add_f32 v209, v177, v209\n"
"v_add_f32 v209, v178, v209\n"
"v_add_f32 v209, v179, v209\n"
"v_add_f32 v209, v180, v209\n"
"v_add_f32 v209, v181, v209\n"
"v_add_f32 v209, v182, v209\n"
"v_add_f32 v209, v183, v209\n"
"v_cvt_pk_bf16_f32 v200, v176, v177\n"
"v_cvt_pk_bf16_f32 v201, v178, v179\n"
"v_cvt_pk_bf16_f32 v202, v180, v181\n"
"v_cvt_pk_bf16_f32 v203, v182, v183\n"
"ds_write_b128 v30, v[200:203] offset:41984\n"
"s_waitcnt lgkmcnt(0)\n"
"v_mfma_f32_16x16x32_bf16 v[176:179], v[64:67], v[128:131], v[224:227]\n"
"v_mfma_f32_16x16x32_bf16 v[180:183], v[68:71], v[128:131], v[224:227]\n"
"v_mfma_f32_16x16x32_bf16 v[176:179], v[72:75], v[132:135], v[176:179]\n"
"v_mfma_f32_16x16x32_bf16 v[180:183], v[76:79], v[132:135], v[180:183]\n"
"v_mfma_f32_16x16x32_bf16 v[176:179], v[80:83], v[136:139], v[176:179]\n"
"v_mfma_f32_16x16x32_bf16 v[180:183], v[84:87], v[136:139], v[180:183]\n"
"v_mfma_f32_16x16x32_bf16 v[176:179], v[88:91], v[140:143], v[176:179]\n"
"v_mfma_f32_16x16x32_bf16 v[180:183], v[92:95], v[140:143], v[180:183]\n"
"s_nop 6\n"
"v_exp_f32 v176, v176\n"
"v_exp_f32 v177, v177\n"
"v_exp_f32 v178, v178\n"
"v_exp_f32 v179, v179\n"
"v_exp_f32 v180, v180\n"
"v_exp_f32 v181, v181\n"
"v_exp_f32 v182, v182\n"
"v_exp_f32 v183, v183\n"
"v_add_f32 v210, v176, v210\n"
"v_add_f32 v210, v177, v210\n"
"v_add_f32 v210, v178, v210\n"
"v_add_f32 v210, v179, v210\n"
"v_add_f32 v210, v180, v210\n"
"v_add_f32 v210, v181, v210\n"
"v_add_f32 v210, v182, v210\n"
"v_add_f32 v210, v183, v210\n"
"v_cvt_pk_bf16_f32 v200, v176, v177\n"
"v_cvt_pk_bf16_f32 v201, v178, v179\n"
"v_cvt_pk_bf16_f32 v202, v180, v181\n"
"v_cvt_pk_bf16_f32 v203, v182, v183\n"
"ds_write_b128 v30, v[200:203] offset:43008\n"
"s_waitcnt lgkmcnt(0)\n"
"v_mfma_f32_16x16x32_bf16 v[176:179], v[64:67], v[144:147], v[228:231]\n"
"v_mfma_f32_16x16x32_bf16 v[180:183], v[68:71], v[144:147], v[228:231]\n"
"v_mfma_f32_16x16x32_bf16 v[176:179], v[72:75], v[148:151], v[176:179]\n"
"v_mfma_f32_16x16x32_bf16 v[180:183], v[76:79], v[148:151], v[180:183]\n"
"v_mfma_f32_16x16x32_bf16 v[176:179], v[80:83], v[152:155], v[176:179]\n"
"v_mfma_f32_16x16x32_bf16 v[180:183], v[84:87], v[152:155], v[180:183]\n"
"v_mfma_f32_16x16x32_bf16 v[176:179], v[88:91], v[156:159], v[176:179]\n"
"v_mfma_f32_16x16x32_bf16 v[180:183], v[92:95], v[156:159], v[180:183]\n"
"s_nop 6\n"
"v_exp_f32 v176, v176\n"
"v_exp_f32 v177, v177\n"
"v_exp_f32 v178, v178\n"
"v_exp_f32 v179, v179\n"
"v_exp_f32 v180, v180\n"
"v_exp_f32 v181, v181\n"
"v_exp_f32 v182, v182\n"
"v_exp_f32 v183, v183\n"
"v_add_f32 v211, v176, v211\n"
"v_add_f32 v211, v177, v211\n"
"v_add_f32 v211, v178, v211\n"
"v_add_f32 v211, v179, v211\n"
"v_add_f32 v211, v180, v211\n"
"v_add_f32 v211, v181, v211\n"
"v_add_f32 v211, v182, v211\n"
"v_add_f32 v211, v183, v211\n"
"v_cvt_pk_bf16_f32 v200, v176, v177\n"
"v_cvt_pk_bf16_f32 v201, v178, v179\n"
"v_cvt_pk_bf16_f32 v202, v180, v181\n"
"v_cvt_pk_bf16_f32 v203, v182, v183\n"
"ds_write_b128 v30, v[200:203] offset:44032\n"
"s_waitcnt lgkmcnt(0)\n"
"v_mfma_f32_16x16x32_bf16 v[176:179], v[64:67], v[160:163], v[232:235]\n"
"v_mfma_f32_16x16x32_bf16 v[180:183], v[68:71], v[160:163], v[232:235]\n"
"v_mfma_f32_16x16x32_bf16 v[176:179], v[72:75], v[164:167], v[176:179]\n"
"v_mfma_f32_16x16x32_bf16 v[180:183], v[76:79], v[164:167], v[180:183]\n"
"v_mfma_f32_16x16x32_bf16 v[176:179], v[80:83], v[168:171], v[176:179]\n"
"v_mfma_f32_16x16x32_bf16 v[180:183], v[84:87], v[168:171], v[180:183]\n"
"v_mfma_f32_16x16x32_bf16 v[176:179], v[88:91], v[172:175], v[176:179]\n"
"v_mfma_f32_16x16x32_bf16 v[180:183], v[92:95], v[172:175], v[180:183]\n"
"s_nop 6\n"
"v_exp_f32 v176, v176\n"
"v_exp_f32 v177, v177\n"
"v_exp_f32 v178, v178\n"
"v_exp_f32 v179, v179\n"
"v_exp_f32 v180, v180\n"
"v_exp_f32 v181, v181\n"
"v_exp_f32 v182, v182\n"
"v_exp_f32 v183, v183\n"
"v_add_f32 v212, v176, v212\n"
"v_add_f32 v212, v177, v212\n"
"v_add_f32 v212, v178, v212\n"
"v_add_f32 v212, v179, v212\n"
"v_add_f32 v212, v180, v212\n"
"v_add_f32 v212, v181, v212\n"
"v_add_f32 v212, v182, v212\n"
"v_add_f32 v212, v183, v212\n"
"v_cvt_pk_bf16_f32 v200, v176, v177\n"
"v_cvt_pk_bf16_f32 v201, v178, v179\n"
"v_cvt_pk_bf16_f32 v202, v180, v181\n"
"v_cvt_pk_bf16_f32 v203, v182, v183\n"
"ds_write_b128 v30, v[200:203] offset:45056\n"
"s_waitcnt lgkmcnt(0)\n"
"s_barrier\n"
"s_barrier\n"
"s_add_u32 s74, s74, 1\n"
"s_cmp_lt_u32 s74, 8\n"
"s_cbranch_scc1 .Lk_s2_73_%=\n"
"v_mov_b32 v236, v208\n"
"v_mov_b32 v237, v209\n"
"v_mov_b32 v238, v210\n"
"v_mov_b32 v239, v211\n"
"v_mov_b32 v240, v212\n"
"v_permlane32_swap_b32 v208, v236\n"
"v_permlane32_swap_b32 v209, v237\n"
"v_permlane32_swap_b32 v210, v238\n"
"v_permlane32_swap_b32 v211, v239\n"
"v_permlane32_swap_b32 v212, v240\n"
"v_add_f32 v208, v208, v236\n"
"v_add_f32 v209, v209, v237\n"
"v_add_f32 v210, v210, v238\n"
"v_add_f32 v211, v211, v239\n"
"v_add_f32 v212, v212, v240\n"
"ds_write_b32 v31, v208 offset:0\n"
"ds_write_b32 v31, v209 offset:128\n"
"ds_write_b32 v31, v210 offset:256\n"
"ds_write_b32 v31, v211 offset:384\n"
"ds_write_b32 v31, v212 offset:512\n"
"s_waitcnt lgkmcnt(0)\n"
"s_barrier\n"
"s_barrier\n"
"ds_read_b128 v[40:43], v32 offset:0\n"
"ds_read_b128 v[44:47], v32 offset:1024\n"
"s_add_u32 s65, s72, 0\n"
"s_waitcnt lgkmcnt(1)\n"
"buffer_store_dwordx4 v[40:43], v33, s[40:43], s65 offen offset:0 sc1\n"
"s_waitcnt lgkmcnt(0)\n"
"buffer_store_dwordx4 v[44:47], v33, s[40:43], s65 offen offset:1024 sc1\n"
"ds_read_b128 v[48:51], v32 offset:2048\n"
"ds_read_b128 v[52:55], v32 offset:3072\n"
"s_add_u32 s65, s72, 2048\n"
"s_waitcnt lgkmcnt(1)\n"
"buffer_store_dwordx4 v[48:51], v33, s[40:43], s65 offen offset:0 sc1\n"
"s_waitcnt lgkmcnt(0)\n"
"buffer_store_dwordx4 v[52:55], v33, s[40:43], s65 offen offset:1024 sc1\n"
"ds_read_b128 v[40:43], v32 offset:4096\n"
"ds_read_b128 v[44:47], v32 offset:5120\n"
"s_add_u32 s65, s72, 4096\n"
"s_waitcnt lgkmcnt(1)\n"
"buffer_store_dwordx4 v[40:43], v33, s[40:43], s65 offen offset:0 sc1\n"
"s_waitcnt lgkmcnt(0)\n"
"buffer_store_dwordx4 v[44:47], v33, s[40:43], s65 offen offset:1024 sc1\n"
"ds_read_b128 v[48:51], v32 offset:6144\n"
"ds_read_b128 v[52:55], v32 offset:7168\n"
"s_add_u32 s65, s72, 6144\n"
"s_waitcnt lgkmcnt(1)\n"
"buffer_store_dwordx4 v[48:51], v33, s[40:43], s65 offen offset:0 sc1\n"
"s_waitcnt lgkmcnt(0)\n"
"buffer_store_dwordx4 v[52:55], v33, s[40:43], s65 offen offset:1024 sc1\n"
"s_branch .Lk_JA_6_%=\n"
".Lk_SB_5_%=:\n"
"s_nop 7\n"
"s_waitcnt vmcnt(0) lgkmcnt(0)\n"
"s_barrier\n"
"s_mov_b32 s74, 0\n"
".Lk_s1_74_%=:\n"
"s_nop 7\n"
"s_add_u32 m0, s79, 110592\n"
"s_add_u32 s65, s79, s74\n"
"buffer_load_dwordx4 v10, s[28:31], s65 offen offset:0 lds\n"
"buffer_load_dwordx4 v11, s[28:31], s65 offen offset:1024 lds\n"
"buffer_load_dwordx4 v12, s[28:31], s65 offen offset:2048 lds\n"
"buffer_load_dwordx4 v13, s[28:31], s65 offen offset:3072 lds\n"
"s_waitcnt vmcnt(0)\n"
"s_barrier\n"
"s_barrier\n"
"s_add_u32 s74, s74, 16384\n"
"s_cmp_lt_u32 s74, 131072\n"
"s_cbranch_scc1 .Lk_s1_74_%=\n"
"s_barrier\n"
"v_mov_b32 v176, 0\n"
"v_mov_b32 v177, 0\n"
"v_mov_b32 v178, 0\n"
"v_mov_b32 v179, 0\n"
"v_mov_b32 v180, 0\n"
"v_mov_b32 v181, 0\n"
"v_mov_b32 v182, 0\n"
"v_mov_b32 v183, 0\n"
"v_mov_b32 v184, 0\n"
"v_mov_b32 v185, 0\n"
"v_mov_b32 v186, 0\n"
"v_mov_b32 v187, 0\n"
"v_mov_b32 v188, 0\n"
"v_mov_b32 v189, 0\n"
"v_mov_b32 v190, 0\n"
"v_mov_b32 v191, 0\n"
"v_mov_b32 v192, 0\n"
"v_mov_b32 v193, 0\n"
"v_mov_b32 v194, 0\n"
"v_mov_b32 v195, 0\n"
"v_mov_b32 v196, 0\n"
"v_mov_b32 v197, 0\n"
"v_mov_b32 v198, 0\n"
"v_mov_b32 v199, 0\n"
"v_mov_b32 v200, 0\n"
"v_mov_b32 v201, 0\n"
"v_mov_b32 v202, 0\n"
"v_mov_b32 v203, 0\n"
"v_mov_b32 v204, 0\n"
"v_mov_b32 v205, 0\n"
"v_mov_b32 v206, 0\n"
"v_mov_b32 v207, 0\n"
"v_mov_b32 v208, 0\n"
"v_mov_b32 v209, 0\n"
"v_mov_b32 v210, 0\n"
"v_mov_b32 v211, 0\n"
"v_mov_b32 v212, 0\n"
"v_mov_b32 v213, 0\n"
"v_mov_b32 v214, 0\n"
"v_mov_b32 v215, 0\n"
"v_mov_b32 v216, 0\n"
"v_mov_b32 v217, 0\n"
"v_mov_b32 v218, 0\n"
"v_mov_b32 v219, 0\n"
"v_mov_b32 v220, 0\n"
"v_mov_b32 v221, 0\n"
"v_mov_b32 v222, 0\n"
"v_mov_b32 v223, 0\n"
"v_mov_b32 v224, 0\n"
"v_mov_b32 v225, 0\n"
"v_mov_b32 v226, 0\n"
"v_mov_b32 v227, 0\n"
"v_mov_b32 v228, 0\n"
"v_mov_b32 v229, 0\n"
"v_mov_b32 v230, 0\n"
"v_mov_b32 v231, 0\n"
"v_mov_b32 v232, 0\n"
"v_mov_b32 v233, 0\n"
"v_mov_b32 v234, 0\n"
"v_mov_b32 v235, 0\n"
"v_mov_b32 v236, 0\n"
"v_mov_b32 v237, 0\n"
"v_mov_b32 v238, 0\n"
"v_mov_b32 v239, 0\n"
"v_mov_b32 v240, 0\n"
"v_mov_b32 v241, 0\n"
"v_mov_b32 v242, 0\n"
"v_mov_b32 v243, 0\n"
"v_mov_b32 v244, 0\n"
"v_mov_b32 v245, 0\n"
"v_mov_b32 v246, 0\n"
"v_mov_b32 v247, 0\n"
"v_mov_b32 v248, 0\n"
"v_mov_b32 v249, 0\n"
"v_mov_b32 v250, 0\n"
"v_mov_b32 v251, 0\n"
"v_mov_b32 v252, 0\n"
"v_mov_b32 v253, 0\n"
"v_mov_b32 v254, 0\n"
"v_mov_b32 v255, 0\n"
"s_mov_b32 s74, 0\n"
".Lk_s2_75_%=:\n"
"s_nop 7\n"
"s_add_u32 m0, s79, 110592\n"
"s_add_u32 s65, s79, s74\n"
"buffer_load_dwordx4 v10, s[28:31], s65 offen offset:0 lds\n"
"buffer_load_dwordx4 v11, s[28:31], s65 offen offset:1024 lds\n"
"buffer_load_dwordx4 v12, s[28:31], s65 offen offset:2048 lds\n"
"buffer_load_dwordx4 v13, s[28:31], s65 offen offset:3072 lds\n"
"s_add_u32 m0, s79, 126976\n"
"s_add_u32 s65, s79, s74\n"
"buffer_load_dwordx4 v14, s[32:35], s65 offen offset:0 lds\n"
"buffer_load_dwordx4 v15, s[32:35], s65 offen offset:1024 lds\n"
"buffer_load_dwordx4 v16, s[32:35], s65 offen offset:2048 lds\n"
"buffer_load_dwordx4 v17, s[32:35], s65 offen offset:3072 lds\n"
"s_waitcnt vmcnt(0)\n"
"s_barrier\n"
"s_barrier\n"
"ds_read_b128 v[96:99], v19 offset:40960\n"
"ds_read_b128 v[100:103], v19 offset:41984\n"
"ds_read_b128 v[104:107], v19 offset:43008\n"
"ds_read_b128 v[108:111], v19 offset:44032\n"
"ds_read_b128 v[112:115], v19 offset:45056\n"
"ds_read_b64_tr_b16 v[136:137], v20 offset:16384\n"
"ds_read_b64_tr_b16 v[138:139], v20 offset:20480\n"
"ds_read_b64_tr_b16 v[140:141], v21 offset:16384\n"
"ds_read_b64_tr_b16 v[142:143], v21 offset:20480\n"
"ds_read_b64_tr_b16 v[144:145], v22 offset:16384\n"
"ds_read_b64_tr_b16 v[146:147], v22 offset:20480\n"
"ds_read_b64_tr_b16 v[148:149], v23 offset:16384\n"
"ds_read_b64_tr_b16 v[150:151], v23 offset:20480\n"
"s_waitcnt lgkmcnt(6)\n"
"v_mfma_f32_16x16x32_bf16 v[176:179], v[136:139], v[96:99], v[176:179]\n"
"v_mfma_f32_16x16x32_bf16 v[180:183], v[136:139], v[100:103], v[180:183]\n"
"v_mfma_f32_16x16x32_bf16 v[184:187], v[136:139], v[104:107], v[184:187]\n"
"v_mfma_f32_16x16x32_bf16 v[188:191], v[136:139], v[108:111], v[188:191]\n"
"v_mfma_f32_16x16x32_bf16 v[192:195], v[136:139], v[112:115], v[192:195]\n"
"s_waitcnt lgkmcnt(4)\n"
"v_mfma_f32_16x16x32_bf16 v[196:199], v[140:143], v[96:99], v[196:199]\n"
"v_mfma_f32_16x16x32_bf16 v[200:203], v[140:143], v[100:103], v[200:203]\n"
"v_mfma_f32_16x16x32_bf16 v[204:207], v[140:143], v[104:107], v[204:207]\n"
"v_mfma_f32_16x16x32_bf16 v[208:211], v[140:143], v[108:111], v[208:211]\n"
"v_mfma_f32_16x16x32_bf16 v[212:215], v[140:143], v[112:115], v[212:215]\n"
"s_waitcnt lgkmcnt(2)\n"
"v_mfma_f32_16x16x32_bf16 v[216:219], v[144:147], v[96:99], v[216:219]\n"
"v_mfma_f32_16x16x32_bf16 v[220:223], v[144:147], v[100:103], v[220:223]\n"
"v_mfma_f32_16x16x32_bf16 v[224:227], v[144:147], v[104:107], v[224:227]\n"
"v_mfma_f32_16x16x32_bf16 v[228:231], v[144:147], v[108:111], v[228:231]\n"
"v_mfma_f32_16x16x32_bf16 v[232:235], v[144:147], v[112:115], v[232:235]\n"
"s_waitcnt lgkmcnt(0)\n"
"v_mfma_f32_16x16x32_bf16 v[236:239], v[148:151], v[96:99], v[236:239]\n"
"v_mfma_f32_16x16x32_bf16 v[240:243], v[148:151], v[100:103], v[240:243]\n"
"v_mfma_f32_16x16x32_bf16 v[244:247], v[148:151], v[104:107], v[244:247]\n"
"v_mfma_f32_16x16x32_bf16 v[248:251], v[148:151], v[108:111], v[248:251]\n"
"v_mfma_f32_16x16x32_bf16 v[252:255], v[148:151], v[112:115], v[252:255]\n"
"ds_read_b128 v[116:119], v19 offset:46080\n"
"ds_read_b128 v[120:123], v19 offset:47104\n"
"ds_read_b128 v[124:127], v19 offset:48128\n"
"ds_read_b128 v[128:131], v19 offset:49152\n"
"ds_read_b128 v[132:135], v19 offset:50176\n"
"ds_read_b64_tr_b16 v[136:137], v20 offset:24576\n"
"ds_read_b64_tr_b16 v[138:139], v20 offset:28672\n"
"ds_read_b64_tr_b16 v[140:141], v21 offset:24576\n"
"ds_read_b64_tr_b16 v[142:143], v21 offset:28672\n"
"ds_read_b64_tr_b16 v[144:145], v22 offset:24576\n"
"ds_read_b64_tr_b16 v[146:147], v22 offset:28672\n"
"ds_read_b64_tr_b16 v[148:149], v23 offset:24576\n"
"ds_read_b64_tr_b16 v[150:151], v23 offset:28672\n"
"s_waitcnt lgkmcnt(6)\n"
"v_mfma_f32_16x16x32_bf16 v[176:179], v[136:139], v[116:119], v[176:179]\n"
"v_mfma_f32_16x16x32_bf16 v[180:183], v[136:139], v[120:123], v[180:183]\n"
"v_mfma_f32_16x16x32_bf16 v[184:187], v[136:139], v[124:127], v[184:187]\n"
"v_mfma_f32_16x16x32_bf16 v[188:191], v[136:139], v[128:131], v[188:191]\n"
"v_mfma_f32_16x16x32_bf16 v[192:195], v[136:139], v[132:135], v[192:195]\n"
"s_waitcnt lgkmcnt(4)\n"
"v_mfma_f32_16x16x32_bf16 v[196:199], v[140:143], v[116:119], v[196:199]\n"
"v_mfma_f32_16x16x32_bf16 v[200:203], v[140:143], v[120:123], v[200:203]\n"
"v_mfma_f32_16x16x32_bf16 v[204:207], v[140:143], v[124:127], v[204:207]\n"
"v_mfma_f32_16x16x32_bf16 v[208:211], v[140:143], v[128:131], v[208:211]\n"
"v_mfma_f32_16x16x32_bf16 v[212:215], v[140:143], v[132:135], v[212:215]\n"
"s_waitcnt lgkmcnt(2)\n"
"v_mfma_f32_16x16x32_bf16 v[216:219], v[144:147], v[116:119], v[216:219]\n"
"v_mfma_f32_16x16x32_bf16 v[220:223], v[144:147], v[120:123], v[220:223]\n"
"v_mfma_f32_16x16x32_bf16 v[224:227], v[144:147], v[124:127], v[224:227]\n"
"v_mfma_f32_16x16x32_bf16 v[228:231], v[144:147], v[128:131], v[228:231]\n"
"v_mfma_f32_16x16x32_bf16 v[232:235], v[144:147], v[132:135], v[232:235]\n"
"s_waitcnt lgkmcnt(0)\n"
"v_mfma_f32_16x16x32_bf16 v[236:239], v[148:151], v[116:119], v[236:239]\n"
"v_mfma_f32_16x16x32_bf16 v[240:243], v[148:151], v[120:123], v[240:243]\n"
"v_mfma_f32_16x16x32_bf16 v[244:247], v[148:151], v[124:127], v[244:247]\n"
"v_mfma_f32_16x16x32_bf16 v[248:251], v[148:151], v[128:131], v[248:251]\n"
"v_mfma_f32_16x16x32_bf16 v[252:255], v[148:151], v[132:135], v[252:255]\n"
"s_barrier\n"
"s_add_u32 s74, s74, 16384\n"
"s_cmp_lt_u32 s74, 131072\n"
"s_cbranch_scc1 .Lk_s2_75_%=\n"
"s_barrier\n"
"ds_read2_b32 v[40:41], v32 offset0:0 offset1:160\n"
"ds_read2_b32 v[42:43], v32 offset0:32 offset1:192\n"
"ds_read2_b32 v[44:45], v32 offset0:64 offset1:224\n"
"ds_read_b32 v46, v32 offset:384\n"
"ds_read_b32 v47, v32 offset:1024\n"
"ds_read_b32 v48, v32 offset:512\n"
"ds_read_b32 v49, v32 offset:1152\n"
"s_waitcnt lgkmcnt(6)\n"
"v_add_f32 v40, v40, v41\n"
"s_waitcnt lgkmcnt(5)\n"
"v_add_f32 v42, v42, v43\n"
"s_waitcnt lgkmcnt(4)\n"
"v_add_f32 v44, v44, v45\n"
"s_waitcnt lgkmcnt(2)\n"
"v_add_f32 v46, v46, v47\n"
"s_waitcnt lgkmcnt(0)\n"
"v_add_f32 v48, v48, v49\n"
"v_mov_b32 v41, v40\n"
"v_mov_b32 v43, v42\n"
"v_mov_b32 v45, v44\n"
"v_mov_b32 v47, v46\n"
"v_mov_b32 v49, v48\n"
"v_permlane16_swap_b32 v40, v41\n"
"v_permlane16_swap_b32 v42, v43\n"
"v_permlane16_swap_b32 v44, v45\n"
"v_permlane16_swap_b32 v46, v47\n"
"v_permlane16_swap_b32 v48, v49\n"
"v_add_f32 v40, v40, v41\n"
"v_add_f32 v42, v42, v43\n"
"v_add_f32 v44, v44, v45\n"
"v_add_f32 v46, v46, v47\n"
"v_add_f32 v48, v48, v49\n"
"v_rcp_f32 v40, v40\n"
"v_rcp_f32 v42, v42\n"
"v_rcp_f32 v44, v44\n"
"v_rcp_f32 v46, v46\n"
"v_rcp_f32 v48, v48\n"
"v_mul_f32 v176, v40, v176\n"
"v_mul_f32 v177, v40, v177\n"
"v_mul_f32 v178, v40, v178\n"
"v_mul_f32 v179, v40, v179\n"
"v_mul_f32 v196, v40, v196\n"
"v_mul_f32 v197, v40, v197\n"
"v_mul_f32 v198, v40, v198\n"
"v_mul_f32 v199, v40, v199\n"
"v_cvt_pk_bf16_f32 v176, v176, v177\n"
"v_cvt_pk_bf16_f32 v177, v178, v179\n"
"v_mul_f32 v216, v40, v216\n"
"v_mul_f32 v217, v40, v217\n"
"ds_write_b64 v28, v[176:177] offset:0\n"
"v_mul_f32 v218, v40, v218\n"
"v_mul_f32 v219, v40, v219\n"
"v_cvt_pk_bf16_f32 v196, v196, v197\n"
"v_cvt_pk_bf16_f32 v197, v198, v199\n"
"v_mul_f32 v236, v40, v236\n"
"v_mul_f32 v237, v40, v237\n"
"ds_write_b64 v29, v[196:197] offset:0\n"
"v_mul_f32 v238, v40, v238\n"
"v_mul_f32 v239, v40, v239\n"
"v_cvt_pk_bf16_f32 v216, v216, v217\n"
"v_cvt_pk_bf16_f32 v217, v218, v219\n"
"v_cvt_pk_bf16_f32 v236, v236, v237\n"
"ds_write_b64 v30, v[216:217] offset:0\n"
"v_cvt_pk_bf16_f32 v237, v238, v239\n"
"ds_write_b64 v31, v[236:237] offset:0\n"
"v_mul_f32 v180, v42, v180\n"
"v_mul_f32 v181, v42, v181\n"
"v_mul_f32 v182, v42, v182\n"
"v_mul_f32 v183, v42, v183\n"
"v_mul_f32 v200, v42, v200\n"
"v_mul_f32 v201, v42, v201\n"
"v_mul_f32 v202, v42, v202\n"
"v_mul_f32 v203, v42, v203\n"
"v_cvt_pk_bf16_f32 v180, v180, v181\n"
"v_cvt_pk_bf16_f32 v181, v182, v183\n"
"v_mul_f32 v220, v42, v220\n"
"v_mul_f32 v221, v42, v221\n"
"ds_write_b64 v28, v[180:181] offset:2048\n"
"v_mul_f32 v222, v42, v222\n"
"v_mul_f32 v223, v42, v223\n"
"v_cvt_pk_bf16_f32 v200, v200, v201\n"
"v_cvt_pk_bf16_f32 v201, v202, v203\n"
"v_mul_f32 v240, v42, v240\n"
"v_mul_f32 v241, v42, v241\n"
"ds_write_b64 v29, v[200:201] offset:2048\n"
"v_mul_f32 v242, v42, v242\n"
"v_mul_f32 v243, v42, v243\n"
"v_cvt_pk_bf16_f32 v220, v220, v221\n"
"v_cvt_pk_bf16_f32 v221, v222, v223\n"
"v_cvt_pk_bf16_f32 v240, v240, v241\n"
"ds_write_b64 v30, v[220:221] offset:2048\n"
"v_cvt_pk_bf16_f32 v241, v242, v243\n"
"ds_write_b64 v31, v[240:241] offset:2048\n"
"v_mul_f32 v184, v44, v184\n"
"v_mul_f32 v185, v44, v185\n"
"v_mul_f32 v186, v44, v186\n"
"v_mul_f32 v187, v44, v187\n"
"v_mul_f32 v204, v44, v204\n"
"v_mul_f32 v205, v44, v205\n"
"v_mul_f32 v206, v44, v206\n"
"v_mul_f32 v207, v44, v207\n"
"v_cvt_pk_bf16_f32 v184, v184, v185\n"
"v_cvt_pk_bf16_f32 v185, v186, v187\n"
"v_mul_f32 v224, v44, v224\n"
"v_mul_f32 v225, v44, v225\n"
"ds_write_b64 v28, v[184:185] offset:4096\n"
"v_mul_f32 v226, v44, v226\n"
"v_mul_f32 v227, v44, v227\n"
"v_cvt_pk_bf16_f32 v204, v204, v205\n"
"v_cvt_pk_bf16_f32 v205, v206, v207\n"
"v_mul_f32 v244, v44, v244\n"
"v_mul_f32 v245, v44, v245\n"
"ds_write_b64 v29, v[204:205] offset:4096\n"
"v_mul_f32 v246, v44, v246\n"
"v_mul_f32 v247, v44, v247\n"
"v_cvt_pk_bf16_f32 v224, v224, v225\n"
"v_cvt_pk_bf16_f32 v225, v226, v227\n"
"v_cvt_pk_bf16_f32 v244, v244, v245\n"
"ds_write_b64 v30, v[224:225] offset:4096\n"
"v_cvt_pk_bf16_f32 v245, v246, v247\n"
"ds_write_b64 v31, v[244:245] offset:4096\n"
"v_mul_f32 v188, v46, v188\n"
"v_mul_f32 v189, v46, v189\n"
"v_mul_f32 v190, v46, v190\n"
"v_mul_f32 v191, v46, v191\n"
"v_mul_f32 v208, v46, v208\n"
"v_mul_f32 v209, v46, v209\n"
"v_mul_f32 v210, v46, v210\n"
"v_mul_f32 v211, v46, v211\n"
"v_cvt_pk_bf16_f32 v188, v188, v189\n"
"v_cvt_pk_bf16_f32 v189, v190, v191\n"
"v_mul_f32 v228, v46, v228\n"
"v_mul_f32 v229, v46, v229\n"
"ds_write_b64 v28, v[188:189] offset:6144\n"
"v_mul_f32 v230, v46, v230\n"
"v_mul_f32 v231, v46, v231\n"
"v_cvt_pk_bf16_f32 v208, v208, v209\n"
"v_cvt_pk_bf16_f32 v209, v210, v211\n"
"v_mul_f32 v248, v46, v248\n"
"v_mul_f32 v249, v46, v249\n"
"ds_write_b64 v29, v[208:209] offset:6144\n"
"v_mul_f32 v250, v46, v250\n"
"v_mul_f32 v251, v46, v251\n"
"v_cvt_pk_bf16_f32 v228, v228, v229\n"
"v_cvt_pk_bf16_f32 v229, v230, v231\n"
"v_cvt_pk_bf16_f32 v248, v248, v249\n"
"ds_write_b64 v30, v[228:229] offset:6144\n"
"v_cvt_pk_bf16_f32 v249, v250, v251\n"
"ds_write_b64 v31, v[248:249] offset:6144\n"
"v_mul_f32 v192, v48, v192\n"
"v_mul_f32 v193, v48, v193\n"
"v_mul_f32 v194, v48, v194\n"
"v_mul_f32 v195, v48, v195\n"
"v_mul_f32 v212, v48, v212\n"
"v_mul_f32 v213, v48, v213\n"
"v_mul_f32 v214, v48, v214\n"
"v_mul_f32 v215, v48, v215\n"
"v_cvt_pk_bf16_f32 v192, v192, v193\n"
"v_cvt_pk_bf16_f32 v193, v194, v195\n"
"v_mul_f32 v232, v48, v232\n"
"v_mul_f32 v233, v48, v233\n"
"ds_write_b64 v28, v[192:193] offset:8192\n"
"v_mul_f32 v234, v48, v234\n"
"v_mul_f32 v235, v48, v235\n"
"v_cvt_pk_bf16_f32 v212, v212, v213\n"
"v_cvt_pk_bf16_f32 v213, v214, v215\n"
"v_mul_f32 v252, v48, v252\n"
"v_mul_f32 v253, v48, v253\n"
"ds_write_b64 v29, v[212:213] offset:8192\n"
"v_mul_f32 v254, v48, v254\n"
"v_mul_f32 v255, v48, v255\n"
"v_cvt_pk_bf16_f32 v232, v232, v233\n"
"v_cvt_pk_bf16_f32 v233, v234, v235\n"
"v_cvt_pk_bf16_f32 v252, v252, v253\n"
"ds_write_b64 v30, v[232:233] offset:8192\n"
"v_cvt_pk_bf16_f32 v253, v254, v255\n"
"ds_write_b64 v31, v[252:253] offset:8192\n"
"s_bfe_u32 s61, s20, 262156\n"
"s_lshl_b32 s61, s61, 17\n"
"s_lshl_b32 s62, s78, 15\n"
"s_add_u32 s65, s61, s62\n"
"s_add_u32 m0, s79, 110592\n"
"s_nop 0\n"
"buffer_load_dwordx4 v6, s[36:39], s65 offen offset:0  lds\n"
"buffer_load_dwordx4 v7, s[36:39], s65 offen offset:1024  lds\n"
"buffer_load_dwordx4 v8, s[36:39], s65 offen offset:2048  lds\n"
"buffer_load_dwordx4 v9, s[36:39], s65 offen offset:3072  lds\n"
"s_waitcnt lgkmcnt(0)\n"
"s_waitcnt vmcnt(0)\n"
"s_barrier\n"
"s_branch .Lk_JB_7_%=\n"
".Lk_E_3_%=:\n"
"s_waitcnt vmcnt(0) lgkmcnt(0)\n"
".Lk_end_%=:\n"
  :: "s"(__builtin_amdgcn_kernarg_segment_ptr()),"s"(blockIdx.x),"v"(threadIdx.x),"s"((unsigned)(size_t)smem)
  : "v1","v2","v3","v4","v5","v6","v7","v8","v9","v10","v11","v12","v13","v14","v15","v16","v17","v18","v19","v20","v21","v22","v23","v24","v25","v26","v27","v28","v29","v30","v31","v32","v33","v34","v35","v36","v37","v38","v39","v40","v41","v42","v43","v44","v45","v46","v47","v48","v49","v50","v51","v52","v53","v54","v55","v56","v57","v58","v59","v60","v61","v62","v63","v64","v65","v66","v67","v68","v69","v70","v71","v72","v73","v74","v75","v76","v77","v78","v79","v80","v81","v82","v83","v84","v85","v86","v87","v88","v89","v90","v91","v92","v93","v94","v95","v96","v97","v98","v99","v100","v101","v102","v103","v104","v105","v106","v107","v108","v109","v110","v111","v112","v113","v114","v115","v116","v117","v118","v119","v120","v121","v122","v123","v124","v125","v126","v127","v128","v129","v130","v131","v132","v133","v134","v135","v136","v137","v138","v139","v140","v141","v142","v143","v144","v145","v146","v147","v148","v149","v150","v151","v152","v153","v154","v155","v156","v157","v158","v159","v160","v161","v162","v163","v164","v165","v166","v167","v168","v169","v170","v171","v172","v173","v174","v175","v176","v177","v178","v179","v180","v181","v182","v183","v184","v185","v186","v187","v188","v189","v190","v191","v192","v193","v194","v195","v196","v197","v198","v199","v200","v201","v202","v203","v204","v205","v206","v207","v208","v209","v210","v211","v212","v213","v214","v215","v216","v217","v218","v219","v220","v221","v222","v223","v224","v225","v226","v227","v228","v229","v230","v231","v232","v233","v234","v235","v236","v237","v238","v239","v240","v241","v242","v243","v244","v245","v246","v247","v248","v249","v250","v251","v252","v253","v254","v255","s4","s5","s6","s7","s8","s9","s10","s11","s12","s13","s14","s15","s16","s17","s18","s19","s20","s21","s22","s23","s24","s25","s26","s27","s28","s29","s30","s31","s32","s33","s34","s35","s36","s37","s38","s39","s40","s41","s42","s43","s44","s45","s46","s47","s48","s49","s50","s51","s52","s53","s54","s55","s56","s57","s58","s59","s60","s61","s62","s63","s64","s65","s66","s67","s68","s69","s70","s71","s72","s73","s74","s75","s76","s77","s78","s79","vcc","scc","memory");
}

extern "C" void kernel_launch(void* const* d_in, const int* in_sizes, int n_in, void* d_out, int out_size, void* d_ws, size_t ws_size, hipStream_t stream) {
  mha_fused<<<256, 512, 0, stream>>>(d_in[0], d_in[1], d_in[2], d_in[3], d_in[4], d_out, d_ws);
}
```
